```python
import torch

def _softmax(x, axis=-1):
    return torch.softmax(x, dim=axis)
def _gelu(x):
    return torch.nn.functional.gelu(x, approximate="tanh")
def _silu(x):
    return torch.nn.functional.silu(x)
def _sigmoid(x):
    return torch.sigmoid(x)
def _layernorm(x, axis=-1, eps=1e-5):
    m = x.mean(dim=axis, keepdim=True)
    v = x.var(dim=axis, keepdim=True, unbiased=False)
    return (x - m) / torch.sqrt(v + eps)
def _scatter_add(tgt, idx, src, axis=0):
    return tgt.clone().index_add_(axis, idx, src)
def _segment_sum(src, seg, n):
    out = torch.zeros((n,) + src.shape[1:], dtype=src.dtype)
    return out.index_add_(0, seg, src)
def _pad(x, pw):
    flat = tuple(v for pair in reversed(pw) for v in pair)
    return torch.nn.functional.pad(x, flat)

kernel_name = 'interleave_layers__grouped_kv_mqa__sliding_block__prompt'

def setup_inputs():

    _w = (lambda a: (lambda t: t.to(torch.bfloat16) if t.is_floating_point() else t)(torch.as_tensor(a)))
    def _A_setup():
        B, S, D, H = 1, 2048, 448, 16
        Dh = D // H
        return {
            "x": _w((lambda *s: torch.randn(*s, dtype=torch.bfloat16) * 0.05)(B, S, D)),
            "wq": _w((lambda *s: torch.randn(*s, dtype=torch.bfloat16) * 0.05)(D, H * Dh)),
            "wk": _w((lambda *s: torch.randn(*s, dtype=torch.bfloat16) * 0.05)(D, Dh)),
            "wv": _w((lambda *s: torch.randn(*s, dtype=torch.bfloat16) * 0.05)(D, Dh)),
            "wo": _w((lambda *s: torch.randn(*s, dtype=torch.bfloat16) * 0.05)(H * Dh, D)),
        }
    _A_in = _A_setup()

    def _B_setup():
        B, S, D = 1, 2048, 448
        return {
            "x": _w((lambda *s: torch.randn(*s, dtype=torch.bfloat16) * 0.05)(B, S, D)),
            "wqkv": _w((lambda *s: torch.randn(*s, dtype=torch.bfloat16) * 0.05)(D, 3 * D)),
            "wo": _w((lambda *s: torch.randn(*s, dtype=torch.bfloat16) * 0.05)(D, D)),
        }
    _B_in = _B_setup()

    return {
        "A_x": _A_in["x"],
        "A_wq": _A_in["wq"],
        "A_wk": _A_in["wk"],
        "A_wv": _A_in["wv"],
        "A_wo": _A_in["wo"],
        "B_x": _B_in["x"],
        "B_wqkv": _B_in["wqkv"],
        "B_wo": _B_in["wo"],
    }

def reference(A_x, A_wq, A_wk, A_wv, A_wo, B_x, B_wqkv, B_wo):
    def _A_ref(x, wq, wk, wv, wo):
        B, S, D = x.shape
        H = 16
        Dh = D // H
        q = (lambda x, *d: x.permute(*d))((x @ wq).reshape(B, S, H, Dh), 0, 2, 1, 3)
        k = x @ wk
        v = x @ wv
        scores = torch.einsum("bhqd,bkd->bhqk", q, k) * (Dh ** -0.5)
        p = _softmax(scores, axis=-1)
        o = torch.einsum("bhqk,bkd->bhqd", p, v)
        return (lambda x, *d: x.permute(*d))(o, 0, 2, 1, 3).reshape(B, S, H * Dh) @ wo

    def _B_ref(x, wqkv, wo):
        B, S, D = x.shape
        H = 16; Dh = D // H; W = 256
        qkv = (lambda x, *d: x.permute(*d))((x @ wqkv).reshape(B, S, 3, H, Dh), 2, 0, 3, 1, 4)
        q, k, v = qkv[0], qkv[1], qkv[2]
        scores = torch.einsum("bhqd,bhkd->bhqk", q, k) * (Dh ** -0.5)
        idx = torch.arange(S)
        mask = (idx[:, None] - idx[None, :] >= 0) & (idx[:, None] - idx[None, :] < W)
        scores = torch.where(mask[None, None], scores, -1e9)
        p = _softmax(scores, axis=-1)
        o = torch.einsum("bhqk,bhkd->bhqd", p, v)
        return (lambda x, *d: x.permute(*d))(o, 0, 2, 1, 3).reshape(B, S, D) @ wo

    x = A_x
    x = x + _A_ref(_layernorm(x, axis=-1), A_wq, A_wk, A_wv, A_wo)
    x = x + _B_ref(_layernorm(x, axis=-1), B_wqkv, B_wo)
    x = x + _A_ref(_layernorm(x, axis=-1), A_wq, A_wk, A_wv, A_wo)
    x = x + _B_ref(_layernorm(x, axis=-1), B_wqkv, B_wo)
    return x
```

```cpp
#include <hip/hip_runtime.h>
#include <hip/hip_bf16.h>
typedef unsigned short u16;
__device__ inline float b2f(u16 b){ return __uint_as_float(((unsigned)b)<<16); }
__device__ inline u16 f2b(float f){ __hip_bfloat16 h = __float2bfloat16(f); return *(u16*)&h; }
constexpr int S=2048, D=448, H=16, DH=28;
__global__ void k_ln(const float* x, float* xn){
  int row=blockIdx.x; int l=threadIdx.x;
  float v[7]; float s=0;
  for(int i=0;i<7;i++){ v[i]=x[row*D+l+64*i]; s+=v[i]; }
  for(int o=32;o;o>>=1) s+=__shfl_xor(s,o);
  float m=s/D; float q=0;
  for(int i=0;i<7;i++){ float d=v[i]-m; q+=d*d; }
  for(int o=32;o;o>>=1) q+=__shfl_xor(q,o);
  float r=rsqrtf(q/D+1e-5f);
  for(int i=0;i<7;i++) xn[row*D+l+64*i]=b2f(f2b((v[i]-m)*r));
}
__global__ void k_cvt(const u16* a, float* o, int n){ int i=blockIdx.x*256+threadIdx.x; if(i<n) o[i]=b2f(a[i]); }
__global__ void k_out(const float* a, u16* o, int n){ int i=blockIdx.x*256+threadIdx.x; if(i<n) o[i]=f2b(a[i]); }
__global__ void k_gemm(const float* A, const u16* W, int N, float* C, int ldc, const float* res){
  int n=blockIdx.x*64+threadIdx.x; int m=blockIdx.y; if(n>=N) return;
  float acc=0; for(int k=0;k<D;k++) acc+=A[m*D+k]*b2f(W[k*N+n]);
  acc=b2f(f2b(acc));
  if(res) acc=b2f(f2b(acc+res[m*ldc+n]));
  C[m*ldc+n]=acc;
}
__global__ void k_attn(const float* q,int ldq,const float* k,int ldk,int khs,const float* v,float* o,int win){
  int t=blockIdx.x*64+threadIdx.x; int h=blockIdx.y;
  float qq[DH]; for(int d=0;d<DH;d++) qq[d]=q[t*ldq+h*DH+d];
  int lo=0, hi=S-1; if(win){ hi=t; lo=t-255<0?0:t-255; }
  float m=-1e30f,l=0,acc[DH]; for(int d=0;d<DH;d++) acc[d]=0;
  for(int j=lo;j<=hi;j++){ float s=0; for(int d=0;d<DH;d++) s+=qq[d]*k[j*ldk+h*khs+d]; s*=0.18898223650461361f;
    float mn=fmaxf(m,s); float a=__expf(m-mn), p=__expf(s-mn); l=l*a+p; for(int d=0;d<DH;d++) acc[d]=acc[d]*a+p*v[j*ldk+h*khs+d]; m=mn; }
  for(int d=0;d<DH;d++) o[t*D+h*DH+d]=b2f(f2b(acc[d]/l));
}
extern "C" void kernel_launch(void* const* d_in, const int* in_sizes, int n_in, void* d_out, int out_size, void* d_ws, size_t ws_size, hipStream_t stream){
  const u16* x=(const u16*)d_in[0]; const u16* wq=(const u16*)d_in[1]; const u16* wk=(const u16*)d_in[2]; const u16* wv=(const u16*)d_in[3]; const u16* wo=(const u16*)d_in[4];
  const u16* wqkv=(const u16*)d_in[6]; const u16* wob=(const u16*)d_in[7];
  float* ws=(float*)d_ws; float* X=ws; float* X2=X+S*D; float* XN=X2+S*D; float* QKV=XN+S*D; float* O=QKV+S*1344;
  int n=S*D;
  k_cvt<<<(n+255)/256,256,0,stream>>>(x,X,n);
  for(int it=0;it<2;it++){
    k_ln<<<S,64,0,stream>>>(X,XN);
    k_gemm<<<dim3(7,S),64,0,stream>>>(XN,wq,448,QKV,1344,nullptr);
    k_gemm<<<dim3(1,S),64,0,stream>>>(XN,wk,28,QKV+448,1344,nullptr);
    k_gemm<<<dim3(1,S),64,0,stream>>>(XN,wv,28,QKV+896,1344,nullptr);
    k_attn<<<dim3(S/64,H),64,0,stream>>>(QKV,1344,QKV+448,1344,0,QKV+896,O,0);
    k_gemm<<<dim3(7,S),64,0,stream>>>(O,wo,448,X2,448,X);
    k_ln<<<S,64,0,stream>>>(X2,XN);
    k_gemm<<<dim3(21,S),64,0,stream>>>(XN,wqkv,1344,QKV,1344,nullptr);
    k_attn<<<dim3(S/64,H),64,0,stream>>>(QKV,1344,QKV+448,1344,28,QKV+896,O,1);
    k_gemm<<<dim3(7,S),64,0,stream>>>(O,wob,448,X,448,X2);
  }
  k_out<<<(n+255)/256,256,0,stream>>>(X,(u16*)d_out,n);
}
```

```cpp
#include <hip/hip_runtime.h>
#include <hip/hip_bf16.h>

typedef float f32x16 __attribute__((ext_vector_type(16)));
typedef float f32x4 __attribute__((ext_vector_type(4)));
typedef float f32x2 __attribute__((ext_vector_type(2)));
typedef unsigned u32x4 __attribute__((ext_vector_type(4)));
typedef unsigned u32x2 __attribute__((ext_vector_type(2)));
typedef short bf16x8 __attribute__((ext_vector_type(8)));
typedef __bf16 bf2 __attribute__((ext_vector_type(2)));
typedef unsigned short u16;
#define GAS __attribute__((address_space(1)))
#define LAS __attribute__((address_space(3)))
typedef GAS unsigned gu32;
#define RLX __ATOMIC_RELAXED, __HIP_MEMORY_SCOPE_AGENT
#define SBAR() __builtin_amdgcn_sched_barrier(0)
#define PIN(x) asm volatile("" : "+v"(x))
#define DRAIN() asm volatile("s_waitcnt vmcnt(0)" ::: "memory")

constexpr int SEQ = 2048, DM = 448, NH = 16, DH = 28, NB = 64, NTL = 14;
constexpr int KB = 1024;
constexpr int FLAG_OFF = 0;
constexpr int REPA = 64;
constexpr int FA_OFF = 0;
constexpr int FX_OFF = FA_OFF + 2 * REPA * 4 * 128;
constexpr int FO_OFF = FX_OFF + 4 * 256 * 128;
constexpr int FB_OFF = FO_OFF + 4 * 256 * 128;
constexpr int FLAG_BYTES = FB_OFF + 2 * 256 * 128;
constexpr int ZERO_OFF = FLAG_BYTES;
constexpr int ZEROED_BYTES = ZERO_OFF + 4 * KB;
constexpr int WQA_OFF = ZEROED_BYTES;
constexpr int WKV_OFF = WQA_OFF + 16 * 28 * KB;
constexpr int WOA_OFF = WKV_OFF + 56 * KB;
constexpr int WOB_OFF = WOA_OFF + 14 * 32 * KB;
constexpr int WB_OFF = WOB_OFF + 14 * 32 * KB;
constexpr int CS_OFF = WB_OFF + 16 * 3 * 28 * KB;
constexpr int XT_OFF = CS_OFF + 12 * KB;
constexpr int XT_WG = 9 * KB, XT_BLK = 4 * XT_WG, XT_L = NB * XT_BLK;
constexpr int OT_OFF = XT_OFF + 4 * XT_L;
constexpr int OT_BLK = 32 * KB, OT_L = NB * OT_BLK;
constexpr int KVA_OFF = OT_OFF + 4 * OT_L;
constexpr int KVA_L = NB * 4 * KB;
constexpr int KVB_OFF = KVA_OFF + 2 * KVA_L;
constexpr int KVB_BLK = 64 * KB, KVB_L = NB * KVB_BLK;
constexpr int WS_BYTES = KVB_OFF + 2 * KVB_L;
static_assert(XT_OFF % 1024 == 0, "align");
constexpr int L_X = 0;
constexpr int L_O = L_X + 36 * KB;
constexpr int L_Q = L_O + 32 * KB;
constexpr int L_C = L_Q + 8 * KB;
constexpr int L_MS = L_C + 48 * KB;
constexpr int L_MISC = L_MS + 1 * KB;
constexpr int L_BYTES = L_MISC + 4 * KB;

constexpr float QSCALE = 0.18898223650461361f * 1.4426950408889634f;
constexpr unsigned SPIN_CAP = 1u << 22;

__device__ __forceinline__ unsigned pk(float a, float b) { f32x2 v = {a, b}; bf2 r = __builtin_convertvector(v, bf2); return __builtin_bit_cast(unsigned, r); }
__device__ __forceinline__ float bflo(unsigned u) { return __uint_as_float(u << 16); }
__device__ __forceinline__ float bfhi(unsigned u) { return __uint_as_float(u & 0xffff0000u); }
__device__ __forceinline__ f32x16 mfma32(u32x4 a, u32x4 b, f32x16 c) {
  return __builtin_amdgcn_mfma_f32_32x32x16_bf16(__builtin_bit_cast(bf16x8, a), __builtin_bit_cast(bf16x8, b), c, 0, 0, 0); }
__device__ __forceinline__ f32x4 mfma16(u32x4 a, u32x4 b, f32x4 c) {
  return __builtin_amdgcn_mfma_f32_16x16x32_bf16(__builtin_bit_cast(bf16x8, a), __builtin_bit_cast(bf16x8, b), c, 0, 0, 0); }
__device__ __forceinline__ float half_sum(float v) { auto r = __builtin_amdgcn_permlane32_swap(__float_as_uint(v), __float_as_uint(v), false, false); return __uint_as_float(r[0]) + __uint_as_float(r[1]); }
__device__ __forceinline__ float half_max(float v) { auto r = __builtin_amdgcn_permlane32_swap(__float_as_uint(v), __float_as_uint(v), false, false); return fmaxf(__uint_as_float(r[0]), __uint_as_float(r[1])); }
__device__ __forceinline__ float half_hi(float v) { auto r = __builtin_amdgcn_permlane32_swap(__float_as_uint(v), __float_as_uint(v), false, false); return __uint_as_float(r[1]); }
__device__ __forceinline__ unsigned bf_ceil_bits(float v) { unsigned u = __float_as_uint(v); unsigned t = u & 0xffff0000u; return (v > 0.f && t != u) ? t + 0x10000u : t; }
__device__ __forceinline__ void pack16(const f32x16& v, u32x4& f0, u32x4& f1) {
  f0 = (u32x4){pk(v[0], v[1]), pk(v[2], v[3]), pk(v[4], v[5]), pk(v[6], v[7])};
  f1 = (u32x4){pk(v[8], v[9]), pk(v[10], v[11]), pk(v[12], v[13]), pk(v[14], v[15])}; }

#define LDG(voff, soff) __builtin_amdgcn_raw_buffer_load_b128(rs, (voff), (soff), 0)
#define LDC(voff, soff) __builtin_amdgcn_raw_buffer_load_b128(rs, (voff), (soff), 16)
#define STC(v, voff, soff) __builtin_amdgcn_raw_buffer_store_b128((v), rs, (voff), (soff), 16)
#define LDS128(off) (*(LAS u32x4*)(lds + (off)))

__device__ __forceinline__ void tile_stats(const u32x4& f0, const u32x4& f1, f32x16& v, float& mean, float& m2) {
#pragma unroll
  for (int i = 0; i < 4; ++i) { v[2 * i] = bflo(f0[i]); v[2 * i + 1] = bfhi(f0[i]); v[8 + 2 * i] = bflo(f1[i]); v[9 + 2 * i] = bfhi(f1[i]); }
  float s = 0.f;
#pragma unroll
  for (int i = 0; i < 16; ++i) s += v[i];
  mean = half_sum(s) * (1.f / 32.f);
  float q = 0.f;
#pragma unroll
  for (int i = 0; i < 16; ++i) { float d = v[i] - mean; q = fmaf(d, d, q); }
  m2 = half_sum(q);
}

constexpr int NFR_Q = 448, NFR_KV = 56, NFR_O = 448, NFR_B = 1344;
constexpr int T_FR = NFR_Q + NFR_KV + 2 * NFR_O + NFR_B;
constexpr int T_X = NB * NTL;
constexpr int PREP_WG_A = (T_FR + T_X) / 4;
constexpr int PREP_WG_CS = 66;
constexpr int PREP_WG_Z = ZEROED_BYTES / (256 * 16 * 4);
constexpr int PREP_GRID = PREP_WG_A + PREP_WG_CS + PREP_WG_Z + 1;

__global__ __launch_bounds__(256) void prep_kernel(const u16* __restrict__ x, const u16* __restrict__ wq, const u16* __restrict__ wk, const u16* __restrict__ wv,
                                                   const u16* __restrict__ woa, const u16* __restrict__ wb, const u16* __restrict__ wob, unsigned char* __restrict__ ws) {
  const int tid = threadIdx.x, lane = tid & 63, w = tid >> 6, r = lane & 31, hh = lane >> 5;
  const int b = blockIdx.x;
  if (b < PREP_WG_A) {
    int task = b * 4 + w;
    if (task < T_FR) {
      const u16* src; int ld, k0, col, kmode; bool colok = true; int dst;
      int hsel = hh; int dlim = 1 << 30;
      int dbase = 0;
      if (task < NFR_Q) { int head = task / 28, ks = task % 28; src = wq; ld = 448; k0 = 16 * ks; col = 28 * head + r; colok = r < 28; dst = WQA_OFF + task * KB; }
      else if (task < NFR_Q + NFR_KV) { int t = task - NFR_Q; int kv = t / 28, dh = (t / 14) & 1, kt = t % 14; src = kv ? wv : wk; ld = 28;
        int kg = lane >> 4, c = lane & 15; k0 = 32 * kt + 16 * (kg >> 1); hsel = kg & 1; col = 16 * dh + c; colok = col < 28; dst = WKV_OFF + t * KB; }
      else if (task < NFR_Q + NFR_KV + 2 * NFR_O) { int t = task - NFR_Q - NFR_KV; int which = t / NFR_O; t %= NFR_O; int tl = t / 32, ks = t % 32; src = which ? wob : woa; ld = 448;
        k0 = 28 * (ks >> 1) + 16 * (ks & 1); dbase = 16 * (ks & 1); dlim = 28; col = 32 * tl + r; dst = (which ? WOB_OFF : WOA_OFF) + t * KB; }
      else { int t = task - NFR_Q - NFR_KV - 2 * NFR_O; int head = t / 84, c3 = (t / 28) % 3, ks = t % 28; src = wb; ld = 1344; k0 = 16 * ks; col = 448 * c3 + 28 * head + r; colok = r < 28; dst = WB_OFF + t * KB; }
      unsigned e[8];
#pragma unroll
      for (int j = 0; j < 8; ++j) { int dk = 8 * (j >> 2) + 4 * hsel + (j & 3); bool ok = colok && (dbase + dk < dlim); e[j] = ok ? (unsigned)src[(k0 + dk) * ld + col] : 0u; }
      u32x4 v = {e[0] | (e[1] << 16), e[2] | (e[3] << 16), e[4] | (e[5] << 16), e[6] | (e[7] << 16)};
      *(u32x4*)(ws + dst + lane * 16) = v;
    } else {
      int t = task - T_FR; int i = t / NTL, tl = t % NTL;
      const u16* p = x + (32 * i + r) * DM + 32 * tl + 4 * hh;
      u32x2 c0 = *(const u32x2*)(p), c1 = *(const u32x2*)(p + 8), c2 = *(const u32x2*)(p + 16), c3 = *(const u32x2*)(p + 24);
      u32x4 f0 = {c0.x, c0.y, c1.x, c1.y}, f1 = {c2.x, c2.y, c3.x, c3.y};
      f32x16 v; float mean, m2; tile_stats(f0, f1, v, mean, m2);
      unsigned char* d = ws + XT_OFF + i * XT_BLK + (tl >> 2) * XT_WG;
      *(u32x4*)(d + (tl & 3) * 2048 + lane * 16) = f0; *(u32x4*)(d + (tl & 3) * 2048 + 1024 + lane * 16) = f1;
      if (hh == 0) *(f32x2*)(d + 8192 + (tl & 3) * 256 + r * 8) = (f32x2){mean, m2};
    }
  } else if (b < PREP_WG_A + PREP_WG_CS) {
    __shared__ float red[8][32];
    int t = b - PREP_WG_A; const u16* src; int ld, col;
    if (t < 16) { src = wq; ld = 448; col = 28 * t + r; } else if (t == 16) { src = wk; ld = 28; col = r; } else if (t == 17) { src = wv; ld = 28; col = r; }
    else { int u = t - 18; src = wb; ld = 1344; col = 448 * (u % 3) + 28 * (u / 3) + r; }
    float s = 0.f; const int part = tid >> 5;
    if (r < 28) {
#pragma unroll 14
      for (int k = 0; k < 56; ++k) s += __uint_as_float((unsigned)src[(part * 56 + k) * ld + col] << 16);
    }
    red[part][r] = s; __syncthreads();
    if (tid < 32) { float a = 0.f;
#pragma unroll
      for (int p = 0; p < 8; ++p) a += red[p][tid];
      *(float*)(ws + CS_OFF + t * 128 + tid * 4) = a; }
  } else {
    int t = b - PREP_WG_A - PREP_WG_CS;
#pragma unroll
    for (int j = 0; j < 4; ++j) { int off = t * 16384 + j * 4096 + tid * 16; if (off < ZEROED_BYTES) *(u32x4*)(ws + off) = (u32x4){0, 0, 0, 0}; }
  }
}

#define MS0(S, A, B) asm volatile("v_mfma_f32_32x32x16_bf16 %0, %1, %2, 0" : "=&v"(S) : "v"(A), "v"(B))
#define MS1(S, A, B) asm volatile("v_mfma_f32_32x32x16_bf16 %0, %1, %2, %0" : "+v"(S) : "v"(A), "v"(B))
#define MO(O, A, B) asm volatile("v_mfma_f32_32x32x16_bf16 %0, %1, %2, %0" : "+a"(O) : "v"(A), "v"(B))
#define EX(x) __builtin_amdgcn_exp2f(x)
#define E4(E, S, i) { E[0] = EX(S[i]); E[1] = EX(S[i + 1]); E[2] = EX(S[i + 2]); E[3] = EX(S[i + 3]); PIN(E); SBAR(); }
#define C2(P, j, E) { P[j] = pk(E[0], E[1]); P[j + 1] = pk(E[2], E[3]); PIN(P); SBAR(); }
#define STEP(SC, SN, KN0, KN1, QN0, QN1, OP, VP0, VP1, PP0, PP1, PC0, PC1, LDA, LDB) \
  MS0(SN, KN0, QN0); SBAR(); E4(ea, SC, 0) C2(PP1, 2, eb) \
  MS1(SN, KN1, QN1); SBAR(); LDA; E4(eb, SC, 4) C2(PC0, 0, ea) \
  MO(OP, VP0, PP0); SBAR(); E4(ea, SC, 8) C2(PC0, 2, eb) \
  MO(OP, VP1, PP1); SBAR(); LDB; E4(eb, SC, 12) C2(PC1, 0, ea)
#define NOP16() asm volatile("s_nop 7\n\ts_nop 7")

__device__ __forceinline__ void wait_flag(gu32* p) {
  unsigned spins = 0;
  while (!__all(__hip_atomic_load(p, RLX) != 0u)) { if (++spins > SPIN_CAP) break; }
  __builtin_amdgcn_fence(__ATOMIC_ACQUIRE, "wavefront");
}

struct Ctx { __amdgpu_buffer_rsrc_t rs; LAS unsigned char* lds; gu32* flags; int lane, w, r, hh, i, g, cl, lo; };

__device__ __forceinline__ void x_gather(const Ctx& c, int layer, float& mu, float& rstd) {
  const __amdgpu_buffer_rsrc_t rs = c.rs; LAS unsigned char* lds = c.lds;
  if (layer > 0) wait_flag(c.flags + (FX_OFF + (layer * 256 + c.cl + c.w) * 128) / 4);
  const int src = XT_OFF + layer * XT_L + c.i * XT_BLK + c.w * XT_WG;
  u32x4 t[9];
#pragma unroll
  for (int f = 0; f < 9; ++f) t[f] = LDC(c.lo, src + f * KB);
#pragma unroll
  for (int f = 0; f < 9; ++f) LDS128(L_X + c.w * XT_WG + f * KB + c.lo) = t[f];
  __syncthreads();
  f32x2 st[7];
#pragma unroll
  for (int k = 0; k < 7; ++k) { int tl = 2 * k + c.hh; st[k] = *(LAS f32x2*)(lds + L_X + (tl >> 2) * XT_WG + 8192 + (tl & 3) * 256 + c.r * 8); }
  float s = 0.f;
#pragma unroll
  for (int k = 0; k < 7; ++k) s += st[k].x;
  mu = half_sum(s) * (1.f / 14.f);
  float q = 0.f;
#pragma unroll
  for (int k = 0; k < 7; ++k) { float d = st[k].x - mu; q += fmaf(32.f * d, d, st[k].y); }
  q = half_sum(q);
  rstd = 1.0f / sqrtf(q * (1.f / 448.f) + 1e-5f);
  if (c.hh == 0) *(LAS f32x2*)(lds + L_MS + c.w * 256 + c.r * 8) = (f32x2){mu, rstd};
}
#define XFRAG(ks) LDS128(L_X + ((ks) >> 3) * XT_WG + ((ks) & 7) * KB + c.lo)

__device__ __forceinline__ void epi_alpha(const Ctx& c, f32x16& a, int cstile, float mu, float sc) {
  const __amdgpu_buffer_rsrc_t rs = c.rs;
#pragma unroll
  for (int q4 = 0; q4 < 4; ++q4) {
    f32x4 cs = __builtin_bit_cast(f32x4, LDG(c.hh * 16, CS_OFF + cstile * 128 + q4 * 32));
#pragma unroll
    for (int e = 0; e < 4; ++e) a[4 * q4 + e] = (a[4 * q4 + e] - mu * cs[e]) * sc;
  }
}

__device__ __forceinline__ void z_phase(const Ctx& c, int layer, int wo_off, f32x16& xres, u16* out) {
  const __amdgpu_buffer_rsrc_t rs = c.rs; LAS unsigned char* lds = c.lds;
  const int tl = 4 * c.g + c.w; const bool act = tl < NTL; const int tlc = act ? tl : 0;
  u32x4 W[32];
#pragma unroll
  for (int k = 0; k < 32; ++k) W[k] = LDG(c.lo, wo_off + (tlc * 32 + k) * KB);
  SBAR();
  wait_flag(c.flags + (FO_OFF + (layer * 256 + c.cl + c.w) * 128) / 4);
  const int src = OT_OFF + layer * OT_L + c.i * OT_BLK + c.w * 8 * KB;
  u32x4 t[8];
#pragma unroll
  for (int f = 0; f < 8; ++f) t[f] = LDC(c.lo, src + f * KB);
#pragma unroll
  for (int f = 0; f < 8; ++f) LDS128(L_O + (c.w * 8 + f) * KB + c.lo) = t[f];
  __syncthreads();
  f32x16 acc = xres;
#pragma unroll
  for (int k = 0; k < 32; ++k) acc = mfma32(W[k], LDS128(L_O + k * KB + c.lo), acc);
  u32x4 f0, f1; pack16(acc, f0, f1);
  if (layer == 3) {
    if (act) { u16* p = out + (32 * c.i + c.r) * DM + 32 * tl + 4 * c.hh;
      *(u32x2*)(p) = (u32x2){f0.x, f0.y}; *(u32x2*)(p + 8) = (u32x2){f0.z, f0.w}; *(u32x2*)(p + 16) = (u32x2){f1.x, f1.y}; *(u32x2*)(p + 24) = (u32x2){f1.z, f1.w}; }
    return;
  }
  const int dst = XT_OFF + (layer + 1) * XT_L + c.i * XT_BLK + c.g * XT_WG;
  if (act) { STC(f0, c.lo, dst + c.w * 2048); STC(f1, c.lo, dst + c.w * 2048 + 1024); }
  float mean, m2; tile_stats(f0, f1, xres, mean, m2);
  if (act && c.hh == 0) __builtin_amdgcn_raw_buffer_store_b64((u32x2){__float_as_uint(mean), __float_as_uint(m2)}, rs, c.r * 8, dst + 8192 + c.w * 256, 16);
  DRAIN();
  __syncthreads();
  if (threadIdx.x == 0) __hip_atomic_store(c.flags + (FX_OFF + ((layer + 1) * 256 + c.cl + c.g) * 128) / 4, 1u, RLX);
}

__device__ __forceinline__ void o_store(const Ctx& c, int layer, const f32x16& o, float l) {
  const __amdgpu_buffer_rsrc_t rs = c.rs;
  const float inv = 1.0f / l; f32x16 v;
#pragma unroll
  for (int e = 0; e < 16; ++e) v[e] = o[e] * inv;
  u32x4 f0, f1; pack16(v, f0, f1);
  if (c.hh) { f1.z = 0u; f1.w = 0u; }
  const int dst = OT_OFF + layer * OT_L + c.i * OT_BLK + (4 * c.g + c.w) * 2 * KB;
  STC(f0, c.lo, dst); STC(f1, c.lo, dst + KB);
}

__global__ __launch_bounds__(256, 1) void main_kernel(unsigned char* __restrict__ ws, u16* __restrict__ out) {
  extern __shared__ __attribute__((aligned(16))) unsigned char smem[];
  Ctx c;
  c.rs = __builtin_amdgcn_make_buffer_rsrc((void*)ws, 0, WS_BYTES, 0x00020000);
  c.lds = (LAS unsigned char*)smem; c.flags = (gu32*)ws;
  const int tid = threadIdx.x, b = blockIdx.x;
  c.lane = tid & 63; c.w = __builtin_amdgcn_readfirstlane(tid >> 6); c.r = c.lane & 31; c.hh = c.lane >> 5; c.lo = c.lane * 16;
  c.i = (b & 7) * 8 + (b >> 5); c.g = (b >> 3) & 3; c.cl = c.i * 4;
  const __amdgpu_buffer_rsrc_t rs = c.rs; LAS unsigned char* lds = c.lds;
  const int lane = c.lane, w = c.w, hh = c.hh, lo = c.lo, head = 4 * c.g + c.w;

  f32x16 xres;
  { const int tl = 4 * c.g + w, tlc = tl < NTL ? tl : 0; const int src = XT_OFF + c.i * XT_BLK + (tlc >> 2) * XT_WG + (tlc & 3) * 2048;
    u32x4 f0 = LDG(lo, src), f1 = LDG(lo, src + KB);
#pragma unroll
    for (int q = 0; q < 4; ++q) { xres[2 * q] = bflo(f0[q]); xres[2 * q + 1] = bfhi(f0[q]); xres[8 + 2 * q] = bflo(f1[q]); xres[9 + 2 * q] = bfhi(f1[q]); } }

#pragma unroll 1
  for (int pair = 0; pair < 2; ++pair) {
    {
      const int layer = 2 * pair;
      u32x4 W16[14], Wq[28];
      const int dh = w & 1, th = w >> 1;
      if (c.g < 2) {
#pragma unroll
        for (int k = 0; k < 14; ++k) W16[k] = LDG(lo, WKV_OFF + ((c.g * 2 + dh) * 14 + k) * KB);
      }
#pragma unroll
      for (int k = 0; k < 28; ++k) Wq[k] = LDG(lo, WQA_OFF + (head * 28 + k) * KB);
      SBAR();
      float mu, rstd; x_gather(c, layer, mu, rstd);
      if (c.g < 2) {
        const int G = lane >> 4, cc = lane & 15;
        const int xo = (G >> 1) * KB + (16 * th + cc + 32 * (G & 1)) * 16;
        f32x4 a = {0.f, 0.f, 0.f, 0.f}; u32x2 o2; int dsto; unsigned fl = 1u;
        if (c.g == 0) {
#pragma unroll
          for (int k = 0; k < 14; ++k) a = mfma16(W16[k], LDS128(L_X + (k >> 2) * XT_WG + (k & 3) * 2048 + xo), a);
          const f32x2 ms = *(LAS f32x2*)(lds + L_MS + w * 256 + (16 * th + cc) * 8);
          const f32x4 cs = __builtin_bit_cast(f32x4, LDG(G * 16, CS_OFF + 16 * 128 + dh * 64));
          float n2 = 0.f;
#pragma unroll
          for (int e = 0; e < 4; ++e) { a[e] = (a[e] - ms.x * cs[e]) * ms.y; n2 = fmaf(a[e], a[e], n2); }
          n2 += __shfl_xor(n2, 16); n2 += __shfl_xor(n2, 32);
#pragma unroll
          for (int o = 1; o < 16; o <<= 1) n2 = fmaxf(n2, __shfl_xor(n2, o));
          if (lane == 0) *(LAS float*)(lds + L_MISC + 64 + w * 4) = n2;
          if (dh == 1 && G == 3) { a[0] = 1.f; a[1] = 0.f; a[2] = 0.f; a[3] = 0.f; }
          dsto = dh * KB + (16 * th + cc + 32 * (G & 1)) * 16 + 8 * (G >> 1);
        } else {
#pragma unroll
          for (int k = 0; k < 14; ++k) a = mfma16(LDS128(L_X + (k >> 2) * XT_WG + (k & 3) * 2048 + xo), W16[k], a);
          const f32x4 m0 = *(LAS f32x4*)(lds + L_MS + w * 256 + (16 * th + 4 * G) * 8), m1 = *(LAS f32x4*)(lds + L_MS + w * 256 + (16 * th + 4 * G) * 8 + 16);
          const float cs = __uint_as_float(__builtin_amdgcn_raw_buffer_load_b32(rs, cc * 4, CS_OFF + 17 * 128 + dh * 64, 0));
          a[0] = (a[0] - m0.x * cs) * m0.y; a[1] = (a[1] - m0.z * cs) * m0.w; a[2] = (a[2] - m1.x * cs) * m1.y; a[3] = (a[3] - m1.z * cs) * m1.w;
          if (dh == 1 && cc == 12) { a[0] = 1.f; a[1] = 1.f; a[2] = 1.f; a[3] = 1.f; }
          dsto = 2 * KB + th * KB + (16 * dh + cc + 32 * (G & 1)) * 16 + 8 * (G >> 1);
        }
        o2 = (u32x2){pk(a[0], a[1]), pk(a[2], a[3])};
        __builtin_amdgcn_raw_buffer_store_b64(o2, rs, dsto, KVA_OFF + pair * KVA_L + c.i * 4 * KB, 16);
        DRAIN();
        __syncthreads();
        if (w == 0) {
          if (c.g == 0) { const f32x4 n = *(LAS f32x4*)(lds + L_MISC + 64); fl = __float_as_uint(fmaxf(n.x + n.y, n.z + n.w)) | 1u; }
          __hip_atomic_store(c.flags + (FA_OFF + ((pair * REPA + lane) * 4 + (c.i >> 4)) * 128) / 4 + (c.i & 15) * 2 + c.g, fl, RLX);
        }
      }
      { f32x16 a = {};
#pragma unroll
        for (int k = 0; k < 28; ++k) a = mfma32(Wq[k], XFRAG(k), a);
        epi_alpha(c, a, head, mu, rstd * QSCALE);
        float n2 = 0.f;
#pragma unroll
        for (int e = 0; e < 16; ++e) n2 = fmaf(a[e], a[e], n2);
        n2 = half_sum(n2);
        u32x4 f0, f1; pack16(a, f0, f1);
        if (hh) { f1.z = bf_ceil_bits(sqrtf(n2) * 1.02f) >> 16; f1.w = 0u; }
        LDS128(L_Q + w * 2 * KB + lo) = f0; LDS128(L_Q + w * 2 * KB + KB + lo) = f1;
      }
      const int base = KVA_OFF + pair * KVA_L + w * 64 * KB;
      { gu32* fp = c.flags + (FA_OFF + ((pair * REPA + (b & (REPA - 1))) * 4 + w) * 128) / 4 + c.r;
        unsigned fv, spins = 0;
        do { fv = __hip_atomic_load(fp, RLX); } while (!__all(fv != 0u) && ++spins < SPIN_CAP);
        __builtin_amdgcn_fence(__ATOMIC_ACQUIRE, "wavefront");
        float k2 = (lane & 1) ? 0.f : __uint_as_float(fv);
#pragma unroll
        for (int o = 2; o < 32; o <<= 1) k2 = fmaxf(k2, __shfl_xor(k2, o));
        if (lane == 0) *(LAS float*)(lds + L_MISC + w * 4) = k2;
      }
      __syncthreads();
      u32x4 q[4][2];
#pragma unroll
      for (int h = 0; h < 4; ++h) { q[h][0] = LDS128(L_Q + h * 2 * KB + lo); q[h][1] = LDS128(L_Q + h * 2 * KB + KB + lo); }
      { const f32x4 k4 = *(LAS f32x4*)(lds + L_MISC); const float kmax = sqrtf(fmaxf(fmaxf(k4.x, k4.y), fmaxf(k4.z, k4.w)));
#pragma unroll
        for (int h = 0; h < 4; ++h) if (hh) q[h][1].z = (bf_ceil_bits(bflo(q[h][1].z) * kmax) >> 16) ^ 0x8000u; }
#pragma unroll 1
      for (int attempt = 0; attempt < 2; ++attempt) {
        f32x16 o0 = {}, o1 = {}, o2 = {}, o3 = {};
        u32x4 ka0 = LDC(lo, base), ka1 = LDC(lo, base + KB), va0 = LDC(lo, base + 2 * KB), va1 = LDC(lo, base + 3 * KB);
        u32x4 kb0 = LDC(lo, base + 4 * KB), kb1 = LDC(lo, base + 5 * KB), vb0 = {0, 0, 0, 0}, vb1 = {0, 0, 0, 0};
        f32x16 sA, sB; u32x4 pA0, pA1, pB0 = {0, 0, 0, 0}, pB1 = {0, 0, 0, 0}; f32x4 ea, eb = {0.f, 0.f, 0.f, 0.f};
        asm volatile("s_nop 1");
        MS0(sA, ka0, q[0][0]); MS1(sA, ka1, q[0][1]);
        NOP16(); SBAR();
#pragma unroll 1
        for (int it = 0; it < 8; ++it) {
          const int o_ = base + it * 8 * KB;
          const int on = (it == 7) ? base : o_ + 8 * KB;
          STEP(sA, sB, ka0, ka1, q[1][0], q[1][1], o3, vb0, vb1, pB0, pB1, pA0, pA1, , );
          STEP(sB, sA, ka0, ka1, q[2][0], q[2][1], o0, va0, va1, pA0, pA1, pB0, pB1, vb0 = LDC(lo, o_ + 6 * KB), vb1 = LDC(lo, o_ + 7 * KB));
          STEP(sA, sB, ka0, ka1, q[3][0], q[3][1], o1, va0, va1, pB0, pB1, pA0, pA1, , );
          STEP(sB, sA, kb0, kb1, q[0][0], q[0][1], o2, va0, va1, pA0, pA1, pB0, pB1, ka0 = LDC(lo, on), ka1 = LDC(lo, on + KB));
          STEP(sA, sB, kb0, kb1, q[1][0], q[1][1], o3, va0, va1, pB0, pB1, pA0, pA1, , );
          STEP(sB, sA, kb0, kb1, q[2][0], q[2][1], o0, vb0, vb1, pA0, pA1, pB0, pB1, va0 = LDC(lo, on + 2 * KB), va1 = LDC(lo, on + 3 * KB));
          STEP(sA, sB, kb0, kb1, q[3][0], q[3][1], o1, vb0, vb1, pB0, pB1, pA0, pA1, , );
          STEP(sB, sA, ka0, ka1, q[0][0], q[0][1], o2, vb0, vb1, pA0, pA1, pB0, pB1, kb0 = LDC(lo, on + 4 * KB), kb1 = LDC(lo, on + 5 * KB));
        }
        C2(pB1, 2, eb) asm volatile("s_nop 1");
        MO(o3, vb0, pB0); MO(o3, vb1, pB1);
        NOP16(); SBAR();
#define PUTO(O, h) if (w != h) { const int s_ = (w > h) ? w - 1 : w; _Pragma("unroll") for (int e = 0; e < 4; ++e) \
          *(LAS f32x4*)(lds + L_C + (h * 3 + s_) * 4 * KB + e * KB + lo) = (f32x4){O[4 * e], O[4 * e + 1], O[4 * e + 2], O[4 * e + 3]}; }
        PUTO(o0, 0) PUTO(o1, 1) PUTO(o2, 2) PUTO(o3, 3)
        f32x16 o = (w == 0) ? o0 : (w == 1) ? o1 : (w == 2) ? o2 : o3;
        __syncthreads();
#pragma unroll
        for (int s = 0; s < 3; ++s)
#pragma unroll
          for (int e = 0; e < 4; ++e) { const f32x4 t = *(LAS f32x4*)(lds + L_C + (w * 3 + s) * 4 * KB + e * KB + lo);
            o[4 * e] += t.x; o[4 * e + 1] += t.y; o[4 * e + 2] += t.z; o[4 * e + 3] += t.w; }
        const float l = half_hi(o[12]);
        o_store(c, layer, o, l);
        const bool bad = !__all(l >= 1e-18f);
        if (lane == 0) *(LAS unsigned*)(lds + L_MISC + 16 + w * 4) = bad ? 1u : 0u;
        DRAIN();
        __syncthreads();
        const u32x4 bw = *(LAS u32x4*)(lds + L_MISC + 16);
        if (attempt == 1 || __builtin_amdgcn_readfirstlane(bw.x | bw.y | bw.z | bw.w) == 0u) break;
        float mx[4] = {-INFINITY, -INFINITY, -INFINITY, -INFINITY};
#pragma unroll
        for (int h = 0; h < 4; ++h) if (hh) q[h][1].z = 0u;
#pragma unroll 1
        for (int t = 0; t < 16; ++t) {
          const u32x4 k0 = LDC(lo, base + t * 4 * KB), k1 = LDC(lo, base + t * 4 * KB + KB);
#pragma unroll
          for (int h = 0; h < 4; ++h) { f32x16 s = {}; s = mfma32(k0, q[h][0], s); s = mfma32(k1, q[h][1], s);
#pragma unroll
            for (int e = 0; e < 16; ++e) mx[h] = fmaxf(mx[h], s[e]); }
        }
#pragma unroll
        for (int h = 0; h < 4; ++h) { const float m = half_max(mx[h]); if (hh == 0) *(LAS float*)(lds + L_MISC + 1024 + (w * 4 + h) * 128 + c.r * 4) = m; }
        __syncthreads();
#pragma unroll
        for (int h = 0; h < 4; ++h) { float m = -INFINITY;
#pragma unroll
          for (int s = 0; s < 4; ++s) m = fmaxf(m, *(LAS float*)(lds + L_MISC + 1024 + (s * 4 + h) * 128 + c.r * 4));
          if (hh) q[h][1].z = (bf_ceil_bits(m) >> 16) ^ 0x8000u; }
      }
      if (tid == 0) __hip_atomic_store(c.flags + (FO_OFF + (layer * 256 + c.cl + c.g) * 128) / 4, 1u, RLX);
      z_phase(c, layer, WOA_OFF, xres, out);
    }
    {
      const int layer = 2 * pair + 1;
      u32x4 Wk[28], Wv[28];
#pragma unroll
      for (int k = 0; k < 28; ++k) { Wk[k] = LDG(lo, WB_OFF + ((head * 3 + 1) * 28 + k) * KB); Wv[k] = LDG(lo, WB_OFF + ((head * 3 + 2) * 28 + k) * KB); }
      SBAR();
      float mu, rstd; x_gather(c, layer, mu, rstd);
      u32x4 K[9][2], V[9][2];
      {
        f32x16 ak = {}, av = {};
#pragma unroll
        for (int k = 0; k < 28; ++k) { const u32x4 xf = XFRAG(k); ak = mfma32(Wk[k], xf, ak); av = mfma32(xf, Wv[k], av); }
        epi_alpha(c, ak, 18 + head * 3 + 1, mu, rstd);
        if (hh) { ak[12] = 1.f; ak[13] = 0.f; ak[14] = 0.f; ak[15] = 0.f; }
        pack16(ak, K[8][0], K[8][1]);
        const float cs = __uint_as_float(__builtin_amdgcn_raw_buffer_load_b32(rs, c.r * 4, CS_OFF + (18 + head * 3 + 2) * 128, 0));
#pragma unroll
        for (int q4 = 0; q4 < 4; ++q4) {
          const f32x4 m0 = *(LAS f32x4*)(lds + L_MS + w * 256 + (8 * q4 + 4 * hh) * 8), m1 = *(LAS f32x4*)(lds + L_MS + w * 256 + (8 * q4 + 4 * hh) * 8 + 16);
          av[4 * q4] = (av[4 * q4] - m0.x * cs) * m0.y; av[4 * q4 + 1] = (av[4 * q4 + 1] - m0.z * cs) * m0.w;
          av[4 * q4 + 2] = (av[4 * q4 + 2] - m1.x * cs) * m1.y; av[4 * q4 + 3] = (av[4 * q4 + 3] - m1.z * cs) * m1.w;
        }
        pack16(av, V[8][0], V[8][1]);
        if (c.r == 28) { V[8][0] = (u32x4){0x3f803f80u, 0x3f803f80u, 0x3f803f80u, 0x3f803f80u}; V[8][1] = V[8][0]; }
        const int dst = KVB_OFF + pair * KVB_L + c.i * KVB_BLK + head * 4 * KB;
        STC(K[8][0], lo, dst); STC(K[8][1], lo, dst + KB); STC(V[8][0], lo, dst + 2 * KB); STC(V[8][1], lo, dst + 3 * KB);
        DRAIN();
        __syncthreads();
        if (tid == 0) __hip_atomic_store(c.flags + (FB_OFF + (pair * 256 + c.cl + c.g) * 128) / 4, 1u, RLX);
      }
      u32x4 q0, q1;
      { u32x4 Wq[28];
#pragma unroll
        for (int k = 0; k < 28; ++k) Wq[k] = LDG(lo, WB_OFF + ((head * 3) * 28 + k) * KB);
        SBAR();
        f32x16 a = {};
#pragma unroll
        for (int k = 0; k < 28; ++k) a = mfma32(Wq[k], XFRAG(k), a);
        epi_alpha(c, a, 18 + head * 3, mu, rstd * QSCALE);
        pack16(a, q0, q1);
        if (hh) { q1.z = 0u; q1.w = 0u; }
      }
      { const int j = lane & 7; int blk = c.i - 8 + j; if (blk < 0) blk = c.i;
        wait_flag(c.flags + (FB_OFF + (pair * 256 + blk * 4 + c.g) * 128) / 4); }
#pragma unroll
      for (int j = 7; j >= 0; --j) { const int blk = c.i - 8 + j; const int src = blk < 0 ? ZERO_OFF : KVB_OFF + pair * KVB_L + blk * KVB_BLK + head * 4 * KB;
        K[j][0] = LDC(lo, src); K[j][1] = LDC(lo, src + KB); }
#pragma unroll
      for (int j = 7; j >= 0; --j) { const int blk = c.i - 8 + j; const int src = blk < 0 ? ZERO_OFF : KVB_OFF + pair * KVB_L + blk * KVB_BLK + head * 4 * KB;
        V[j][0] = LDC(lo, src + 2 * KB); V[j][1] = LDC(lo, src + 3 * KB); }
      float mx = -INFINITY;
#pragma unroll
      for (int j = 8; j >= 0; --j) {
        if (c.i - 8 + j >= 0) {
          f32x16 s = {}; s = mfma32(K[j][0], q0, s); s = mfma32(K[j][1], q1, s);
#pragma unroll
          for (int e = 0; e < 16; ++e) { const int row = (e & 3) + 8 * (e >> 2) + 4 * hh; float v = s[e];
            if (j == 8) v = (row <= c.r) ? v : -INFINITY; if (j == 0) v = (row > c.r) ? v : -INFINITY;
            mx = fmaxf(mx, v); }
        }
      }
      mx = half_max(mx);
      if (hh) q1.z = (bf_ceil_bits(mx) >> 16) ^ 0x8000u;
      f32x16 o = {};
#pragma unroll
      for (int j = 8; j >= 0; --j) {
        f32x16 s = {}; s = mfma32(K[j][0], q0, s); s = mfma32(K[j][1], q1, s);
#pragma unroll
        for (int e = 0; e < 16; ++e) { const int row = (e & 3) + 8 * (e >> 2) + 4 * hh; float p = EX(s[e]);
          if (j == 8) p = (row <= c.r) ? p : 0.f; if (j == 0) p = (row > c.r) ? p : 0.f;
          s[e] = p; }
        u32x4 p0, p1; pack16(s, p0, p1);
        o = mfma32(V[j][0], p0, o); o = mfma32(V[j][1], p1, o);
      }
      o_store(c, layer, o, half_hi(o[12]));
      DRAIN();
      __syncthreads();
      if (tid == 0) __hip_atomic_store(c.flags + (FO_OFF + (layer * 256 + c.cl + c.g) * 128) / 4, 1u, RLX);
      z_phase(c, layer, WOB_OFF, xres, out);
    }
  }
}

extern "C" void kernel_launch(void* const* d_in, const int* in_sizes, int n_in, void* d_out, int out_size, void* d_ws, size_t ws_size, hipStream_t stream) {
  (void)in_sizes; (void)n_in; (void)out_size; (void)ws_size;
  (void)hipFuncSetAttribute((const void*)main_kernel, hipFuncAttributeMaxDynamicSharedMemorySize, L_BYTES);
  prep_kernel<<<PREP_GRID, 256, 0, stream>>>((const u16*)d_in[0], (const u16*)d_in[1], (const u16*)d_in[2], (const u16*)d_in[3], (const u16*)d_in[4],
                                             (const u16*)d_in[6], (const u16*)d_in[7], (unsigned char*)d_ws);
  main_kernel<<<256, 256, L_BYTES, stream>>>((unsigned char*)d_ws, (u16*)d_out);
}
```

```cpp
#include <hip/hip_runtime.h>
#include <hip/hip_bf16.h>

typedef float f32x16 __attribute__((ext_vector_type(16)));
typedef float f32x4 __attribute__((ext_vector_type(4)));
typedef float f32x2 __attribute__((ext_vector_type(2)));
typedef unsigned u32x4 __attribute__((ext_vector_type(4)));
typedef unsigned u32x2 __attribute__((ext_vector_type(2)));
typedef short bf16x8 __attribute__((ext_vector_type(8)));
typedef __bf16 bf2 __attribute__((ext_vector_type(2)));
typedef unsigned short u16;
#define GAS __attribute__((address_space(1)))
#define LAS __attribute__((address_space(3)))
typedef GAS unsigned gu32;
#define RLX __ATOMIC_RELAXED, __HIP_MEMORY_SCOPE_AGENT
#define SBAR() __builtin_amdgcn_sched_barrier(0)
#define PIN(x) asm volatile("" : "+v"(x))
#define DRAIN() asm volatile("s_waitcnt vmcnt(0)" ::: "memory")

constexpr int SEQ = 2048, DM = 448, NH = 16, DH = 28, NB = 64, NTL = 14;
constexpr int KB = 1024;
constexpr int FLAG_OFF = 0;
constexpr int REPA = 8;
constexpr int FA_OFF = 0;
constexpr int FX_OFF = FA_OFF + 2 * REPA * 1024;
constexpr int FO_OFF = FX_OFF + 4 * 256 * 128;
constexpr int FB_OFF = FO_OFF + 4 * 256 * 128;
constexpr int FLAG_BYTES = FB_OFF + 2 * 256 * 128;
constexpr int ZERO_OFF = FLAG_BYTES;
constexpr int ZEROED_BYTES = ZERO_OFF + 4 * KB;
constexpr int WQA_OFF = ZEROED_BYTES;
constexpr int WKV_OFF = WQA_OFF + 16 * 28 * KB;
constexpr int WOA_OFF = WKV_OFF + 56 * KB;
constexpr int WOB_OFF = WOA_OFF + 14 * 32 * KB;
constexpr int WB_OFF = WOB_OFF + 14 * 32 * KB;
constexpr int CS_OFF = WB_OFF + 16 * 3 * 28 * KB;
constexpr int KM_OFF = CS_OFF + 10 * KB;
constexpr int XT_OFF = CS_OFF + 12 * KB;
constexpr int XT_WG = 9 * KB, XT_BLK = 4 * XT_WG, XT_L = NB * XT_BLK;
constexpr int OT_OFF = XT_OFF + 4 * XT_L;
constexpr int OT_BLK = 32 * KB, OT_L = NB * OT_BLK;
constexpr int KVA_OFF = OT_OFF + 4 * OT_L;
constexpr int KVA_L = NB * 4 * KB;
constexpr int KVB_OFF = KVA_OFF + 2 * KVA_L;
constexpr int KVB_BLK = 64 * KB, KVB_L = NB * KVB_BLK;
constexpr int WS_BYTES = KVB_OFF + 2 * KVB_L;
static_assert(XT_OFF % 1024 == 0, "align");
constexpr int NDMA = 24;
constexpr int L_X = 0;
constexpr int L_O = 0;
constexpr int L_C = 0;
constexpr int L_MS = 48 * KB;
constexpr int L_MISC = L_MS + 1 * KB;
constexpr int L_Q = L_MISC + 3 * KB;
constexpr int L_W = L_Q + 8 * KB;
constexpr int L_BYTES = L_W + 4 * NDMA * KB;

#define STAMP(n) do {} while (0)
constexpr float QSCALE = 0.18898223650461361f * 1.4426950408889634f;
constexpr unsigned SPIN_CAP = 1u << 22;

__device__ __forceinline__ unsigned pk(float a, float b) { f32x2 v = {a, b}; bf2 r = __builtin_convertvector(v, bf2); return __builtin_bit_cast(unsigned, r); }
__device__ __forceinline__ float bflo(unsigned u) { return __uint_as_float(u << 16); }
__device__ __forceinline__ float bfhi(unsigned u) { return __uint_as_float(u & 0xffff0000u); }
__device__ __forceinline__ f32x16 mfma32(u32x4 a, u32x4 b, f32x16 c) {
  return __builtin_amdgcn_mfma_f32_32x32x16_bf16(__builtin_bit_cast(bf16x8, a), __builtin_bit_cast(bf16x8, b), c, 0, 0, 0); }
__device__ __forceinline__ f32x4 mfma16(u32x4 a, u32x4 b, f32x4 c) {
  return __builtin_amdgcn_mfma_f32_16x16x32_bf16(__builtin_bit_cast(bf16x8, a), __builtin_bit_cast(bf16x8, b), c, 0, 0, 0); }
__device__ __forceinline__ float half_sum(float v) { auto r = __builtin_amdgcn_permlane32_swap(__float_as_uint(v), __float_as_uint(v), false, false); return __uint_as_float(r[0]) + __uint_as_float(r[1]); }
__device__ __forceinline__ float half_max(float v) { auto r = __builtin_amdgcn_permlane32_swap(__float_as_uint(v), __float_as_uint(v), false, false); return fmaxf(__uint_as_float(r[0]), __uint_as_float(r[1])); }
__device__ __forceinline__ float half_hi(float v) { auto r = __builtin_amdgcn_permlane32_swap(__float_as_uint(v), __float_as_uint(v), false, false); return __uint_as_float(r[1]); }
__device__ __forceinline__ unsigned bf_ceil_bits(float v) { unsigned u = __float_as_uint(v); unsigned t = u & 0xffff0000u; return (v > 0.f && t != u) ? t + 0x10000u : t; }
__device__ __forceinline__ void pack16(const f32x16& v, u32x4& f0, u32x4& f1) {
  f0 = (u32x4){pk(v[0], v[1]), pk(v[2], v[3]), pk(v[4], v[5]), pk(v[6], v[7])};
  f1 = (u32x4){pk(v[8], v[9]), pk(v[10], v[11]), pk(v[12], v[13]), pk(v[14], v[15])}; }

#define LDG(voff, soff) __builtin_amdgcn_raw_buffer_load_b128(rs, (voff), (soff), 0)
#define LDC(voff, soff) __builtin_amdgcn_raw_buffer_load_b128(rs, (voff), (soff), 16)
#define STC(v, voff, soff) __builtin_amdgcn_raw_buffer_store_b128((v), rs, (voff), (soff), 16)
#define LDS128(off) (*(LAS u32x4*)(lds + (off)))

__device__ __forceinline__ void tile_stats(const u32x4& f0, const u32x4& f1, f32x16& v, float& mean, float& m2) {
#pragma unroll
  for (int i = 0; i < 4; ++i) { v[2 * i] = bflo(f0[i]); v[2 * i + 1] = bfhi(f0[i]); v[8 + 2 * i] = bflo(f1[i]); v[9 + 2 * i] = bfhi(f1[i]); }
  float s = 0.f;
#pragma unroll
  for (int i = 0; i < 16; ++i) s += v[i];
  mean = half_sum(s) * (1.f / 32.f);
  float q = 0.f;
#pragma unroll
  for (int i = 0; i < 16; ++i) { float d = v[i] - mean; q = fmaf(d, d, q); }
  m2 = half_sum(q);
}

constexpr int NFR_Q = 448, NFR_KV = 56, NFR_O = 448, NFR_B = 1344;
constexpr int T_FR = NFR_Q + NFR_KV + 2 * NFR_O + NFR_B;
constexpr int T_X = NB * NTL;
constexpr int PREP_WG_A = (T_FR + T_X) / 4;
constexpr int PREP_WG_CS = 66;
constexpr int PREP_WG_Z = ZEROED_BYTES / (256 * 16 * 4);
constexpr int PREP_GRID = PREP_WG_A + PREP_WG_CS + PREP_WG_Z + 1;

__global__ __launch_bounds__(256) void prep_kernel(const u16* __restrict__ x, const u16* __restrict__ wq, const u16* __restrict__ wk, const u16* __restrict__ wv,
                                                   const u16* __restrict__ woa, const u16* __restrict__ wb, const u16* __restrict__ wob, unsigned char* __restrict__ ws) {
  const int tid = threadIdx.x, lane = tid & 63, w = tid >> 6, r = lane & 31, hh = lane >> 5;
  const int b = blockIdx.x;
  if (b < PREP_WG_A) {
    int task = b * 4 + w;
    if (task < T_FR) {
      const u16* src; int ld, k0, col, kmode; bool colok = true; int dst;
      int hsel = hh; int dlim = 1 << 30;
      int dbase = 0;
      if (task < NFR_Q) { int head = task / 28, ks = task % 28; src = wq; ld = 448; k0 = 16 * ks; col = 28 * head + r; colok = r < 28; dst = WQA_OFF + task * KB; }
      else if (task < NFR_Q + NFR_KV) { int t = task - NFR_Q; int kv = t / 28, dh = (t / 14) & 1, kt = t % 14; src = kv ? wv : wk; ld = 28;
        int kg = lane >> 4, c = lane & 15; k0 = 32 * kt + 16 * (kg >> 1); hsel = kg & 1; col = 16 * dh + c; colok = col < 28; dst = WKV_OFF + t * KB; }
      else if (task < NFR_Q + NFR_KV + 2 * NFR_O) { int t = task - NFR_Q - NFR_KV; int which = t / NFR_O; t %= NFR_O; int tl = t / 32, ks = t % 32; src = which ? wob : woa; ld = 448;
        k0 = 28 * (ks >> 1) + 16 * (ks & 1); dbase = 16 * (ks & 1); dlim = 28; col = 32 * tl + r; dst = (which ? WOB_OFF : WOA_OFF) + t * KB; }
      else { int t = task - NFR_Q - NFR_KV - 2 * NFR_O; int head = t / 84, c3 = (t / 28) % 3, ks = t % 28; src = wb; ld = 1344; k0 = 16 * ks; col = 448 * c3 + 28 * head + r; colok = r < 28; dst = WB_OFF + t * KB; }
      unsigned e[8];
#pragma unroll
      for (int j = 0; j < 8; ++j) { int dk = 8 * (j >> 2) + 4 * hsel + (j & 3); bool ok = colok && (dbase + dk < dlim); e[j] = ok ? (unsigned)src[(k0 + dk) * ld + col] : 0u; }
      u32x4 v = {e[0] | (e[1] << 16), e[2] | (e[3] << 16), e[4] | (e[5] << 16), e[6] | (e[7] << 16)};
      *(u32x4*)(ws + dst + lane * 16) = v;
    } else {
      int t = task - T_FR; int i = t / NTL, tl = t % NTL;
      const u16* p = x + (32 * i + r) * DM + 32 * tl + 4 * hh;
      u32x2 c0 = *(const u32x2*)(p), c1 = *(const u32x2*)(p + 8), c2 = *(const u32x2*)(p + 16), c3 = *(const u32x2*)(p + 24);
      u32x4 f0 = {c0.x, c0.y, c1.x, c1.y}, f1 = {c2.x, c2.y, c3.x, c3.y};
      f32x16 v; float mean, m2; tile_stats(f0, f1, v, mean, m2);
      unsigned char* d = ws + XT_OFF + i * XT_BLK + (tl >> 2) * XT_WG;
      *(u32x4*)(d + (tl & 3) * 2048 + lane * 16) = f0; *(u32x4*)(d + (tl & 3) * 2048 + 1024 + lane * 16) = f1;
      if (hh == 0) *(f32x2*)(d + 8192 + (tl & 3) * 256 + r * 8) = (f32x2){mean, m2};
    }
  } else if (b < PREP_WG_A + PREP_WG_CS) {
    __shared__ float red[8][32];
    int t = b - PREP_WG_A; const u16* src; int ld, col;
    if (t < 16) { src = wq; ld = 448; col = 28 * t + r; } else if (t == 16) { src = wk; ld = 28; col = r; } else if (t == 17) { src = wv; ld = 28; col = r; }
    else { int u = t - 18; src = wb; ld = 1344; col = 448 * (u % 3) + 28 * (u / 3) + r; }
    float s = 0.f; const int part = tid >> 5;
    if (r < 28) {
#pragma unroll 14
      for (int k = 0; k < 56; ++k) s += __uint_as_float((unsigned)src[(part * 56 + k) * ld + col] << 16);
    }
    red[part][r] = s; __syncthreads();
    if (tid < 32) { float a = 0.f;
#pragma unroll
      for (int p = 0; p < 8; ++p) a += red[p][tid];
      *(float*)(ws + CS_OFF + t * 128 + tid * 4) = a; }
  } else {
    int t = b - PREP_WG_A - PREP_WG_CS;
#pragma unroll
    for (int j = 0; j < 4; ++j) { int off = t * 16384 + j * 4096 + tid * 16; if (off < ZEROED_BYTES) *(u32x4*)(ws + off) = (u32x4){0, 0, 0, 0}; }
  }
}

#define MS0(S, A, B) asm volatile("v_mfma_f32_32x32x16_bf16 %0, %1, %2, 0" : "=&v"(S) : "v"(A), "v"(B))
#define MS1(S, A, B) asm volatile("v_mfma_f32_32x32x16_bf16 %0, %1, %2, %0" : "+v"(S) : "v"(A), "v"(B))
#define MO(O, A, B) asm volatile("v_mfma_f32_32x32x16_bf16 %0, %1, %2, %0" : "+a"(O) : "v"(A), "v"(B))
#define EX(x) __builtin_amdgcn_exp2f(x)
#define E4(E, S, i) { E[0] = EX(S[i]); E[1] = EX(S[i + 1]); E[2] = EX(S[i + 2]); E[3] = EX(S[i + 3]); PIN(E); SBAR(); }
#define C2(P, j, E) { P[j] = pk(E[0], E[1]); P[j + 1] = pk(E[2], E[3]); PIN(P); SBAR(); }
#define STEP(SC, SN, KN0, KN1, QN0, QN1, OP, VP0, VP1, PP0, PP1, PC0, PC1, W0, W2, LDA, LDB) \
  W0; MS0(SN, KN0, QN0); SBAR(); E4(ea, SC, 0) C2(PP1, 2, eb) \
  MS1(SN, KN1, QN1); LDA; SBAR(); E4(eb, SC, 4) C2(PC0, 0, ea) \
  W2; MO(OP, VP0, PP0); SBAR(); E4(ea, SC, 8) C2(PC0, 2, eb) \
  MO(OP, VP1, PP1); LDB; SBAR(); E4(eb, SC, 12) C2(PC1, 0, ea)
#define NOP16() asm volatile("s_nop 7\n\ts_nop 7")
#define VMW(n) asm volatile("s_waitcnt vmcnt(" #n ")")
#define ALD(dst, soff, imm) asm volatile("buffer_load_dwordx4 %0, %1, %2, %3 offen offset:" #imm " sc1" : "+v"(dst) : "v"(lo), "s"(desc), "s"(soff))
#define ALDA(dst, soff, imm) asm volatile("buffer_load_dwordx4 %0, %1, %2, %3 offen offset:%4 sc1" : "=a"(dst) : "v"(lo), "s"(desc), "s"(soff), "i"(imm))
#define ADMA(ldsaddr, soff) asm volatile("s_mov_b32 m0, %0\n\ts_nop 0\n\tbuffer_load_dwordx4 %1, %2, %3 offen sc1 lds" :: "s"(ldsaddr), "v"(lo), "s"(desc), "s"(soff) : "memory")
#define ALD0(dst, soff, imm) asm volatile("buffer_load_dwordx4 %0, %1, %2, %3 offen offset:" #imm " sc1" : "=v"(dst) : "v"(lo), "s"(desc), "s"(soff))
#define LDWA(dst, base, k) { const int so_ = (base) + ((k) >> 2) * 4 * KB; ALDA(dst, so_, ((k) & 3) * KB); }
#define OPQ(x) asm volatile("" : "+s"(x))
#define LDW(base, k) LDC(lo + ((k) & 3) * KB, (base) + ((k) >> 2) * 4 * KB)

typedef GAS unsigned long long gu64;
__device__ __forceinline__ void poll(gu64* p, unsigned long long tag) {
  unsigned spins = 0;
  while (__hip_atomic_load(p, RLX) != tag) { if (++spins > SPIN_CAP) break; }
}
#define AFTER_POLL() __builtin_amdgcn_fence(__ATOMIC_ACQUIRE, "wavefront")

__global__ __launch_bounds__(256, 1) void main_kernel(unsigned char* __restrict__ ws, u16* __restrict__ out) {
  extern __shared__ __attribute__((aligned(16))) unsigned char smem[];
  const __amdgpu_buffer_rsrc_t rs = __builtin_amdgcn_make_buffer_rsrc((void*)ws, 0, WS_BYTES, 0x00020000);
  const u32x4 desc = {(unsigned)(size_t)ws, (unsigned)((size_t)ws >> 32) & 0xffffu, (unsigned)WS_BYTES, 0x00020000u};
  LAS unsigned char* lds = (LAS unsigned char*)smem;
  gu64* flags = (gu64*)ws;
  const unsigned long long tag = 1ull;
  int tid_ = threadIdx.x, b_ = blockIdx.x, w_ = __builtin_amdgcn_readfirstlane(threadIdx.x >> 6);
#define IDX() asm volatile("" : "+v"(tid_)); asm volatile("" : "+s"(b_), "+s"(w_)); \
  const int tid = tid_, b = b_, w = w_, lane = tid & 63, r = lane & 31, hh = lane >> 5, lo = lane * 16; \
  const int blk = (b & 7) * 8 + (b >> 5), g = (b >> 3) & 3, head = 4 * g + w, tl = head; const bool act = tl < NTL; const int tlc = act ? tl : 0; \
  const int dh = w & 1, th = w >> 1; (void)r; (void)hh; (void)dh; (void)th; (void)tlc; (void)blk; (void)lo
  STAMP(66);

  f32x16 xres;
  { IDX(); const int src = XT_OFF + blk * XT_BLK + (tlc >> 2) * XT_WG + (tlc & 3) * 2048;
    u32x4 f0 = LDC(lo, src), f1 = LDC(lo + KB, src);
#pragma unroll
    for (int q = 0; q < 4; ++q) { xres[2 * q] = bflo(f0[q]); xres[2 * q + 1] = bfhi(f0[q]); xres[8 + 2 * q] = bflo(f1[q]); xres[9 + 2 * q] = bfhi(f1[q]); } }

  u32x4 W16[14], WqA[28];
  { IDX(); int wb = WKV_OFF + ((g & 1) * 2 + dh) * 14 * KB; OPQ(wb);
#pragma unroll
    for (int k = 0; k < 14; ++k) LDWA(W16[k], wb, k);
    int wq = WQA_OFF + head * 28 * KB; OPQ(wq);
#pragma unroll
    for (int k = 0; k < 12; ++k) WqA[k] = LDW(wq, k);
    SBAR(); }

#define X_GATHER(layer) { \
    STAMP((layer) * 16 + 0); \
    if ((layer) > 0) { poll(flags + (FX_OFF + (((layer) * 256 + blk * 4 + w) * 128)) / 8, tag); AFTER_POLL(); } \
    STAMP((layer) * 16 + 1); \
    int src_ = XT_OFF + (layer) * XT_L + blk * XT_BLK + w * XT_WG; OPQ(src_); \
    u32x4 t_[9]; \
    _Pragma("unroll") for (int f = 0; f < 9; ++f) t_[f] = LDW(src_, f); \
    _Pragma("unroll") for (int f = 0; f < 9; ++f) LDS128(L_X + w * XT_WG + f * KB + lo) = t_[f]; \
    __syncthreads(); \
    STAMP((layer) * 16 + 2); }
#define X_STATS(mu, rstd) { \
    f32x2 st_[7]; \
    _Pragma("unroll") for (int k = 0; k < 7; ++k) st_[k] = *(LAS f32x2*)(lds + L_X + (k >> 1) * XT_WG + 8192 + (k & 1) * 512 + lane * 8); \
    mu = half_sum(((st_[0].x + st_[1].x) + (st_[2].x + st_[3].x)) + ((st_[4].x + st_[5].x) + st_[6].x)) * (1.f / 14.f); \
    float q_ = 0.f, q2_ = 0.f; \
    _Pragma("unroll") for (int k = 0; k < 7; ++k) { const float d = st_[k].x - mu; q_ = fmaf(d, d, q_); q2_ += st_[k].y; } \
    rstd = __builtin_amdgcn_rsqf(half_sum(fmaf(32.f, q_, q2_)) * (1.f / 448.f) + 1e-5f); \
    if (hh == 0) *(LAS f32x2*)(lds + L_MS + w * 256 + r * 8) = (f32x2){mu, rstd}; }
#define XFRAG(ks) LDS128(L_X + ((ks) >> 3) * XT_WG + ((ks) & 7) * KB + lo)
#define LDCS(cs, tile) { int o_ = CS_OFF + (tile) * 128; OPQ(o_); _Pragma("unroll") for (int q4 = 0; q4 < 4; ++q4) cs[q4] = __builtin_bit_cast(f32x4, LDC(hh * 16 + q4 * 32, o_)); }
#define EPI_ALPHA(a, cs, mu, sc) { const float nm_ = -(mu) * (sc); _Pragma("unroll") for (int e = 0; e < 16; ++e) a[e] = fmaf(a[e], (sc), nm_ * cs[e >> 2][e & 3]); }
#define PUBLISH(off) { DRAIN(); __syncthreads(); if (tid == 0) __hip_atomic_store(flags + (off) / 8, tag, RLX); }
#define O_STORE(layer, o, l) { const float inv_ = __builtin_amdgcn_rcpf(l); f32x16 v_; \
    _Pragma("unroll") for (int e = 0; e < 16; ++e) v_[e] = o[e] * inv_; \
    u32x4 f0_, f1_; pack16(v_, f0_, f1_); if (hh) { f1_.z = 0u; f1_.w = 0u; } \
    const int dst_ = OT_OFF + (layer) * OT_L + blk * OT_BLK + head * 2 * KB; STC(f0_, lo, dst_); STC(f1_, lo + KB, dst_); }
#define Z_PHASE(layer, PRE, SIDE, POST) { \
    STAMP((layer) * 16 + 9); \
    PRE; SBAR(); \
    STAMP((layer) * 16 + 10); \
    poll(flags + (FO_OFF + (((layer) * 256 + blk * 4 + w) * 128)) / 8, tag); AFTER_POLL(); \
    STAMP((layer) * 16 + 11); \
    int src_ = OT_OFF + (layer) * OT_L + blk * OT_BLK + w * 8 * KB; OPQ(src_); \
    u32x4 t_[8]; \
    _Pragma("unroll") for (int f = 0; f < 8; ++f) t_[f] = LDW(src_, f); \
    _Pragma("unroll") for (int f = 0; f < 8; ++f) LDS128(L_O + (w * 8 + f) * KB + lo) = t_[f]; \
    __syncthreads(); \
    STAMP((layer) * 16 + 12); \
    u32x4 of_[32]; \
    _Pragma("unroll") for (int k = 0; k < 6; ++k) of_[k] = LDS128(L_O + k * KB + lo); \
    SBAR(); \
    _Pragma("unroll") for (int k = 0; k < 32; ++k) { if (k + 6 < 32) of_[k + 6] = LDS128(L_O + (k + 6) * KB + lo); SIDE; xres = mfma32(WZ[k], of_[k], xres); SBAR(); } \
    u32x4 f0, f1; pack16(xres, f0, f1); \
    STAMP((layer) * 16 + 13); \
    if ((layer) == 3) { \
      if (act) { u16* p = out + (32 * blk + r) * DM + 32 * tl + 4 * hh; \
        *(u32x2*)(p) = (u32x2){f0.x, f0.y}; *(u32x2*)(p + 8) = (u32x2){f0.z, f0.w}; *(u32x2*)(p + 16) = (u32x2){f1.x, f1.y}; *(u32x2*)(p + 24) = (u32x2){f1.z, f1.w}; } \
      return; \
    } \
    const int dst = XT_OFF + ((layer) + 1) * XT_L + blk * XT_BLK + g * XT_WG; \
    if (act) { STC(f0, lo, dst + w * 2048); STC(f1, lo + KB, dst + w * 2048); } \
    float mean, m2; tile_stats(f0, f1, xres, mean, m2); \
    if (act && hh == 0) __builtin_amdgcn_raw_buffer_store_b64((u32x2){__float_as_uint(mean), __float_as_uint(m2)}, rs, r * 8, dst + 8192 + w * 256, 16); \
    STAMP((layer) * 16 + 14); \
    PUBLISH(FX_OFF + (((layer) + 1) * 256 + blk * 4 + g) * 128); \
    STAMP((layer) * 16 + 15); \
    POST; SBAR(); }

#pragma unroll 1
  for (int pair = 0; pair < 2; ++pair) {
    u32x4 WZ[32], Wv[28], WkT[4], WqB[28];
    {
      const int layer = 2 * pair;
      IDX();
      const int wbB = WB_OFF + head * 3 * 28 * KB;
      { int wq = WQA_OFF + head * 28 * KB; OPQ(wq);
#pragma unroll
        for (int k = 12; k < 28; ++k) WqA[k] = LDW(wq, k); }
      f32x4 csq[4]; LDCS(csq, head);
      const int G = lane >> 4, cc = lane & 15;
      f32x4 cskv = {0.f, 0.f, 0.f, 0.f};
      if (g == 0) cskv = __builtin_bit_cast(f32x4, LDC(G * 16, CS_OFF + 16 * 128 + dh * 64));
      else cskv.x = __uint_as_float(__builtin_amdgcn_raw_buffer_load_b32(rs, cc * 4, CS_OFF + 17 * 128 + dh * 64, 16));
      X_GATHER(layer);
      float mu, rstd;
      if (g < 2) {
        const int xo = L_X + (G >> 1) * KB + (16 * th + cc + 32 * (G & 1)) * 16;
        u32x4 xf[14];
#pragma unroll
        for (int k = 0; k < 14; ++k) xf[k] = LDS128(xo + (k >> 2) * XT_WG + (k & 3) * 2048);
        X_STATS(mu, rstd);
        f32x4 a = {0.f, 0.f, 0.f, 0.f}; int dsto;
        const int kvdst = KVA_OFF + pair * KVA_L + blk * 4 * KB;
        if (g == 0) {
#pragma unroll
          for (int k = 0; k < 14; ++k) a = mfma16(W16[k], xf[k], a);
          const f32x2 ms = *(LAS f32x2*)(lds + L_MS + w * 256 + (16 * th + cc) * 8);
          float n2 = 0.f;
#pragma unroll
          for (int e = 0; e < 4; ++e) { a[e] = (a[e] - ms.x * cskv[e]) * ms.y; n2 = fmaf(a[e], a[e], n2); }
          if (dh == 1 && G == 3) { a[0] = 1.f; a[1] = 0.f; a[2] = 0.f; a[3] = 0.f; }
          dsto = dh * KB + (16 * th + cc + 32 * (G & 1)) * 16 + 8 * (G >> 1);
          __builtin_amdgcn_raw_buffer_store_b64((u32x2){pk(a[0], a[1]), pk(a[2], a[3])}, rs, dsto, kvdst, 16);
          n2 = half_sum(n2); n2 += __shfl_xor(n2, 16);
#pragma unroll
          for (int o = 1; o < 16; o <<= 1) n2 = fmaxf(n2, __shfl_xor(n2, o));
          if (lane == 0) __builtin_amdgcn_raw_buffer_store_b32(__float_as_uint(n2), rs, 0, KM_OFF + (pair * 64 + blk) * 16 + w * 4, 16);
        } else {
#pragma unroll
          for (int k = 0; k < 14; ++k) a = mfma16(xf[k], W16[k], a);
          const f32x4 m0 = *(LAS f32x4*)(lds + L_MS + w * 256 + (16 * th + 4 * G) * 8), m1 = *(LAS f32x4*)(lds + L_MS + w * 256 + (16 * th + 4 * G) * 8 + 16);
          const float cs = cskv.x;
          a[0] = (a[0] - m0.x * cs) * m0.y; a[1] = (a[1] - m0.z * cs) * m0.w; a[2] = (a[2] - m1.x * cs) * m1.y; a[3] = (a[3] - m1.z * cs) * m1.w;
          if (dh == 1 && cc == 12) { a[0] = 1.f; a[1] = 1.f; a[2] = 1.f; a[3] = 1.f; }
          dsto = 2 * KB + th * KB + (16 * dh + cc + 32 * (G & 1)) * 16 + 8 * (G >> 1);
          __builtin_amdgcn_raw_buffer_store_b64((u32x2){pk(a[0], a[1]), pk(a[2], a[3])}, rs, dsto, kvdst, 16);
        }
        DRAIN();
        __syncthreads();
        if (tid < REPA) __hip_atomic_store(flags + (FA_OFF + (pair * REPA + tid) * KB) / 8 + blk * 2 + g, tag, RLX);
      } else {
        X_STATS(mu, rstd);
      }
      STAMP(layer * 16 + 3);
      int woz = WOA_OFF + tlc * 32 * KB; OPQ(woz);
      { f32x16 a = {}; u32x4 xf[28];
#pragma unroll
        for (int k = 0; k < 6; ++k) xf[k] = XFRAG(k);
        SBAR();
#pragma unroll
        for (int k = 0; k < 28; ++k) { if (k + 6 < 28) xf[k + 6] = XFRAG(k + 6);
          if (k & 1) { const int so = woz + (k >> 3) * 4 * KB; ALDA(WZ[k >> 1], so, ((k >> 1) & 3) * KB); }
          a = mfma32(WqA[k], xf[k], a); SBAR(); }
#pragma unroll
        for (int k = 14; k < 32; ++k) { const int so = woz + (k >> 2) * 4 * KB; ALDA(WZ[k], so, (k & 3) * KB); }
        SBAR();
        EPI_ALPHA(a, csq, mu, rstd * QSCALE);
        float n2 = 0.f;
#pragma unroll
        for (int e = 0; e < 16; ++e) n2 = fmaf(a[e], a[e], n2);
        n2 = half_sum(n2);
        u32x4 f0, f1; pack16(a, f0, f1);
        if (hh) { f1.z = bf_ceil_bits(__builtin_amdgcn_sqrtf(n2) * 1.02f) >> 16; f1.w = 0u; }
        LDS128(L_Q + w * 2 * KB + lo) = f0; LDS128(L_Q + w * 2 * KB + KB + lo) = f1;
      }
      STAMP(layer * 16 + 4);
      if (w < 2) { poll(flags + (FA_OFF + (pair * REPA + (b & (REPA - 1))) * KB) / 8 + tid, tag); AFTER_POLL(); }
      STAMP(layer * 16 + 5);
      __syncthreads();
      STAMP(layer * 16 + 6);
      int base = KVA_OFF + pair * KVA_L + w * 64 * KB; OPQ(base);
      u32x4 ka0, ka1, va0, va1, kb0, kb1, vb0, vb1;
      u32x4 q[4][2];
      { const f32x4 km = __builtin_bit_cast(f32x4, LDC(lo, KM_OFF + pair * KB));
        SBAR();
        ALD0(ka0, base, 0); ALD0(ka1, base, 1024); ALD0(va0, base, 2048); ALD0(va1, base, 3072);
        { const int b4 = base + 4 * KB; ALD0(kb0, b4, 0); ALD0(kb1, b4, 1024); }
#pragma unroll
        for (int h = 0; h < 4; ++h) { q[h][0] = LDS128(L_Q + h * 2 * KB + lo); q[h][1] = LDS128(L_Q + h * 2 * KB + KB + lo); }
        float k2 = fmaxf(km.x + km.y, km.z + km.w);
        k2 = half_max(k2);
#pragma unroll
        for (int o = 1; o < 32; o <<= 1) k2 = fmaxf(k2, __shfl_xor(k2, o));
        const float kmax = __builtin_amdgcn_sqrtf(k2);
#pragma unroll
        for (int h = 0; h < 4; ++h) { const unsigned m = (bf_ceil_bits(bflo(q[h][1].z) * kmax) >> 16) ^ 0x8000u; q[h][1].z = hh ? m : q[h][1].z; }
      }
#pragma unroll 1
      for (int attempt = 0; attempt < 2; ++attempt) {
        float z = 0.f; PIN(z); const unsigned zu = __float_as_uint(z);
        f32x16 o0 = z, o1 = z, o2 = z, o3 = z;
        vb0 = zu; vb1 = zu;
        f32x16 sA, sB; u32x4 pA0 = zu, pA1 = zu, pB0 = zu, pB1 = zu; f32x4 ea, eb = z;
        PIN(q[0][1]); PIN(q[1][1]); PIN(q[2][1]); PIN(q[3][1]); PIN(vb0); PIN(vb1); PIN(pA0); PIN(pA1); PIN(pB0); PIN(pB1); PIN(eb);
        asm volatile("" : "+a"(o0), "+a"(o1), "+a"(o2), "+a"(o3));
        VMW(4); SBAR();
        MS0(sA, ka0, q[0][0]); MS1(sA, ka1, q[0][1]);
        NOP16(); SBAR();
        unsigned dl = (unsigned)(size_t)(lds + L_W) + w * NDMA * KB; int dsrc = wbB + 28 * KB;
#pragma unroll 1
        for (int it = 0; it < 8; ++it) {
          const int o4 = base + it * 8 * KB + 4 * KB;
          const int on = (it == 7) ? base : o4 + 4 * KB, on4 = on + 4 * KB;
          const unsigned dl1 = dl + KB, dl2 = dl + 2 * KB; const int ds1 = dsrc + KB, ds2 = dsrc + 2 * KB;
          STEP(sA, sB, ka0, ka1, q[1][0], q[1][1], o3, vb0, vb1, pB0, pB1, pA0, pA1, , , ADMA(dl, dsrc), );
          STEP(sB, sA, ka0, ka1, q[2][0], q[2][1], o0, va0, va1, pA0, pA1, pB0, pB1, , VMW(4), ALD(vb0, o4, 2048), ALD(vb1, o4, 3072));
          STEP(sA, sB, ka0, ka1, q[3][0], q[3][1], o1, va0, va1, pB0, pB1, pA0, pA1, , , ADMA(dl1, ds1), );
          STEP(sB, sA, kb0, kb1, q[0][0], q[0][1], o2, va0, va1, pA0, pA1, pB0, pB1, VMW(4), , ALD(ka0, on, 0), ALD(ka1, on, 1024));
          STEP(sA, sB, kb0, kb1, q[1][0], q[1][1], o3, va0, va1, pB0, pB1, pA0, pA1, , , ADMA(dl2, ds2), );
          STEP(sB, sA, kb0, kb1, q[2][0], q[2][1], o0, vb0, vb1, pA0, pA1, pB0, pB1, , VMW(5), ALD(va0, on, 2048), ALD(va1, on, 3072));
          STEP(sA, sB, kb0, kb1, q[3][0], q[3][1], o1, vb0, vb1, pB0, pB1, pA0, pA1, , , , );
          STEP(sB, sA, ka0, ka1, q[0][0], q[0][1], o2, vb0, vb1, pA0, pA1, pB0, pB1, VMW(3), , ALD(kb0, on4, 0), ALD(kb1, on4, 1024));
          dl += 3 * KB; dsrc += 3 * KB;
        }
        C2(pB1, 2, eb) asm volatile("s_nop 1");
        MO(o3, vb0, pB0); MO(o3, vb1, pB1);
        NOP16(); SBAR();
        STAMP(layer * 16 + 7);
#define PUTO(O, h) if (w != h) { const int s_ = (w > h) ? w - 1 : w; _Pragma("unroll") for (int e = 0; e < 4; ++e) \
          *(LAS f32x4*)(lds + L_C + (h * 3 + s_) * 4 * KB + e * KB + lo) = (f32x4){O[4 * e], O[4 * e + 1], O[4 * e + 2], O[4 * e + 3]}; }
        PUTO(o0, 0) PUTO(o1, 1) PUTO(o2, 2) PUTO(o3, 3)
        f32x16 o = (w == 0) ? o0 : (w == 1) ? o1 : (w == 2) ? o2 : o3;
        __syncthreads();
#pragma unroll
        for (int s = 0; s < 3; ++s)
#pragma unroll
          for (int e = 0; e < 4; ++e) { const f32x4 t = *(LAS f32x4*)(lds + L_C + (w * 3 + s) * 4 * KB + e * KB + lo);
            o[4 * e] += t.x; o[4 * e + 1] += t.y; o[4 * e + 2] += t.z; o[4 * e + 3] += t.w; }
        const float l = half_hi(o[12]);
        O_STORE(layer, o, l);
        const bool bad = !__all(l >= 1e-18f);
        if (lane == 0) *(LAS unsigned*)(lds + L_MISC + w * 4) = bad ? 1u : 0u;
        STAMP(layer * 16 + 8);
        DRAIN();
        PIN(ka0); PIN(ka1); PIN(va0); PIN(va1); PIN(kb0); PIN(kb1);
        __syncthreads();
        const u32x4 bw = *(LAS u32x4*)(lds + L_MISC);
        if (attempt == 1 || __builtin_amdgcn_readfirstlane(bw.x | bw.y | bw.z | bw.w) == 0u) break;
        float mx[4] = {-INFINITY, -INFINITY, -INFINITY, -INFINITY};
#pragma unroll
        for (int h = 0; h < 4; ++h) q[h][1].z = hh ? 0u : q[h][1].z;
#pragma unroll 1
        for (int t = 0; t < 16; ++t) {
          const u32x4 k0 = LDC(lo, base + t * 4 * KB), k1 = LDC(lo + KB, base + t * 4 * KB);
#pragma unroll
          for (int h = 0; h < 4; ++h) { f32x16 s = {}; s = mfma32(k0, q[h][0], s); s = mfma32(k1, q[h][1], s);
#pragma unroll
            for (int e = 0; e < 16; ++e) mx[h] = fmaxf(mx[h], s[e]); }
        }
#pragma unroll
        for (int h = 0; h < 4; ++h) { const float m = half_max(mx[h]); if (hh == 0) *(LAS float*)(lds + L_MISC + 1024 + (w * 4 + h) * 128 + r * 4) = m; }
        __syncthreads();
#pragma unroll
        for (int h = 0; h < 4; ++h) { float m = -INFINITY;
#pragma unroll
          for (int s = 0; s < 4; ++s) m = fmaxf(m, *(LAS float*)(lds + L_MISC + 1024 + (s * 4 + h) * 128 + r * 4));
          q[h][1].z = hh ? ((bf_ceil_bits(m) >> 16) ^ 0x8000u) : q[h][1].z; }
      }
      if (tid == 0) __hip_atomic_store(flags + (FO_OFF + (layer * 256 + blk * 4 + g) * 128) / 8, tag, RLX);
      const int wvb = wbB + 56 * KB, wkb = wbB + 28 * KB;
      Z_PHASE(layer,
              _Pragma("unroll") for (int k = 0; k < 16; ++k) LDWA(Wv[k], wvb, k),
              if ((k & 1) && k < 24) LDWA(Wv[16 + (k >> 1)], wvb, 16 + (k >> 1)),
              _Pragma("unroll") for (int k = 0; k < 4; ++k) WkT[k] = LDW(wkb, 24 + k); _Pragma("unroll") for (int k = 0; k < 12; ++k) WqB[k] = LDW(wbB, k));
    }
    {
      const int layer = 2 * pair + 1;
      IDX();
      const int wbB = WB_OFF + head * 3 * 28 * KB;
      f32x4 csq[4], csk[4]; LDCS(csq, 18 + head * 3); LDCS(csk, 18 + head * 3 + 1);
      const float csv = __uint_as_float(__builtin_amdgcn_raw_buffer_load_b32(rs, r * 4, CS_OFF + (18 + head * 3 + 2) * 128, 16));
      X_GATHER(layer);
      float mu, rstd;
      u32x4 K[9][2], V[9][2];
      float kn2;
      {
        f32x16 ak = {}, av = {}; u32x4 xf[28], wk[24];
#pragma unroll
        for (int k = 0; k < 4; ++k) { xf[k] = XFRAG(k); wk[k] = LDS128(L_W + (w * NDMA + k) * KB + lo); }
        X_STATS(mu, rstd);
        SBAR();
#pragma unroll
        for (int k = 0; k < 28; ++k) {
          if (k + 4 < 28) xf[k + 4] = XFRAG(k + 4);
          if (k + 4 < 24) wk[k + 4] = LDS128(L_W + (w * NDMA + k + 4) * KB + lo);
          if (k < 16) WqB[12 + k] = LDW(wbB, 12 + k);
          ak = mfma32(k < 24 ? wk[k] : WkT[k - 24], xf[k], ak); av = mfma32(xf[k], Wv[k], av); SBAR(); }
        EPI_ALPHA(ak, csk, mu, rstd);
        kn2 = 0.f;
#pragma unroll
        for (int e = 0; e < 16; ++e) kn2 = fmaf(ak[e], ak[e], kn2);
        if (hh) { ak[12] = 1.f; ak[13] = 0.f; ak[14] = 0.f; ak[15] = 0.f; }
        pack16(ak, K[8][0], K[8][1]);
        const int dst = KVB_OFF + pair * KVB_L + blk * KVB_BLK + head * 4 * KB;
        STC(K[8][0], lo, dst); STC(K[8][1], lo + KB, dst);
#pragma unroll
        for (int q4 = 0; q4 < 4; ++q4) {
          const f32x4 m0 = *(LAS f32x4*)(lds + L_MS + w * 256 + (8 * q4 + 4 * hh) * 8), m1 = *(LAS f32x4*)(lds + L_MS + w * 256 + (8 * q4 + 4 * hh) * 8 + 16);
          av[4 * q4] = (av[4 * q4] - m0.x * csv) * m0.y; av[4 * q4 + 1] = (av[4 * q4 + 1] - m0.z * csv) * m0.w;
          av[4 * q4 + 2] = (av[4 * q4 + 2] - m1.x * csv) * m1.y; av[4 * q4 + 3] = (av[4 * q4 + 3] - m1.z * csv) * m1.w;
        }
        pack16(av, V[8][0], V[8][1]);
        if (r == 28) { V[8][0] = (u32x4){0x3f803f80u, 0x3f803f80u, 0x3f803f80u, 0x3f803f80u}; V[8][1] = V[8][0]; }
        STC(V[8][0], lo + 2 * KB, dst); STC(V[8][1], lo + 3 * KB, dst);
        PUBLISH(FB_OFF + (pair * 256 + blk * 4 + g) * 128);
      }
      STAMP(layer * 16 + 3);
      int woz = WOB_OFF + tlc * 32 * KB; OPQ(woz);
      u32x4 q0, q1;
      { f32x16 a = {}; u32x4 xf[28];
#pragma unroll
        for (int k = 0; k < 6; ++k) xf[k] = XFRAG(k);
        SBAR();
#pragma unroll
        for (int k = 0; k < 28; ++k) { if (k + 6 < 28) xf[k + 6] = XFRAG(k + 6);
          if (k & 1) { const int so = woz + (k >> 3) * 4 * KB; ALDA(WZ[k >> 1], so, ((k >> 1) & 3) * KB); }
          a = mfma32(WqB[k], xf[k], a); SBAR(); }
        EPI_ALPHA(a, csq, mu, rstd * QSCALE);
        pack16(a, q0, q1);
        if (hh) { q1.z = 0u; q1.w = 0u; }
      }
      STAMP(layer * 16 + 4);
      if (lane < 8 && blk - 8 + lane >= 0) poll(flags + (FB_OFF + (pair * 256 + (blk - 8 + lane) * 4 + g) * 128) / 8, tag);
      AFTER_POLL();
      STAMP(layer * 16 + 5);
#pragma unroll
      for (int j = 7; j >= 0; --j) { const int bj = blk - 8 + j; const int src = bj < 0 ? ZERO_OFF : KVB_OFF + pair * KVB_L + bj * KVB_BLK + head * 4 * KB;
        K[j][0] = LDC(lo, src); K[j][1] = LDC(lo + KB, src); }
#pragma unroll
      for (int j = 7; j >= 0; --j) { const int bj = blk - 8 + j; const int src = bj < 0 ? ZERO_OFF : KVB_OFF + pair * KVB_L + bj * KVB_BLK + head * 4 * KB;
        V[j][0] = LDC(lo + 2 * KB, src); V[j][1] = LDC(lo + 3 * KB, src); }
      SBAR();
      float mx = -INFINITY;
      PIN(q0); PIN(q1); PIN(K[8][0]); PIN(K[8][1]); asm volatile("s_nop 1");
      { f32x16 sA, sB;
        MS0(sA, K[8][0], q0); MS1(sA, K[8][1], q1);
#define MAXT(j, SC, SN) { if (j > 0) { MS0(SN, K[j > 0 ? j - 1 : 0][0], q0); MS1(SN, K[j > 0 ? j - 1 : 0][1], q1); } NOP16(); SBAR(); \
          float m_ = -INFINITY; \
          _Pragma("unroll") for (int e = 0; e < 16; ++e) { const int row = (e & 3) + 8 * (e >> 2) + 4 * hh; float v = SC[e]; \
            if (j == 8) v = (row <= r) ? v : -INFINITY; if (j == 0) v = (row > r) ? v : -INFINITY; m_ = fmaxf(m_, v); } \
          mx = (blk - 8 + j >= 0) ? fmaxf(mx, m_) : mx; PIN(mx); SBAR(); }
        MAXT(8, sA, sB) MAXT(7, sB, sA) MAXT(6, sA, sB) MAXT(5, sB, sA) MAXT(4, sA, sB) MAXT(3, sB, sA) MAXT(2, sA, sB) MAXT(1, sB, sA) MAXT(0, sA, sB)
      }
      mx = half_max(mx);
      q1.z = hh ? ((bf_ceil_bits(mx) >> 16) ^ 0x8000u) : q1.z;
      STAMP(layer * 16 + 6);
      float z = 0.f; PIN(z); const unsigned zu = __float_as_uint(z);
      f32x16 o = z;
      { f32x16 sA, sB; u32x4 pA0 = zu, pA1 = zu, pB0 = zu, pB1 = zu; f32x4 ea, eb = z;
        PIN(q1); PIN(pA0); PIN(pA1); PIN(pB0); PIN(pB1); PIN(eb); asm volatile("s_nop 1" : "+a"(o)); SBAR();
        MS0(sA, K[8][0], q0); MS1(sA, K[8][1], q1);
        NOP16(); SBAR();
#define MASKT(S, keep) { _Pragma("unroll") for (int e = 0; e < 16; ++e) { const int row = (e & 3) + 8 * (e >> 2) + 4 * hh; S[e] = (keep) ? S[e] : -INFINITY; } PIN(S); SBAR(); }
#define WZL(k) { const int so = woz + ((k) >> 2) * 4 * KB; ALDA(WZ[k], so, ((k) & 3) * KB); }
        MASKT(sA, row <= r)
        STEP(sA, sB, K[7][0], K[7][1], q0, q1, o, V[8][0], V[8][1], pB0, pB1, pA0, pA1, , , WZL(14), WZL(15));
        STEP(sB, sA, K[6][0], K[6][1], q0, q1, o, V[8][0], V[8][1], pA0, pA1, pB0, pB1, , , WZL(16), WZL(17));
        STEP(sA, sB, K[5][0], K[5][1], q0, q1, o, V[7][0], V[7][1], pB0, pB1, pA0, pA1, , , WZL(18) WZL(19), WZL(20));
        STEP(sB, sA, K[4][0], K[4][1], q0, q1, o, V[6][0], V[6][1], pA0, pA1, pB0, pB1, , , WZL(21) WZL(22), WZL(23));
        STEP(sA, sB, K[3][0], K[3][1], q0, q1, o, V[5][0], V[5][1], pB0, pB1, pA0, pA1, , , WZL(24) WZL(25), WZL(26));
        STEP(sB, sA, K[2][0], K[2][1], q0, q1, o, V[4][0], V[4][1], pA0, pA1, pB0, pB1, , , WZL(27) WZL(28), WZL(29));
        STEP(sA, sB, K[1][0], K[1][1], q0, q1, o, V[3][0], V[3][1], pB0, pB1, pA0, pA1, , , WZL(30), WZL(31));
        STEP(sB, sA, K[0][0], K[0][1], q0, q1, o, V[2][0], V[2][1], pA0, pA1, pB0, pB1, , , , );
        MASKT(sA, row > r)
        STEP(sA, sB, K[0][0], K[0][1], q0, q1, o, V[1][0], V[1][1], pB0, pB1, pA0, pA1, , , , );
        C2(pA1, 2, eb) asm volatile("s_nop 1");
        MO(o, V[0][0], pA0); MO(o, V[0][1], pA1);
        NOP16(); SBAR();
      }
      STAMP(layer * 16 + 7);
      { const float l = half_hi(o[12]); O_STORE(layer, o, l); }
      STAMP(layer * 16 + 8);
      PUBLISH(FO_OFF + (layer * 256 + blk * 4 + g) * 128);
      int wb = WKV_OFF + ((g & 1) * 2 + dh) * 14 * KB; OPQ(wb);
      int wq = WQA_OFF + head * 28 * KB; OPQ(wq);
      Z_PHASE(layer,
              _Pragma("unroll") for (int k = 0; k < 14; ++k) LDWA(W16[k], wb, k),
              if ((k & 1) && k < 24) WqA[k >> 1] = LDW(wq, k >> 1),
              );
    }
  }
}

extern "C" void kernel_launch(void* const* d_in, const int* in_sizes, int n_in, void* d_out, int out_size, void* d_ws, size_t ws_size, hipStream_t stream) {
  (void)in_sizes; (void)n_in; (void)out_size; (void)ws_size;
  (void)hipFuncSetAttribute((const void*)main_kernel, hipFuncAttributeMaxDynamicSharedMemorySize, L_BYTES);
  prep_kernel<<<PREP_GRID, 256, 0, stream>>>((const u16*)d_in[0], (const u16*)d_in[1], (const u16*)d_in[2], (const u16*)d_in[3], (const u16*)d_in[4],
                                             (const u16*)d_in[6], (const u16*)d_in[7], (unsigned char*)d_ws);
  main_kernel<<<256, 256, L_BYTES, stream>>>((unsigned char*)d_ws, (u16*)d_out);
}
```

```cpp
#include <hip/hip_runtime.h>
#include <hip/hip_bf16.h>

typedef float f32x16 __attribute__((ext_vector_type(16)));
typedef float f32x4 __attribute__((ext_vector_type(4)));
typedef float f32x2 __attribute__((ext_vector_type(2)));
typedef unsigned u32x4 __attribute__((ext_vector_type(4)));
typedef unsigned u32x2 __attribute__((ext_vector_type(2)));
typedef short bf16x8 __attribute__((ext_vector_type(8)));
typedef short s16x4 __attribute__((ext_vector_type(4)));
typedef __bf16 bf2 __attribute__((ext_vector_type(2)));
typedef unsigned short u16;
typedef unsigned long long u64;
#define GAS __attribute__((address_space(1)))
#define LAS __attribute__((address_space(3)))
typedef GAS u64 gu64;
#define RLX __ATOMIC_RELAXED, __HIP_MEMORY_SCOPE_AGENT
#define SBAR() __builtin_amdgcn_sched_barrier(0)
#define PIN(x) asm volatile("" : "+v"(x))
#define OPQ(x) asm volatile("" : "+s"(x))
#define DRAIN() asm volatile("s_waitcnt vmcnt(0)" ::: "memory")
#define UNR _Pragma("unroll")

constexpr int DM = 448, NB = 64, NTL = 14, KB = 1024;
constexpr int EP_OFF = 0;
constexpr int FA_OFF = 1 * KB;
constexpr int FX_OFF = FA_OFF + 2 * 8 * 2 * KB;
constexpr int FO_OFF = FX_OFF + 4 * 256 * 128;
constexpr int FB_OFF = FO_OFF + 4 * 256 * 128;
constexpr int WOA_OFF = FB_OFF + 2 * 256 * 128;
constexpr int WOB_OFF = WOA_OFF + 14 * 32 * KB;
constexpr int WB_OFF = WOB_OFF + 14 * 32 * KB;
constexpr int ZERO_OFF = WB_OFF + 16 * 3 * 28 * KB;
constexpr int CS_OFF = ZERO_OFF + 4 * KB;
constexpr int KM_OFF = CS_OFF + 6 * KB;
constexpr int KMB_OFF = KM_OFF + 2 * KB;
constexpr int XT_OFF = KMB_OFF + 8 * KB;
constexpr int XT_WG = 9 * KB, XT_BLK = 4 * XT_WG, XT_L = NB * XT_BLK;
constexpr int OT_OFF = XT_OFF + 4 * XT_L;
constexpr int OT_BLK = 32 * KB, OT_L = NB * OT_BLK;
constexpr int KVA_OFF = OT_OFF + 4 * OT_L;
constexpr int KVA_L = NB * 4 * KB;
constexpr int KVB_OFF = KVA_OFF + 2 * KVA_L;
constexpr int KVB_BLK = 64 * KB, KVB_L = NB * KVB_BLK;
constexpr int WKV_OFF = KVB_OFF + 2 * KVB_L;
constexpr int WS_BYTES = WKV_OFF + 128 * 4 * 14 * KB;
constexpr int L_X = 0;
constexpr int L_O = 0, L_C = 0;
constexpr int L_MISC = 48 * KB;
constexpr int L_Q = 52 * KB;
constexpr int L_RK = 36 * KB;
constexpr int L_RQ = L_RK + 448 * 56;
constexpr int L_MS = 159 * KB;
constexpr int L_BYTES = 160 * KB;
static_assert(L_RQ + 448 * 224 + 64 <= L_MS && L_RQ % 16 == 0, "lds");

#define STAMP(n) do {} while (0)
constexpr float QSCALE = 0.18898223650461361f * 1.4426950408889634f;
constexpr unsigned SPIN_CAP = 1u << 16;
constexpr float L_MIN = 1e-18f;

__device__ __forceinline__ unsigned pk(float a, float b) { f32x2 v = {a, b}; bf2 r = __builtin_convertvector(v, bf2); return __builtin_bit_cast(unsigned, r); }
__device__ __forceinline__ float bflo(unsigned u) { return __uint_as_float(u << 16); }
__device__ __forceinline__ float bfhi(unsigned u) { return __uint_as_float(u & 0xffff0000u); }
__device__ __forceinline__ f32x16 mfma32(u32x4 a, u32x4 b, f32x16 c) {
  return __builtin_amdgcn_mfma_f32_32x32x16_bf16(__builtin_bit_cast(bf16x8, a), __builtin_bit_cast(bf16x8, b), c, 0, 0, 0); }
__device__ __forceinline__ f32x4 mfma16(u32x4 a, u32x4 b, f32x4 c) {
  return __builtin_amdgcn_mfma_f32_16x16x32_bf16(__builtin_bit_cast(bf16x8, a), __builtin_bit_cast(bf16x8, b), c, 0, 0, 0); }
__device__ __forceinline__ float half_sum(float v) { auto r = __builtin_amdgcn_permlane32_swap(__float_as_uint(v), __float_as_uint(v), false, false); return __uint_as_float(r[0]) + __uint_as_float(r[1]); }
__device__ __forceinline__ float half_max(float v) { auto r = __builtin_amdgcn_permlane32_swap(__float_as_uint(v), __float_as_uint(v), false, false); return fmaxf(__uint_as_float(r[0]), __uint_as_float(r[1])); }
__device__ __forceinline__ float half_hi(float v) { auto r = __builtin_amdgcn_permlane32_swap(__float_as_uint(v), __float_as_uint(v), false, false); return __uint_as_float(r[1]); }
__device__ __forceinline__ float row_sum(float v) { auto r = __builtin_amdgcn_permlane16_swap(__float_as_uint(v), __float_as_uint(v), false, false); return __uint_as_float(r[0]) + __uint_as_float(r[1]); }
template <int C> __device__ __forceinline__ float dppf(float v) { return __uint_as_float(__builtin_amdgcn_update_dpp(0u, __float_as_uint(v), C, 0xf, 0xf, true)); }
__device__ __forceinline__ float sum8(float v) { v += dppf<0xb1>(v); v += dppf<0x4e>(v); return v + dppf<0x141>(v); }
template <int N> __device__ __forceinline__ unsigned ror(unsigned v) { return __builtin_amdgcn_update_dpp(0u, v, 0x120 + N, 0xf, 0xf, true); }
__device__ __forceinline__ unsigned umax(unsigned a, unsigned b) { return a > b ? a : b; }
__device__ __forceinline__ unsigned umax16(unsigned v) { v = umax(v, ror<1>(v)); v = umax(v, ror<2>(v)); v = umax(v, ror<4>(v)); return umax(v, ror<8>(v)); }
__device__ __forceinline__ unsigned umax32(unsigned v) { v = umax16(v); auto r = __builtin_amdgcn_permlane16_swap(v, v, false, false); return umax(r[0], r[1]); }
__device__ __forceinline__ unsigned umax64(unsigned v) { v = umax32(v); auto r = __builtin_amdgcn_permlane32_swap(v, v, false, false); return umax(r[0], r[1]); }
__device__ __forceinline__ unsigned bf_ceil(float v) { unsigned u = __float_as_uint(v); unsigned t = u >> 16; return (v > 0.f && (u & 0xffffu)) ? t + 1u : t; }
__device__ __forceinline__ void pack16(const f32x16& v, u32x4& f0, u32x4& f1) {
  f0 = (u32x4){pk(v[0], v[1]), pk(v[2], v[3]), pk(v[4], v[5]), pk(v[6], v[7])};
  f1 = (u32x4){pk(v[8], v[9]), pk(v[10], v[11]), pk(v[12], v[13]), pk(v[14], v[15])}; }
__device__ __forceinline__ void unpack16(const u32x4 f0, const u32x4 f1, f32x16& v) {
  UNR for (int i = 0; i < 4; ++i) { v[2 * i] = bflo(f0[i]); v[2 * i + 1] = bfhi(f0[i]); v[8 + 2 * i] = bflo(f1[i]); v[9 + 2 * i] = bfhi(f1[i]); } }
__device__ __forceinline__ void tile_stats(const f32x16& v, float& mean, float& m2) {
  const float s = (((v[0] + v[1]) + (v[2] + v[3])) + ((v[4] + v[5]) + (v[6] + v[7]))) + (((v[8] + v[9]) + (v[10] + v[11])) + ((v[12] + v[13]) + (v[14] + v[15])));
  mean = half_sum(s) * (1.f / 32.f);
  float q0 = 0.f, q1 = 0.f;
  UNR for (int i = 0; i < 8; ++i) { const float d0 = v[i] - mean, d1 = v[8 + i] - mean; q0 = fmaf(d0, d0, q0); q1 = fmaf(d1, d1, q1); }
  m2 = half_sum(q0 + q1);
}
template <int V> struct IC { static constexpr int value = V; };
template <int I, int N, class F> __device__ __forceinline__ void sfor(F&& f) { if constexpr (I < N) { f(IC<I>{}); sfor<I + 1, N>(f); } }
__device__ __forceinline__ u32x4 mkdesc(const void* p, unsigned bytes) { return (u32x4){(unsigned)(size_t)p, (unsigned)((size_t)p >> 32) & 0xffffu, bytes, 0x00020000u}; }

#define LDC(voff, soff) __builtin_amdgcn_raw_buffer_load_b128(rs, (voff), (soff), 16)
#define STC(v, voff, soff) __builtin_amdgcn_raw_buffer_store_b128((v), rs, (voff), (soff), 16)
#define LDS128(off) (*(LAS u32x4*)(lds + (off)))
#define LDW(base, k) LDC(lo + ((k) & 3) * KB, (base) + ((k) >> 2) * 4 * KB)
#define MS0(S, A, B) asm volatile("v_mfma_f32_32x32x16_bf16 %0, %1, %2, 0" : "=&v"(S) : "v"(A), "v"(B))
#define MS1(S, A, B) asm volatile("v_mfma_f32_32x32x16_bf16 %0, %1, %2, %0" : "+v"(S) : "v"(A), "v"(B))
#define MO(O, A, B) asm volatile("v_mfma_f32_32x32x16_bf16 %0, %1, %2, %0" : "+a"(O) : "v"(A), "v"(B))
#define EX(x) __builtin_amdgcn_exp2f(x)
#define E4(E, S, i) { E[0] = EX(S[i]); E[1] = EX(S[i + 1]); E[2] = EX(S[i + 2]); E[3] = EX(S[i + 3]); PIN(E); SBAR(); }
#define C2(P, j, E) { P[j] = pk(E[0], E[1]); P[j + 1] = pk(E[2], E[3]); PIN(P); SBAR(); }
#define STEP(SC, SN, KN0, KN1, QN0, QN1, OP, VP0, VP1, PP0, PP1, PC0, PC1, W0, W2, LDA, LDB) \
  W0; MS0(SN, KN0, QN0); SBAR(); E4(ea, SC, 0) C2(PP1, 2, eb) \
  MS1(SN, KN1, QN1); LDA; SBAR(); E4(eb, SC, 4) C2(PC0, 0, ea) \
  W2; MO(OP, VP0, PP0); SBAR(); E4(ea, SC, 8) C2(PC0, 2, eb) \
  MO(OP, VP1, PP1); LDB; SBAR(); E4(eb, SC, 12) C2(PC1, 0, ea)
#define NOP16V(x) asm volatile("s_nop 7\n\ts_nop 7" : "+v"(x))
#define NOP16A(x) asm volatile("s_nop 7\n\ts_nop 7" : "+a"(x))
#define VMW(n) asm volatile("s_waitcnt vmcnt(" #n ")")
#define ALD(dst, soff, imm) asm volatile("buffer_load_dwordx4 %0, %1, %2, %3 offen offset:" #imm " sc1" : "+v"(dst) : "v"(lo), "s"(desc), "s"(soff))
#define ALD0(dst, soff, imm) asm volatile("buffer_load_dwordx4 %0, %1, %2, %3 offen offset:" #imm " sc1" : "=v"(dst) : "v"(lo), "s"(desc), "s"(soff))
#define ALDA(dst, soff, imm) asm volatile("buffer_load_dwordx4 %0, %1, %2, %3 offen offset:%4 sc1" : "=a"(dst) : "v"(lo), "s"(desc), "s"(soff), "i"(imm))
#define LDWA(dst, base, k) { const int so_ = (base) + ((k) >> 2) * 4 * KB; ALDA(dst, so_, ((k) & 3) * KB); }
#define ADMA(ldsaddr, voff, dsc, soff) asm volatile("s_mov_b32 m0, %0\n\ts_nop 0\n\tbuffer_load_dwordx4 %1, %2, %3 offen lds" :: "s"(ldsaddr), "v"(voff), "s"(dsc), "s"(soff) : "memory")
#define DMAW(base, k) { const unsigned la_ = L_W + w * L_WW + (k) * KB; const int so_ = (base) + (k) * KB; ADMA(la_, lo, desc, so_); }
#define TRRD(off) __builtin_bit_cast(u32x2, __builtin_amdgcn_ds_read_tr16_b64_v4i16((LAS s16x4*)(lds + (off))))

__device__ __forceinline__ void poll(gu64* p, u64 tag) {
  unsigned spins = 0;
  while (__hip_atomic_load(p, RLX) != tag) { if (++spins > SPIN_CAP) break; }
}
#define AFTER_POLL() __builtin_amdgcn_fence(__ATOMIC_ACQUIRE, "wavefront")

__global__ __launch_bounds__(256, 1) void main_kernel(const u16* __restrict__ x, const u16* __restrict__ wq, const u16* __restrict__ wk, const u16* __restrict__ wv,
                                                      const u16* __restrict__ woa, const u16* __restrict__ wb, const u16* __restrict__ wob,
                                                      unsigned char* __restrict__ ws, u16* __restrict__ out) {
  extern __shared__ __attribute__((aligned(16))) unsigned char smem[];
  const __amdgpu_buffer_rsrc_t rs = __builtin_amdgcn_make_buffer_rsrc((void*)ws, 0, WS_BYTES, 0x00020000);
  const u32x4 desc = mkdesc(ws, WS_BYTES);
  LAS unsigned char* lds = (LAS unsigned char*)smem;
  gu64* flags = (gu64*)ws;
  int tid_ = threadIdx.x, b_ = blockIdx.x, w_ = __builtin_amdgcn_readfirstlane(threadIdx.x >> 6);
#define IDX() asm volatile("" : "+v"(tid_)); asm volatile("" : "+s"(b_), "+s"(w_)); \
  const int tid = tid_, b = b_, w = w_, lane = tid & 63, r = lane & 31, hh = lane >> 5, lo = lane * 16; \
  const int blk = (b & 7) * 8 + (b >> 5), g = (b >> 3) & 3, head = 4 * g + w, tl = head; const bool act = tl < NTL; const int tlc = act ? tl : 0; \
  const int dh = w & 1, th = w >> 1; (void)r; (void)hh; (void)dh; (void)th; (void)tlc; (void)blk; (void)lo
  STAMP(64);

  f32x16 xres, csq;
  f32x4 cskv;
  u32x4 W16[14];
  u64 tag;
  const u32x4 ones = {0x3f803f80u, 0x3f803f80u, 0x3f803f80u, 0x3f803f80u};
  {
    IDX();
    const u64 ep = __hip_atomic_load(flags + EP_OFF / 8, RLX);
    u32x4 xr[7];
    UNR for (int f = 0; f < 7; ++f) xr[f] = *(const u32x4*)(x + (32 * blk + 8 * w + (lane >> 3)) * DM + 64 * f + 8 * (lane & 7));
    SBAR();
    const int n = blk * 2 + (g & 1), nw = n - 84;
    const int c3 = (n * 37) >> 10, ks = n - 28 * c3;
    u32x4 cl[4];
    if (g < 2 || (n >= 84 && n < 116)) { const u32x4 dk = g < 2 ? mkdesc(g ? wv : wk, 448 * 56) : mkdesc((nw & 16 ? wob : woa) + (nw & 15) * 28 * 448, 448 * 56);
      UNR for (int k = 0; k < 7; ++k) if (4 * k + w < 25) { const unsigned la = L_RK + (4 * k + w) * KB; const int so = (4 * k + w) * KB;
        if (4 * k + w < 24 || lane < 32) ADMA(la, lo, dk, so); } }
    else if (n < 84) { const u32x4 dk = mkdesc(wb + 16 * ks * 1344 + 448 * c3, 16 * 2688);
      UNR for (int k = 0; k < 4; ++k) if (4 * k + w < 14) { const int c = tid + 256 * k, row = (c * 1171) >> 16; const unsigned la = L_RK + (4 * k + w) * KB;
        ADMA(la, row * 2688 + (c - row * 56) * 16, dk, 0); }
      const __amdgpu_buffer_rsrc_t rb = __builtin_amdgcn_make_buffer_rsrc((void*)wb, 0, 448 * 2688, 0x00020000);
      UNR for (int j = 0; j < 4; ++j) cl[j] = __builtin_amdgcn_raw_buffer_load_b128(rb, (tid >> 1) * 2688 + (tid & 1) * 16, j * 128 * 2688 + n * 32, 0); }
    { const u32x4 dq = mkdesc(wq, 448 * 448 * 2); int vo[7];
      UNR for (int k = 0; k < 7; ++k) { const int c = tid + 256 * k, row = (c * 4682) >> 16; vo[k] = row * 896 + (c - row * 14) * 16 + g * 224; }
      UNR for (int u0 = 0; u0 < 4; ++u0) { const int u = (u0 + blk) & 3;
        UNR for (int k = 0; k < 7; ++k) { const unsigned la = L_RQ + (4 * (7 * u + k) + w) * KB; const int so = u * 128 * 896; ADMA(la, vo[k], dq, so); } } }
    u32x2 xo[4];
    UNR for (int a = 0; a < 4; ++a) xo[a] = *(const u32x2*)(x + (32 * blk + r) * DM + 32 * tlc + 8 * a + 4 * hh);
    SBAR();
    STAMP(67);
    { float v[56]; float s0 = 0.f, s1 = 0.f;
      UNR for (int f = 0; f < 7; ++f) UNR for (int c = 0; c < 4; ++c) { v[8 * f + 2 * c] = bflo(xr[f][c]); v[8 * f + 2 * c + 1] = bfhi(xr[f][c]); s0 += v[8 * f + 2 * c]; s1 += v[8 * f + 2 * c + 1]; }
      const float mu = sum8(s0 + s1) * (1.f / 448.f);
      s0 = 0.f; s1 = 0.f;
      UNR for (int i = 0; i < 56; i += 2) { v[i] -= mu; v[i + 1] -= mu; s0 = fmaf(v[i], v[i], s0); s1 = fmaf(v[i + 1], v[i + 1], s1); }
      const float rstd = __builtin_amdgcn_rsqf(sum8(s0 + s1) * (1.f / 448.f) + 1e-5f);
      const int c = lane & 7;
      UNR for (int f = 0; f < 7; ++f)
        LDS128(L_X + (f >> 1) * XT_WG + (f & 1) * 4 * KB + (c >> 1) * KB + (8 * w + (lane >> 3) + 32 * (c & 1)) * 16) =
          (u32x4){pk(v[8 * f] * rstd, v[8 * f + 1] * rstd), pk(v[8 * f + 2] * rstd, v[8 * f + 3] * rstd), pk(v[8 * f + 4] * rstd, v[8 * f + 5] * rstd), pk(v[8 * f + 6] * rstd, v[8 * f + 7] * rstd)};
      const float one = 32.f * (1.f - 1e-5f);
      *(LAS f32x4*)(lds + L_X + w * XT_WG + 8192 + lo) = (f32x4){0.f, one, 0.f, one};
    }
    tag = (((u64)(unsigned)__builtin_amdgcn_readfirstlane((unsigned)(ep >> 32)) << 32) | (unsigned)__builtin_amdgcn_readfirstlane((unsigned)ep)) + 1ull;
    if (g >= 2 && n < 84) { float s[8];
      UNR for (int c = 0; c < 4; ++c) { s[2 * c] = (bflo(cl[0][c]) + bflo(cl[1][c])) + (bflo(cl[2][c]) + bflo(cl[3][c])); s[2 * c + 1] = (bfhi(cl[0][c]) + bfhi(cl[1][c])) + (bfhi(cl[2][c]) + bfhi(cl[3][c])); }
      *(LAS f32x4*)(lds + L_RK + 15 * KB + tid * 32) = (f32x4){s[0], s[1], s[2], s[3]}; *(LAS f32x4*)(lds + L_RK + 15 * KB + tid * 32 + 16) = (f32x4){s[4], s[5], s[6], s[7]}; }
    unpack16((u32x4){xo[0].x, xo[0].y, xo[1].x, xo[1].y}, (u32x4){xo[2].x, xo[2].y, xo[3].x, xo[3].y}, xres);
    STAMP(68);
    asm volatile("s_waitcnt vmcnt(0) lgkmcnt(0)\n\ts_barrier" ::: "memory");
    STAMP(65);
    const int q4 = (lane & 15) >> 2, p4 = lane & 3, gq = lane >> 4;
    cskv = (f32x4){0.f, 0.f, 0.f, 0.f}; csq = (f32x16){};
    if (g < 2) { const int to = L_RK + (8 * gq + q4) * 56 + dh * 32 + p4 * 8;
      UNR for (int k = 0; k < 14; ++k) { const u32x2 a = TRRD(to + k * 32 * 56), c = TRRD(to + k * 32 * 56 + 4 * 56); W16[k] = (u32x4){a.x, a.y, c.x, c.y}; }
    } else {
      UNR for (int k = 0; k < 14; ++k) W16[k] = ones;
      const int to = L_RK + (4 * hh + q4) * 896 + (gq & 1) * 32 + p4 * 8;
      if (n < 84) {
        UNR for (int j = 0; j < 4; ++j) { const int h = 4 * w + j; const u32x2 a = TRRD(to + h * 56), c = TRRD(to + h * 56 + 8 * 896);
          STC(((u32x4){a.x, a.y, c.x, c.y}), lo, WB_OFF + ((h * 3 + c3) * 28 + ks) * KB); }
        if (w == 0) { float s = 0.f;
          UNR for (int j = 0; j < 32; ++j) s += *(LAS float*)(lds + L_RK + 15 * KB + (j * 4 + gq) * 64 + (lane & 15) * 4);
          s = half_sum(row_sum(s));
          if (lane < 16) __builtin_amdgcn_raw_buffer_store_b32(__float_as_uint(s), rs, lane * 4, CS_OFF + n * 64, 16); }
      } else if (n < 116) {
        UNR for (int j = 0; j < 4; ++j) if (4 * j + w < NTL) { const int t = 4 * j + w;
          const u32x2 a = TRRD(to + t * 64), c = TRRD(to + t * 64 + 8 * 896), d = TRRD(to + t * 64 + 16 * 896), e = TRRD(to + t * 64 + 24 * 896);
          const int dst = (nw & 16 ? WOB_OFF : WOA_OFF) + (t * 32 + 2 * (nw & 15)) * KB;
          STC(((u32x4){a.x, a.y, c.x, c.y}), lo, dst); STC(((u32x4){d.x, d.y, hh ? 0u : e.x, hh ? 0u : e.y}), lo + KB, dst); }
      } else if (n == 116) STC(((u32x4){0u, 0u, 0u, 0u}), lo, ZERO_OFF + w * KB);
    }
    STAMP(66);
  }

#define X_GATHER(layer) { \
    STAMP((layer) * 16 + 0); \
    poll(flags + (FX_OFF + (((layer) * 256 + blk * 4 + w) * 128)) / 8, tag); AFTER_POLL(); \
    STAMP((layer) * 16 + 1); \
    int src_ = XT_OFF + (layer) * XT_L + blk * XT_BLK + w * XT_WG; OPQ(src_); \
    u32x4 t_[9]; \
    UNR for (int f = 0; f < 9; ++f) t_[f] = LDW(src_, f); \
    UNR for (int f = 0; f < 9; ++f) LDS128(L_X + w * XT_WG + f * KB + lo) = t_[f]; \
    __syncthreads(); \
    STAMP((layer) * 16 + 2); }
#define X_STATS_LD() f32x2 st_[7]; \
    UNR for (int k = 0; k < 7; ++k) st_[k] = *(LAS f32x2*)(lds + L_X + (k >> 1) * XT_WG + 8192 + (k & 1) * 512 + lane * 8)
#define X_STATS(mu, rstd) { \
    mu = half_sum(((st_[0].x + st_[1].x) + (st_[2].x + st_[3].x)) + ((st_[4].x + st_[5].x) + st_[6].x)) * (1.f / 14.f); \
    float q_ = 0.f, q2_ = 0.f; \
    UNR for (int k = 0; k < 7; ++k) { const float d = st_[k].x - mu; q_ = fmaf(d, d, q_); q2_ += st_[k].y; } \
    rstd = __builtin_amdgcn_rsqf(half_sum(fmaf(32.f, q_, q2_)) * (1.f / 448.f) + 1e-5f); \
    *(LAS float*)(lds + L_MS + w * 256 + lane * 4) = hh ? -mu * rstd : rstd; }
#define XFRAG(ks) LDS128(L_X + ((ks) >> 3) * XT_WG + ((ks) & 7) * KB + lo)
#define LDCS(cs, col) { int o_ = CS_OFF + (col) * 4; OPQ(o_); UNR for (int q4 = 0; q4 < 4; ++q4) { const f32x4 c_ = __builtin_bit_cast(f32x4, LDC(hh * 16 + q4 * 32, o_)); \
    cs[4 * q4] = c_.x; cs[4 * q4 + 1] = c_.y; cs[4 * q4 + 2] = c_.z; cs[4 * q4 + 3] = c_.w; } }
#define EPI_ALPHA(a, cs, mu, sc, n2) { const float nm_ = -(mu) * (sc); UNR for (int e = 0; e < 16; ++e) a[e] = fmaf(a[e], (sc), nm_ * cs[e]); \
    UNR for (int e = 12; e < 16; ++e) a[e] = hh ? 0.f : a[e]; \
    float n0_ = 0.f, n1_ = 0.f; UNR for (int e = 0; e < 8; ++e) { n0_ = fmaf(a[e], a[e], n0_); n1_ = fmaf(a[8 + e], a[8 + e], n1_); } n2 = half_sum(n0_ + n1_); }
#define PUBLISH(off) { DRAIN(); __syncthreads(); if (tid == 0) __hip_atomic_store(flags + (off) / 8, tag, RLX); }
#define O_STORE(layer, o, l) { const float inv_ = __builtin_amdgcn_rcpf(l); f32x16 v_; \
    UNR for (int e = 0; e < 16; ++e) v_[e] = o[e] * inv_; \
    u32x4 f0_, f1_; pack16(v_, f0_, f1_); if (hh) { f1_.z = 0u; f1_.w = 0u; } \
    const int dst_ = OT_OFF + (layer) * OT_L + blk * OT_BLK + head * 2 * KB; STC(f0_, lo, dst_); STC(f1_, lo + KB, dst_); }
#define Z_PHASE(layer, PRE, SIDE) { \
    STAMP((layer) * 16 + 9); \
    PRE; \
    SBAR(); \
    STAMP((layer) * 16 + 10); \
    poll(flags + (FO_OFF + (((layer) * 256 + blk * 4 + w) * 128)) / 8, tag); AFTER_POLL(); \
    STAMP((layer) * 16 + 11); \
    int src_ = OT_OFF + (layer) * OT_L + blk * OT_BLK + w * 8 * KB; OPQ(src_); \
    u32x4 t_[8]; \
    UNR for (int f = 0; f < 8; ++f) t_[f] = LDW(src_, f); \
    UNR for (int f = 0; f < 8; ++f) LDS128(L_O + (w * 8 + f) * KB + lo) = t_[f]; \
    __syncthreads(); \
    STAMP((layer) * 16 + 12); \
    u32x4 of_[32]; \
    UNR for (int k = 0; k < 6; ++k) of_[k] = LDS128(L_O + k * KB + lo); \
    SBAR(); \
    UNR for (int k = 0; k < 32; ++k) { if (k + 6 < 32) of_[k + 6] = LDS128(L_O + (k + 6) * KB + lo); SIDE; xres = mfma32(WZ[k], of_[k], xres); SBAR(); } \
    u32x4 f0, f1; pack16(xres, f0, f1); \
    STAMP((layer) * 16 + 13); \
    if ((layer) == 3) { \
      if (act) { u16* p = out + (32 * blk + r) * DM + 32 * tl + 4 * hh; \
        *(u32x2*)(p) = (u32x2){f0.x, f0.y}; *(u32x2*)(p + 8) = (u32x2){f0.z, f0.w}; *(u32x2*)(p + 16) = (u32x2){f1.x, f1.y}; *(u32x2*)(p + 24) = (u32x2){f1.z, f1.w}; } \
      __builtin_amdgcn_endpgm(); \
    } \
    const int dst = XT_OFF + ((layer) + 1) * XT_L + blk * XT_BLK + g * XT_WG; \
    if (act) { STC(f0, lo, dst + w * 2048); STC(f1, lo + KB, dst + w * 2048); } \
    float mean, m2; unpack16(f0, f1, xres); tile_stats(xres, mean, m2); \
    if (act && hh == 0) __builtin_amdgcn_raw_buffer_store_b64((u32x2){__float_as_uint(mean), __float_as_uint(m2)}, rs, r * 8, dst + 8192 + w * 256, 16); \
    STAMP((layer) * 16 + 14); \
    PUBLISH(FX_OFF + (((layer) + 1) * 256 + blk * 4 + g) * 128); \
    STAMP((layer) * 16 + 15); }

#pragma unroll 1
  for (int pair = 0;; ++pair) {
    u32x4 WZ[32], Wv[28], Wk[28], WqB[28];
    {
      const int layer = 2 * pair;
      IDX();
      const int wbB = WB_OFF + head * 3 * 28 * KB;
      float mu, rstd;
      X_STATS_LD();
      const int G = lane >> 4, cc = lane & 15;
      if (g < 2) {
        const int xo = L_X + (G >> 1) * KB + (16 * th + cc + 32 * (G & 1)) * 16;
        u32x4 xf[14];
        UNR for (int k = 0; k < 14; ++k) xf[k] = LDS128(xo + (k >> 2) * XT_WG + (k & 3) * 2048);
        X_STATS(mu, rstd);
        f32x4 a = {0.f, 0.f, 0.f, 0.f};
        const int kvdst = KVA_OFF + pair * KVA_L + blk * 4 * KB;
        if (g == 0) {
          const float rs_ = *(LAS float*)(lds + L_MS + w * 256 + (16 * th + cc) * 4), nm_ = *(LAS float*)(lds + L_MS + w * 256 + 128 + (16 * th + cc) * 4);
          UNR for (int k = 0; k < 14; ++k) a = mfma16(W16[k], xf[k], a);
          const bool pad = dh == 1 && G == 3;
          float n2 = 0.f;
          UNR for (int e = 0; e < 4; ++e) { a[e] = pad ? 0.f : fmaf(a[e], rs_, nm_ * cskv[e]); n2 = fmaf(a[e], a[e], n2); }
          a[0] = pad ? 1.f : a[0];
          __builtin_amdgcn_raw_buffer_store_b64((u32x2){pk(a[0], a[1]), pk(a[2], a[3])}, rs, dh * KB + (16 * th + cc + 32 * (G & 1)) * 16 + 8 * (G >> 1), kvdst, 16);
          const unsigned nm = umax16(__float_as_uint(row_sum(half_sum(n2))));
          if (lane == 0) __builtin_amdgcn_raw_buffer_store_b32(nm, rs, 0, KM_OFF + (pair * 64 + blk) * 16 + w * 4, 16);
        } else {
          const f32x4 rs_ = *(LAS f32x4*)(lds + L_MS + w * 256 + (16 * th + 4 * G) * 4), nm_ = *(LAS f32x4*)(lds + L_MS + w * 256 + 128 + (16 * th + 4 * G) * 4);
          UNR for (int k = 0; k < 14; ++k) a = mfma16(xf[k], W16[k], a);
          UNR for (int e = 0; e < 4; ++e) a[e] = fmaf(a[e], rs_[e], nm_[e] * cskv.x);
          u32x2 o2 = {pk(a[0], a[1]), pk(a[2], a[3])};
          if (dh == 1 && cc >= 12) { o2.x = cc == 12 ? 0x3f803f80u : 0u; o2.y = o2.x; }
          __builtin_amdgcn_raw_buffer_store_b64(o2, rs, 2 * KB + th * KB + (16 * dh + cc + 32 * (G & 1)) * 16 + 8 * (G >> 1), kvdst, 16);
        }
        if (pair == 0) {
          const int to = L_RK + (16 * (G >> 1) + 4 * (G & 1) + (cc >> 2)) * 56 + dh * 32 + (cc & 3) * 8;
          UNR for (int k = 0; k < 14; ++k) { const u32x2 a_ = TRRD(to + k * 32 * 56), c_ = TRRD(to + k * 32 * 56 + 8 * 56); W16[k] = (u32x4){a_.x, a_.y, c_.x, c_.y}; } }
      } else {
        X_STATS(mu, rstd);
      }
      STAMP(layer * 16 + 3);
      { f32x16 a = {}; u32x4 xf[28], wf[28];
        const int to = L_RQ + ((pair ? 4 : 8) * hh + (cc >> 2)) * 224 + w * 56 + (G & 1) * 32 + (cc & 3) * 8, to2 = to + (pair ? 8 : 4) * 224;
#define WQF(k) { const u32x2 a_ = TRRD(to + (k) * 16 * 224), c_ = TRRD(to2 + (k) * 16 * 224); wf[k] = (u32x4){a_.x, a_.y, c_.x, c_.y}; }
        UNR for (int k = 0; k < 4; ++k) { WQF(k) xf[k] = XFRAG(k); }
        SBAR();
        UNR for (int k = 0; k < 28; ++k) { if (k + 4 < 28) { WQF(k + 4) xf[k + 4] = XFRAG(k + 4); }
          a = mfma32(wf[k], xf[k], a); SBAR();
          if (k == 13 && (g < 2 || pair == 0)) { asm volatile("s_waitcnt vmcnt(0) lgkmcnt(0)\n\ts_barrier" ::: "memory");
            if (tid < 8) __hip_atomic_store(flags + (FA_OFF + (pair * 8 + tid) * 2 * KB) / 8 + (g >> 1) * 128 + blk * 2 + (g & 1), tag, RLX);
            SBAR(); } }
        float n2; EPI_ALPHA(a, csq, mu, rstd * QSCALE, n2);
        u32x4 f0, f1; pack16(a, f0, f1);
        if (hh) f1.z = bf_ceil(__builtin_amdgcn_sqrtf(n2) * 1.02f);
        LDS128(L_Q + w * 2 * KB + lo) = f0; LDS128(L_Q + w * 2 * KB + KB + lo) = f1;
        if (pair == 0 && g < 2) { const int d_ = WKV_OFF + ((blk * 2 + g) * 4 + w) * 14 * KB; UNR for (int k = 0; k < 14; ++k) STC(W16[k], lo + (k & 3) * KB, d_ + (k >> 2) * 4 * KB); }
      }
      STAMP(layer * 16 + 4);
      if (w < 2 || pair == 0) { poll(flags + (FA_OFF + (pair * 8 + (b & 7)) * 2 * KB) / 8 + tid, tag); AFTER_POLL(); }
      STAMP(layer * 16 + 5);
      __syncthreads();
      STAMP(layer * 16 + 6);
      int base = KVA_OFF + pair * KVA_L + w * 64 * KB; OPQ(base);
      int woz = WOA_OFF + tlc * 32 * KB; OPQ(woz);
      int wvb = wbB + 56 * KB, wkb = wbB + 28 * KB; OPQ(wvb); OPQ(wkb);
      u32x4 q[4][2];
      const f32x4 km = __builtin_bit_cast(f32x4, LDC(lo, KM_OFF + pair * KB));
      if (b == 0 && tid == 0 && pair == 0) __hip_atomic_store(flags + EP_OFF / 8, tag, RLX);
      UNR for (int h = 0; h < 4; ++h) { const int qo = L_Q + ((w + 1 + h) & 3) * 2 * KB + lo; q[h][0] = LDS128(qo); q[h][1] = LDS128(qo + KB); }
#pragma unroll 1
      for (int attempt = 0; attempt < 2; ++attempt) {
        u32x4 Kt[16][2], Vt[16][2];
#define LDK(t) { Kt[t][0] = LDC(lo, base + (t) * 4 * KB); Kt[t][1] = LDC(lo + KB, base + (t) * 4 * KB); }
#define LDV(t) { Vt[t][0] = LDC(lo + 2 * KB, base + (t) * 4 * KB); Vt[t][1] = LDC(lo + 3 * KB, base + (t) * 4 * KB); }
        LDK(0) LDV(0) LDK(1) LDV(1) LDK(2)
        SBAR();
        if (attempt == 0) {
          const float kmax = __builtin_amdgcn_sqrtf(__uint_as_float(umax64(umax(__float_as_uint(km.x + km.y), __float_as_uint(km.z + km.w)))));
          UNR for (int h = 0; h < 4; ++h) { const unsigned m = bf_ceil(bflo(q[h][1].z) * kmax) ^ 0x8000u; q[h][1].z = hh ? m : q[h][1].z; }
        }
        float z = 0.f; PIN(z); const unsigned zu = __float_as_uint(z);
        f32x16 oo[4] = {z, z, z, z};
        f32x16 sA, sB; u32x4 pA0 = zu, pA1 = zu, pB0 = zu, pB1 = zu; f32x4 ea, eb = z;
        PIN(q[0][1]); PIN(q[1][1]); PIN(q[2][1]); PIN(q[3][1]); PIN(pA0); PIN(pA1); PIN(pB0); PIN(pB1); PIN(eb);
        asm volatile("s_nop 1" : "+a"(oo[0]), "+a"(oo[1]), "+a"(oo[2]), "+a"(oo[3]));
        SBAR();
        MS0(sA, Kt[0][0], q[0][0]); MS1(sA, Kt[0][1], q[0][1]);
        NOP16V(sA); SBAR();
#define WPF(n) { if ((n) < 32) WZ[(n) & 31] = LDW(woz, (n)); else if ((n) < 60) Wv[((n) - 32) % 28] = LDW(wvb, (n) - 32); }
#define PUTO(i) { asm volatile("" : "+a"(oo[i])); const int s_ = L_C + (((w + 1 + (i)) & 3) * 3 + (i)) * 4 * KB + lo; UNR for (int e = 0; e < 4; ++e) \
          *(LAS f32x4*)(lds + s_ + e * KB) = (f32x4){oo[i][4 * e], oo[i][4 * e + 1], oo[i][4 * e + 2], oo[i][4 * e + 3]}; }
        UNR for (int t = 0; t < 16; ++t) {
          const int tp = t > 0 ? t - 1 : 0, tn = t < 15 ? t + 1 : 15;
          STEP(sA, sB, Kt[t][0], Kt[t][1], q[1][0], q[1][1], oo[3], Vt[tp][0], Vt[tp][1], pB0, pB1, pA0, pA1, , , WPF(4 * t), WPF(4 * t + 1));
          STEP(sB, sA, Kt[t][0], Kt[t][1], q[2][0], q[2][1], oo[0], Vt[t][0], Vt[t][1], pA0, pA1, pB0, pB1, , ,
               if (t + 2 < 16) Vt[(t + 2) & 15][0] = LDC(lo + 2 * KB, base + (t + 2) * 4 * KB), if (t + 2 < 16) Vt[(t + 2) & 15][1] = LDC(lo + 3 * KB, base + (t + 2) * 4 * KB));
          STEP(sA, sB, Kt[t][0], Kt[t][1], q[3][0], q[3][1], oo[1], Vt[t][0], Vt[t][1], pB0, pB1, pA0, pA1, , , WPF(4 * t + 2), WPF(4 * t + 3) if (t == 15) PUTO(0));
          STEP(sB, sA, Kt[tn][0], Kt[tn][1], q[0][0], q[0][1], oo[2], Vt[t][0], Vt[t][1], pA0, pA1, pB0, pB1, , ,
               if (t + 3 < 16) Kt[(t + 3) & 15][0] = LDC(lo, base + (t + 3) * 4 * KB), if (t + 3 < 16) Kt[(t + 3) & 15][1] = LDC(lo + KB, base + (t + 3) * 4 * KB); if (t == 15) PUTO(1));
        }
        C2(pB1, 2, eb) asm volatile("s_nop 1");
        MO(oo[3], Vt[15][0], pB0); MO(oo[3], Vt[15][1], pB1);
        asm volatile("s_nop 3" : "+a"(oo[2])); SBAR();
        PUTO(2)
        STAMP(layer * 16 + 7);
        UNR for (int k = 0; k < 8; ++k) Wk[k] = LDW(wkb, k);
        NOP16A(oo[3]); SBAR();
        f32x16 o = oo[3];
        __syncthreads();
        UNR for (int s = 0; s < 3; ++s)
          UNR for (int e = 0; e < 4; ++e) { const f32x4 t = *(LAS f32x4*)(lds + L_C + (w * 3 + s) * 4 * KB + e * KB + lo);
            o[4 * e] += t.x; o[4 * e + 1] += t.y; o[4 * e + 2] += t.z; o[4 * e + 3] += t.w; }
        const float l = half_hi(o[12]);
        O_STORE(layer, o, l);
        const bool bad = !__all(l >= L_MIN);
        if (lane == 0) *(LAS unsigned*)(lds + L_MISC + w * 4) = bad ? 1u : 0u;
        STAMP(layer * 16 + 8);
        DRAIN();
        __syncthreads();
        const u32x4 bw = *(LAS u32x4*)(lds + L_MISC);
        if (attempt == 1 || __builtin_amdgcn_readfirstlane(bw.x | bw.y | bw.z | bw.w) == 0u) break;
        float mx[4] = {-INFINITY, -INFINITY, -INFINITY, -INFINITY};
        UNR for (int h = 0; h < 4; ++h) q[h][1].z = hh ? 0u : q[h][1].z;
#pragma unroll 1
        for (int t = 0; t < 16; ++t) {
          const u32x4 k0 = LDC(lo, base + t * 4 * KB), k1 = LDC(lo + KB, base + t * 4 * KB);
          UNR for (int h = 0; h < 4; ++h) { f32x16 s = {}; s = mfma32(k0, q[h][0], s); s = mfma32(k1, q[h][1], s);
            UNR for (int e = 0; e < 16; ++e) mx[h] = fmaxf(mx[h], s[e]); }
        }
        UNR for (int h = 0; h < 4; ++h) { const float m = half_max(mx[h]); if (hh == 0) *(LAS float*)(lds + L_MISC + 1024 + (w * 4 + ((w + 1 + h) & 3)) * 128 + r * 4) = m; }
        __syncthreads();
        UNR for (int h = 0; h < 4; ++h) { float m = -INFINITY;
          UNR for (int s = 0; s < 4; ++s) m = fmaxf(m, *(LAS float*)(lds + L_MISC + 1024 + (s * 4 + ((w + 1 + h) & 3)) * 128 + r * 4));
          q[h][1].z = hh ? (bf_ceil(m) ^ 0x8000u) : q[h][1].z; }
      }
      if (tid == 0) __hip_atomic_store(flags + (FO_OFF + (layer * 256 + blk * 4 + g) * 128) / 8, tag, RLX);
      Z_PHASE(layer,
              UNR for (int k = 8; k < 18; ++k) Wk[k] = LDW(wkb, k),
              if ((k & 1) && k < 20) Wk[18 + (k >> 1)] = LDW(wkb, 18 + (k >> 1)); else if ((k & 1) && k < 24) WqB[(k >> 1) - 10] = LDW(wbB, (k >> 1) - 10));
    }
    {
      const int layer = 2 * pair + 1;
      IDX();
      const int wbB = WB_OFF + head * 3 * 28 * KB;
      UNR for (int k = 2; k < 10; ++k) WqB[k] = LDW(wbB, k);
      int woz = WOB_OFF + tlc * 32 * KB; OPQ(woz);
      f32x16 csqB, cskB; LDCS(csqB, head * 28); LDCS(cskB, 448 + head * 28);
      const float csv = __uint_as_float(__builtin_amdgcn_raw_buffer_load_b32(rs, r * 4, CS_OFF + (896 + head * 28) * 4, 16));
      SBAR();
      X_GATHER(layer);
      float mu, rstd, qn;
      u32x4 K[9][2], V[9][2], q0, q1;
      unsigned kown;
      {
        const int dst = KVB_OFF + pair * KVB_L + blk * KVB_BLK + head * 4 * KB;
        f32x16 av = {}, ak = {}, aq = {}; u32x4 xf[84]; f32x4 rs_[4], nm_[4];
        X_STATS_LD();
        UNR for (int k = 0; k < 6; ++k) xf[k] = XFRAG(k);
        SBAR();
        sfor<0, 84>([&](auto kc) __attribute__((always_inline)) { constexpr int k = decltype(kc)::value;
          if constexpr (k + 6 < 84) xf[k + 6] = XFRAG((k + 6) % 28);
          if constexpr (k % 5 == 1 || k % 5 == 3) { constexpr int i = (k / 5) * 2 + (k % 5 == 3);
            if constexpr (i < 18) WqB[10 + i] = LDW(wbB, 10 + i); else if constexpr (i < 23) WZ[i - 18] = LDW(woz, i - 18); else if constexpr (k > 72) WZ[i - 24] = LDW(woz, i - 24); }
          if constexpr (k < 28) av = mfma32(xf[k], Wv[k], av); else if constexpr (k < 56) ak = mfma32(Wk[k - 28], xf[k], ak); else aq = mfma32(WqB[k - 56], xf[k], aq);
          if constexpr (k == 1) X_STATS(mu, rstd);
          if constexpr (k == 8) { UNR for (int i = 0; i < 4; ++i) { rs_[i] = *(LAS f32x4*)(lds + L_MS + w * 256 + (8 * i + 4 * hh) * 4); nm_[i] = *(LAS f32x4*)(lds + L_MS + w * 256 + 128 + (8 * i + 4 * hh) * 4); } }
          if constexpr (k == 30) asm volatile("" : "+a"(av));
          if constexpr (k >= 30 && k < 34) { constexpr int i = k - 30;
            unsigned p0 = pk(fmaf(av[4 * i], rs_[i].x, nm_[i].x * csv), fmaf(av[4 * i + 1], rs_[i].y, nm_[i].y * csv));
            unsigned p1 = pk(fmaf(av[4 * i + 2], rs_[i].z, nm_[i].z * csv), fmaf(av[4 * i + 3], rs_[i].w, nm_[i].w * csv));
            const unsigned pad = r == 28 ? 0x3f803f80u : 0u;
            V[8][i >> 1][2 * (i & 1)] = r < 28 ? p0 : pad; V[8][i >> 1][2 * (i & 1) + 1] = r < 28 ? p1 : pad; }
          if constexpr (k == 34) { STC(V[8][0], lo + 2 * KB, dst); STC(V[8][1], lo + 3 * KB, dst); }
          if constexpr (k == 57) { asm volatile("" : "+a"(ak));
            float kn2; EPI_ALPHA(ak, cskB, mu, rstd, kn2);
            if (hh) ak[12] = 1.f;
            pack16(ak, K[8][0], K[8][1]);
            STC(K[8][0], lo, dst); STC(K[8][1], lo + KB, dst);
            kown = umax32(__float_as_uint(kn2));
            if (lane == 0) __builtin_amdgcn_raw_buffer_store_b32(kown, rs, 0, KMB_OFF + ((pair * 64 + blk) * 16 + head) * 4, 16);
            STAMP(layer * 16 + 3); }
          SBAR();
          if constexpr (k == 72) { DRAIN(); __builtin_amdgcn_s_barrier();
            if (tid == 0) __hip_atomic_store(flags + (FB_OFF + (pair * 256 + blk * 4 + g) * 128) / 8, tag, RLX);
            SBAR(); } });
        float n2; EPI_ALPHA(aq, csqB, mu, rstd * QSCALE, n2);
        pack16(aq, q0, q1);
        qn = __builtin_amdgcn_sqrtf(n2) * 1.02f;
      }
      STAMP(layer * 16 + 4);
      const bool pv = lane < 8 && blk - 8 + lane >= 0;
      if (pv) poll(flags + (FB_OFF + (pair * 256 + (blk - 8 + lane) * 4 + g) * 128) / 8, tag);
      AFTER_POLL();
      STAMP(layer * 16 + 5);
      unsigned kmx = __builtin_amdgcn_raw_buffer_load_b32(rs, pv ? (blk - 8 + lane) * 64 : blk * 64, KMB_OFF + (pair * 64 * 16 + head) * 4, 16);
      const int kvb = KVB_OFF + pair * KVB_L + (blk - 8) * KVB_BLK + head * 4 * KB;
#define SRCB(j) (blk - 8 + (j) < 0 ? ZERO_OFF : kvb + (j) * KVB_BLK)
#define LKB(j) { const int s_ = SRCB(j); K[j][0] = LDC(lo, s_); K[j][1] = LDC(lo + KB, s_); }
#define LVB(j) { const int s_ = SRCB(j); V[j][0] = LDC(lo + 2 * KB, s_); V[j][1] = LDC(lo + 3 * KB, s_); }
      bool first = true;
      f32x16 o; float l;
#pragma unroll 1
      for (int attempt = 0; attempt < 2; ++attempt) {
        LKB(7) LKB(6) LVB(7) LKB(5) LVB(6) LKB(4)
        SBAR();
        if (first) { kmx = __builtin_amdgcn_readfirstlane(umax16(pv ? kmx : kown));
          q1.z = hh ? (bf_ceil(qn * __builtin_amdgcn_sqrtf(__uint_as_float(kmx))) ^ 0x8000u) : q1.z; first = false; }
        STAMP(layer * 16 + 6);
        float z = 0.f; PIN(z); const unsigned zu = __float_as_uint(z);
        o = z;
        f32x16 sA, sB; u32x4 pA0 = zu, pA1 = zu, pB0 = zu, pB1 = zu; f32x4 ea, eb = z;
        PIN(q0); PIN(q1); PIN(K[8][0]); PIN(K[8][1]); PIN(V[8][0]); PIN(V[8][1]); PIN(pA0); PIN(pA1); PIN(pB0); PIN(pB1); PIN(eb); asm volatile("s_nop 1" : "+a"(o)); SBAR();
        MS0(sA, K[8][0], q0); MS1(sA, K[8][1], q1);
        NOP16V(sA); SBAR();
#define MASKT(S, keep) { UNR for (int e = 0; e < 16; ++e) { const int row = (e & 3) + 8 * (e >> 2) + 4 * hh; S[e] = (keep) ? S[e] : -INFINITY; } PIN(S); SBAR(); }
#define WZ2(k) { WZ[k] = LDW(woz, k); WZ[k + 1] = LDW(woz, k + 1); }
        MASKT(sA, row <= r)
        STEP(sA, sB, K[7][0], K[7][1], q0, q1, o, V[8][0], V[8][1], pB0, pB1, pA0, pA1, , , LVB(5), LKB(3));
        STEP(sB, sA, K[6][0], K[6][1], q0, q1, o, V[8][0], V[8][1], pA0, pA1, pB0, pB1, , , LVB(4), LKB(2));
        STEP(sA, sB, K[5][0], K[5][1], q0, q1, o, V[7][0], V[7][1], pB0, pB1, pA0, pA1, , , LVB(3), LKB(1));
        STEP(sB, sA, K[4][0], K[4][1], q0, q1, o, V[6][0], V[6][1], pA0, pA1, pB0, pB1, , , LVB(2), LKB(0));
        STEP(sA, sB, K[3][0], K[3][1], q0, q1, o, V[5][0], V[5][1], pB0, pB1, pA0, pA1, , , LVB(1), LVB(0));
        STEP(sB, sA, K[2][0], K[2][1], q0, q1, o, V[4][0], V[4][1], pA0, pA1, pB0, pB1, , , WZ2(10), );
        STEP(sA, sB, K[1][0], K[1][1], q0, q1, o, V[3][0], V[3][1], pB0, pB1, pA0, pA1, , , WZ2(12), WZ2(14));
        STEP(sB, sA, K[0][0], K[0][1], q0, q1, o, V[2][0], V[2][1], pA0, pA1, pB0, pB1, , , WZ2(16), WZ2(18));
        MASKT(sA, row > r)
        STEP(sA, sB, K[0][0], K[0][1], q0, q1, o, V[1][0], V[1][1], pB0, pB1, pA0, pA1, , , , );
        C2(pA1, 2, eb) asm volatile("s_nop 1");
        MO(o, V[0][0], pA0); MO(o, V[0][1], pA1);
        NOP16A(o); SBAR();
        l = half_hi(o[12]);
        if (attempt == 1 || __all(l >= L_MIN)) break;
        q1.z = hh ? 0u : q1.z;
        float mx = -INFINITY;
        UNR for (int j = 8; j >= 0; --j) {
          f32x16 s = {}; s = mfma32(K[j][0], q0, s); s = mfma32(K[j][1], q1, s); float m_ = -INFINITY;
          UNR for (int e = 0; e < 16; ++e) { const int row = (e & 3) + 8 * (e >> 2) + 4 * hh; float v = s[e];
            if (j == 8) v = (row <= r) ? v : -INFINITY; if (j == 0) v = (row > r) ? v : -INFINITY; m_ = fmaxf(m_, v); }
          mx = (blk - 8 + j >= 0) ? fmaxf(mx, m_) : mx; SBAR(); }
        mx = half_max(mx);
        q1.z = hh ? (bf_ceil(mx) ^ 0x8000u) : q1.z;
      }
      STAMP(layer * 16 + 7);
      O_STORE(layer, o, l);
      STAMP(layer * 16 + 8);
      PUBLISH(FO_OFF + (layer * 256 + blk * 4 + g) * 128);
      int wkv = g < 2 ? WKV_OFF + ((blk * 2 + g) * 4 + w) * 14 * KB : WS_BYTES; OPQ(wkv);
      Z_PHASE(layer,
              UNR for (int k = 20; k < 32; ++k) WZ[k] = LDW(woz, k),
              if ((k & 1) && k < 28) W16[k >> 1] = LDW(wkv, k >> 1));
      {
        const int G = lane >> 4, cc = lane & 15, to = L_RQ + (4 * hh + (cc >> 2)) * 224 + w * 56 + (G & 1) * 32 + (cc & 3) * 8, to2 = to + 8 * 224;
        u32x4 wf[28]; csq = (f32x16){}; cskv = (f32x4){0.f, 0.f, 0.f, 0.f};
        UNR for (int k = 0; k < 6; ++k) WQF(k)
        SBAR();
        UNR for (int k = 0; k < 28; ++k) { if (k + 6 < 28) WQF(k + 6) csq = mfma32(wf[k], ones, csq); SBAR(); }
        if (g == 0) { UNR for (int k = 0; k < 14; ++k) cskv = mfma16(W16[k], ones, cskv); }
        else if (g == 1) { UNR for (int k = 0; k < 14; ++k) cskv = mfma16(ones, W16[k], cskv); }
      }
      X_GATHER(layer + 1);
    }
  }
}

extern "C" void kernel_launch(void* const* d_in, const int* in_sizes, int n_in, void* d_out, int out_size, void* d_ws, size_t ws_size, hipStream_t stream) {
  (void)in_sizes; (void)n_in; (void)out_size; (void)ws_size;
  (void)hipFuncSetAttribute((const void*)main_kernel, hipFuncAttributeMaxDynamicSharedMemorySize, L_BYTES);
  main_kernel<<<256, 256, L_BYTES, stream>>>((const u16*)d_in[0], (const u16*)d_in[1], (const u16*)d_in[2], (const u16*)d_in[3], (const u16*)d_in[4],
                                             (const u16*)d_in[6], (const u16*)d_in[7], (unsigned char*)d_ws, (u16*)d_out);
}
```

```cpp
#include <hip/hip_runtime.h>
#include <hip/hip_bf16.h>

typedef float f32x16 __attribute__((ext_vector_type(16)));
typedef float f32x4 __attribute__((ext_vector_type(4)));
typedef float f32x2 __attribute__((ext_vector_type(2)));
typedef unsigned u32x4 __attribute__((ext_vector_type(4)));
typedef unsigned u32x2 __attribute__((ext_vector_type(2)));
typedef short bf16x8 __attribute__((ext_vector_type(8)));
typedef short s16x4 __attribute__((ext_vector_type(4)));
typedef __bf16 bf2 __attribute__((ext_vector_type(2)));
typedef unsigned short u16;
typedef unsigned long long u64;
#define GAS __attribute__((address_space(1)))
#define LAS __attribute__((address_space(3)))
typedef GAS u64 gu64;
#define RLX __ATOMIC_RELAXED, __HIP_MEMORY_SCOPE_AGENT
#define SBAR() __builtin_amdgcn_sched_barrier(0)
#define PIN(x) asm volatile("" : "+v"(x))
#define OPQ(x) asm volatile("" : "+s"(x))
#define DRAIN() asm volatile("s_waitcnt vmcnt(0)" ::: "memory")
#define UNR _Pragma("unroll")

constexpr int DM = 448, NB = 64, NTL = 14, KB = 1024;
constexpr int EP_OFF = 0;
constexpr int FA_OFF = 1 * KB;
constexpr int FX_OFF = FA_OFF + 2 * 8 * 2 * KB;
constexpr int FO_OFF = FX_OFF + 4 * 256 * 128;
constexpr int FB_OFF = FO_OFF + 4 * 256 * 128;
constexpr int WOA_OFF = FB_OFF + 2 * 256 * 128;
constexpr int WOB_OFF = WOA_OFF + 14 * 32 * KB;
constexpr int WB_OFF = WOB_OFF + 14 * 32 * KB;
constexpr int ZERO_OFF = WB_OFF + 16 * 3 * 28 * KB;
constexpr int CS_OFF = ZERO_OFF + 4 * KB;
constexpr int KM_OFF = CS_OFF + 6 * KB;
constexpr int KMB_OFF = KM_OFF + 2 * KB;
constexpr int XT_OFF = KMB_OFF + 8 * KB;
constexpr int XT_WG = 9 * KB, XT_BLK = 4 * XT_WG, XT_L = NB * XT_BLK;
constexpr int OT_OFF = XT_OFF + 4 * XT_L;
constexpr int OT_BLK = 32 * KB, OT_L = NB * OT_BLK;
constexpr int KVA_OFF = OT_OFF + 4 * OT_L;
constexpr int KVA_L = NB * 4 * KB;
constexpr int KVB_OFF = KVA_OFF + 2 * KVA_L;
constexpr int KVB_BLK = 64 * KB, KVB_L = NB * KVB_BLK;
constexpr int WKV_OFF = KVB_OFF + 2 * KVB_L;
constexpr int WS_BYTES = WKV_OFF + 128 * 4 * 14 * KB;
constexpr int L_X = 0;
constexpr int L_O = 0, L_C = 0;
constexpr int L_MISC = 48 * KB;
constexpr int L_Q = 52 * KB;
constexpr int L_RK = 36 * KB;
constexpr int L_RQ = L_RK + 448 * 56;
constexpr int L_MS = 159 * KB;
constexpr int L_BYTES = 160 * KB;
static_assert(L_RQ + 448 * 224 + 64 <= L_MS && L_RQ % 16 == 0, "lds");

#define STAMP(n) do {} while (0)
constexpr float QSCALE = 0.18898223650461361f * 1.4426950408889634f;
constexpr unsigned SPIN_CAP = 1u << 16;
constexpr float L_MIN = 1e-18f;

__device__ __forceinline__ unsigned pk(float a, float b) { f32x2 v = {a, b}; bf2 r = __builtin_convertvector(v, bf2); return __builtin_bit_cast(unsigned, r); }
__device__ __forceinline__ float bflo(unsigned u) { return __uint_as_float(u << 16); }
__device__ __forceinline__ float bfhi(unsigned u) { return __uint_as_float(u & 0xffff0000u); }
__device__ __forceinline__ f32x16 mfma32(u32x4 a, u32x4 b, f32x16 c) {
  return __builtin_amdgcn_mfma_f32_32x32x16_bf16(__builtin_bit_cast(bf16x8, a), __builtin_bit_cast(bf16x8, b), c, 0, 0, 0); }
__device__ __forceinline__ f32x4 mfma16(u32x4 a, u32x4 b, f32x4 c) {
  return __builtin_amdgcn_mfma_f32_16x16x32_bf16(__builtin_bit_cast(bf16x8, a), __builtin_bit_cast(bf16x8, b), c, 0, 0, 0); }
__device__ __forceinline__ float half_sum(float v) { auto r = __builtin_amdgcn_permlane32_swap(__float_as_uint(v), __float_as_uint(v), false, false); return __uint_as_float(r[0]) + __uint_as_float(r[1]); }
__device__ __forceinline__ float half_max(float v) { auto r = __builtin_amdgcn_permlane32_swap(__float_as_uint(v), __float_as_uint(v), false, false); return fmaxf(__uint_as_float(r[0]), __uint_as_float(r[1])); }
__device__ __forceinline__ float half_hi(float v) { auto r = __builtin_amdgcn_permlane32_swap(__float_as_uint(v), __float_as_uint(v), false, false); return __uint_as_float(r[1]); }
__device__ __forceinline__ float row_sum(float v) { auto r = __builtin_amdgcn_permlane16_swap(__float_as_uint(v), __float_as_uint(v), false, false); return __uint_as_float(r[0]) + __uint_as_float(r[1]); }
template <int C> __device__ __forceinline__ float dppf(float v) { return __uint_as_float(__builtin_amdgcn_update_dpp(0u, __float_as_uint(v), C, 0xf, 0xf, true)); }
__device__ __forceinline__ float sum8(float v) { v += dppf<0xb1>(v); v += dppf<0x4e>(v); return v + dppf<0x141>(v); }
template <int N> __device__ __forceinline__ unsigned ror(unsigned v) { return __builtin_amdgcn_update_dpp(0u, v, 0x120 + N, 0xf, 0xf, true); }
__device__ __forceinline__ unsigned umax(unsigned a, unsigned b) { return a > b ? a : b; }
__device__ __forceinline__ unsigned umax16(unsigned v) { v = umax(v, ror<1>(v)); v = umax(v, ror<2>(v)); v = umax(v, ror<4>(v)); return umax(v, ror<8>(v)); }
__device__ __forceinline__ unsigned umax32(unsigned v) { v = umax16(v); auto r = __builtin_amdgcn_permlane16_swap(v, v, false, false); return umax(r[0], r[1]); }
__device__ __forceinline__ unsigned umax64(unsigned v) { v = umax32(v); auto r = __builtin_amdgcn_permlane32_swap(v, v, false, false); return umax(r[0], r[1]); }
__device__ __forceinline__ unsigned bf_ceil(float v) { unsigned u = __float_as_uint(v); unsigned t = u >> 16; return (v > 0.f && (u & 0xffffu)) ? t + 1u : t; }
__device__ __forceinline__ void pack16(const f32x16& v, u32x4& f0, u32x4& f1) {
  f0 = (u32x4){pk(v[0], v[1]), pk(v[2], v[3]), pk(v[4], v[5]), pk(v[6], v[7])};
  f1 = (u32x4){pk(v[8], v[9]), pk(v[10], v[11]), pk(v[12], v[13]), pk(v[14], v[15])}; }
__device__ __forceinline__ void unpack16(const u32x4 f0, const u32x4 f1, f32x16& v) {
  UNR for (int i = 0; i < 4; ++i) { v[2 * i] = bflo(f0[i]); v[2 * i + 1] = bfhi(f0[i]); v[8 + 2 * i] = bflo(f1[i]); v[9 + 2 * i] = bfhi(f1[i]); } }
__device__ __forceinline__ void tile_stats(const f32x16& v, float& mean, float& m2) {
  const float s = (((v[0] + v[1]) + (v[2] + v[3])) + ((v[4] + v[5]) + (v[6] + v[7]))) + (((v[8] + v[9]) + (v[10] + v[11])) + ((v[12] + v[13]) + (v[14] + v[15])));
  mean = half_sum(s) * (1.f / 32.f);
  float q0 = 0.f, q1 = 0.f;
  UNR for (int i = 0; i < 8; ++i) { const float d0 = v[i] - mean, d1 = v[8 + i] - mean; q0 = fmaf(d0, d0, q0); q1 = fmaf(d1, d1, q1); }
  m2 = half_sum(q0 + q1);
}
template <int V> struct IC { static constexpr int value = V; };
template <int I, int N, class F> __device__ __forceinline__ void sfor(F&& f) { if constexpr (I < N) { f(IC<I>{}); sfor<I + 1, N>(f); } }
__device__ __forceinline__ u32x4 mkdesc(const void* p, unsigned bytes) { return (u32x4){(unsigned)(size_t)p, (unsigned)((size_t)p >> 32) & 0xffffu, bytes, 0x00020000u}; }

#define LDC(voff, soff) __builtin_amdgcn_raw_buffer_load_b128(rs, (voff), (soff), 16)
#define STC(v, voff, soff) __builtin_amdgcn_raw_buffer_store_b128((v), rs, (voff), (soff), 16)
#define LDS128(off) (*(LAS u32x4*)(lds + (off)))
#define LDW(base, k) LDC(lo + ((k) & 3) * KB, (base) + ((k) >> 2) * 4 * KB)
#define MS0(S, A, B) asm volatile("v_mfma_f32_32x32x16_bf16 %0, %1, %2, 0" : "=&v"(S) : "v"(A), "v"(B))
#define MS1(S, A, B) asm volatile("v_mfma_f32_32x32x16_bf16 %0, %1, %2, %0" : "+v"(S) : "v"(A), "v"(B))
#define MO(O, A, B) asm volatile("v_mfma_f32_32x32x16_bf16 %0, %1, %2, %0" : "+a"(O) : "v"(A), "v"(B))
#define EX(x) __builtin_amdgcn_exp2f(x)
#define E4(E, S, i) { E[0] = EX(S[i]); E[1] = EX(S[i + 1]); E[2] = EX(S[i + 2]); E[3] = EX(S[i + 3]); PIN(E); SBAR(); }
#define C2(P, j, E) { P[j] = pk(E[0], E[1]); P[j + 1] = pk(E[2], E[3]); PIN(P); SBAR(); }
#define STEP(SC, SN, KN0, KN1, QN0, QN1, OP, VP0, VP1, PP0, PP1, PC0, PC1, W0, W2, LDA, LDB) \
  W0; MS0(SN, KN0, QN0); SBAR(); E4(ea, SC, 0) C2(PP1, 2, eb) \
  MS1(SN, KN1, QN1); LDA; SBAR(); E4(eb, SC, 4) C2(PC0, 0, ea) \
  W2; MO(OP, VP0, PP0); SBAR(); E4(ea, SC, 8) C2(PC0, 2, eb) \
  MO(OP, VP1, PP1); LDB; SBAR(); E4(eb, SC, 12) C2(PC1, 0, ea)
#define NOP16V(x) asm volatile("s_nop 7\n\ts_nop 7" : "+v"(x))
#define NOP16A(x) asm volatile("s_nop 7\n\ts_nop 7" : "+a"(x))
#define VMW(n) asm volatile("s_waitcnt vmcnt(" #n ")")
#define ALD(dst, soff, imm) asm volatile("buffer_load_dwordx4 %0, %1, %2, %3 offen offset:" #imm " sc1" : "+v"(dst) : "v"(lo), "s"(desc), "s"(soff))
#define ALD0(dst, soff, imm) asm volatile("buffer_load_dwordx4 %0, %1, %2, %3 offen offset:" #imm " sc1" : "=v"(dst) : "v"(lo), "s"(desc), "s"(soff))
#define ALDA(dst, soff, imm) asm volatile("buffer_load_dwordx4 %0, %1, %2, %3 offen offset:%4 sc1" : "=a"(dst) : "v"(lo), "s"(desc), "s"(soff), "i"(imm))
#define LDWA(dst, base, k) { const int so_ = (base) + ((k) >> 2) * 4 * KB; ALDA(dst, so_, ((k) & 3) * KB); }
#define ADMA(ldsaddr, voff, dsc, soff) asm volatile("s_mov_b32 m0, %0\n\ts_nop 0\n\tbuffer_load_dwordx4 %1, %2, %3 offen lds" :: "s"(ldsaddr), "v"(voff), "s"(dsc), "s"(soff) : "memory")
#define DMAW(base, k) { const unsigned la_ = L_W + w * L_WW + (k) * KB; const int so_ = (base) + (k) * KB; ADMA(la_, lo, desc, so_); }
#define TRRD(off) __builtin_bit_cast(u32x2, __builtin_amdgcn_ds_read_tr16_b64_v4i16((LAS s16x4*)(lds + (off))))

__device__ __forceinline__ void poll(gu64* p, u64 tag) {
  unsigned spins = 0;
  while (__hip_atomic_load(p, RLX) != tag) { if (++spins > SPIN_CAP) break; }
}
#define AFTER_POLL() __builtin_amdgcn_fence(__ATOMIC_ACQUIRE, "wavefront")

__global__ __launch_bounds__(256, 1) void main_kernel(const u16* __restrict__ x, const u16* __restrict__ wq, const u16* __restrict__ wk, const u16* __restrict__ wv,
                                                      const u16* __restrict__ woa, const u16* __restrict__ wb, const u16* __restrict__ wob,
                                                      unsigned char* __restrict__ ws, u16* __restrict__ out) {
  extern __shared__ __attribute__((aligned(16))) unsigned char smem[];
  const __amdgpu_buffer_rsrc_t rs = __builtin_amdgcn_make_buffer_rsrc((void*)ws, 0, WS_BYTES, 0x00020000);
  const u32x4 desc = mkdesc(ws, WS_BYTES);
  LAS unsigned char* lds = (LAS unsigned char*)smem;
  gu64* flags = (gu64*)ws;
  int tid_ = threadIdx.x, b_ = blockIdx.x, w_ = __builtin_amdgcn_readfirstlane(threadIdx.x >> 6);
#define IDX() asm volatile("" : "+v"(tid_)); asm volatile("" : "+s"(b_), "+s"(w_)); \
  const int tid = tid_, b = b_, w = w_, lane = tid & 63, r = lane & 31, hh = lane >> 5, lo = lane * 16; \
  const int blk = (b & 7) * 8 + (b >> 5), g = (b >> 3) & 3, head = 4 * g + w, tl = head; const bool act = tl < NTL; const int tlc = act ? tl : 0; \
  const int dh = w & 1, th = w >> 1; (void)r; (void)hh; (void)dh; (void)th; (void)tlc; (void)blk; (void)lo
  u64 pc_; unsigned len_;
  asm volatile("s_getpc_b64 %0\n.Lcode_here:\n\ts_mov_b32 %1, .Lcode_last-.Lcode_here" : "=s"(pc_), "=s"(len_));
#define CODEPF(ldsoff) { const u32x4 dc = mkdesc((const void*)pc_, len_); const int vo = ((b >> 3) * 4 + w) * 512 + (lane & 3) * 128; const unsigned la = (ldsoff) + w * 256; \
    asm volatile("s_mov_b32 m0, %0\n\ts_nop 0\n\tbuffer_load_dword %1, %2, 0 offen lds" :: "s"(la), "v"(vo), "s"(dc) : "memory"); }
  STAMP(64);

  f32x16 xres;
  f32x4 cskv;
  u32x4 W16[14];
  u64 tag;
  const u32x4 ones = {0x3f803f80u, 0x3f803f80u, 0x3f803f80u, 0x3f803f80u};
  {
    IDX();
    u64 ep; asm volatile("global_load_dwordx2 %0, %1, %2 sc1" : "=v"(ep) : "v"(0), "s"(ws));
    u32x4 xr[7];
    { const u32x4 dx = mkdesc(x + 32 * blk * DM, 32 * 896); const int vo = (8 * w + (lane >> 3)) * 896 + (lane & 7) * 16;
      UNR for (int f = 0; f < 7; ++f) asm volatile("buffer_load_dwordx4 %0, %1, %2, 0 offen offset:%3" : "=v"(xr[f]) : "v"(vo), "s"(dx), "i"(f * 128)); }
    CODEPF(L_X + 31 * KB);
    const int n = blk * 2 + (g & 1), nw = n - 84;
    const int c3 = (n * 37) >> 10, ks = n - 28 * c3;
    u32x4 cl[4];
    if (g < 2 || n >= 84) { const u32x4 dk = g < 2 ? mkdesc(g ? wv : wk, 448 * 56) : mkdesc((nw & 16 ? wob : woa) + (nw & 15) * 28 * 448, n < 116 ? 448 * 56 : 0);
      UNR for (int k = 0; k < 7; ++k) if (4 * k + w < 25) { const unsigned la = L_RK + (4 * k + w) * KB; const int so = (4 * k + w) * KB;
        if (4 * k + w < 24 || lane < 32) ADMA(la, lo, dk, so); } }
    else { const u32x4 dk = mkdesc(wb + 16 * ks * 1344 + 448 * c3, 16 * 2688);
      UNR for (int k = 0; k < 4; ++k) if (4 * k + w < 14) { const int c = tid + 256 * k, row = (c * 1171) >> 16; const unsigned la = L_RK + (4 * k + w) * KB;
        ADMA(la, row * 2688 + (c - row * 56) * 16, dk, 0); }
      const u32x4 db = mkdesc(wb, 448 * 2688); const int vo = (tid >> 1) * 2688 + (tid & 1) * 16;
      UNR for (int j = 0; j < 4; ++j) { const int so = j * 128 * 2688 + n * 32; asm volatile("buffer_load_dwordx4 %0, %1, %2, %3 offen" : "=v"(cl[j]) : "v"(vo), "s"(db), "s"(so)); } }
    const u32x4 dq = mkdesc(wq, 448 * 448 * 2); int vq[7];
    UNR for (int k = 0; k < 7; ++k) { const int c = tid + 256 * k, row = (c * 4682) >> 16; vq[k] = row * 896 + (c - row * 14) * 16 + g * 224; }
#define WQDMA(u0) { const int u = ((u0) + blk) & 3; UNR for (int k = 0; k < 7; ++k) { const int i = 4 * (7 * u + k) + w; \
      const unsigned la = (k < 3 || i < 98) ? L_RQ + i * KB : L_X + 32 * KB; const int so = u * 128 * 896; ADMA(la, vq[k], dq, so); } }
    WQDMA(0) WQDMA(1)
    STAMP(67);
    asm volatile("s_waitcnt vmcnt(14)" : "+v"(xr[0]), "+v"(xr[1]), "+v"(xr[2]), "+v"(xr[3]), "+v"(xr[4]), "+v"(xr[5]), "+v"(xr[6]), "+v"(cl[0]), "+v"(cl[1]), "+v"(cl[2]), "+v"(cl[3]), "+v"(ep) :: "memory");
    { f32x2 v[28]; f32x2 s = {0.f, 0.f};
      UNR for (int f = 0; f < 7; ++f) UNR for (int c = 0; c < 4; ++c) { v[4 * f + c] = (f32x2){bflo(xr[f][c]), bfhi(xr[f][c])}; s += v[4 * f + c]; }
      const float mu = sum8(s.x + s.y) * (1.f / 448.f);
      s = (f32x2){0.f, 0.f};
      UNR for (int i = 0; i < 28; ++i) { v[i] -= mu; s += v[i] * v[i]; }
      const float rstd = __builtin_amdgcn_rsqf(sum8(s.x + s.y) * (1.f / 448.f) + 1e-5f);
      const int c = lane & 7;
      UNR for (int f = 0; f < 7; ++f) {
        u32x4 o; UNR for (int e = 0; e < 4; ++e) { const f32x2 t = v[4 * f + e] * rstd; o[e] = pk(t.x, t.y); }
        LDS128(L_X + (f >> 1) * XT_WG + (f & 1) * 4 * KB + (c >> 1) * KB + (8 * w + (lane >> 3) + 32 * (c & 1)) * 16) = o; }
      const float one = 32.f * (1.f - 1e-5f);
      *(LAS f32x4*)(lds + L_X + w * XT_WG + 8192 + lo) = (f32x4){0.f, one, 0.f, one};
    }
    if (g >= 2 && n < 84) { float s[8];
      UNR for (int c = 0; c < 4; ++c) { s[2 * c] = (bflo(cl[0][c]) + bflo(cl[1][c])) + (bflo(cl[2][c]) + bflo(cl[3][c])); s[2 * c + 1] = (bfhi(cl[0][c]) + bfhi(cl[1][c])) + (bfhi(cl[2][c]) + bfhi(cl[3][c])); }
      *(LAS f32x4*)(lds + L_RK + 15 * KB + tid * 32) = (f32x4){s[0], s[1], s[2], s[3]}; *(LAS f32x4*)(lds + L_RK + 15 * KB + tid * 32 + 16) = (f32x4){s[4], s[5], s[6], s[7]}; }
    SBAR();
    WQDMA(2) WQDMA(3)
    STAMP(68);
    asm volatile("s_waitcnt lgkmcnt(0)\n\ts_barrier" ::: "memory");
    STAMP(65);
    tag = (((u64)(unsigned)__builtin_amdgcn_readfirstlane((unsigned)(ep >> 32)) << 32) | (unsigned)__builtin_amdgcn_readfirstlane((unsigned)ep)) + 1ull;
    const int q4 = (lane & 15) >> 2, p4 = lane & 3, gq = lane >> 4;
    cskv = (f32x4){0.f, 0.f, 0.f, 0.f};
    if (g < 2) { const int to = L_RK + (8 * gq + q4) * 56 + dh * 32 + p4 * 8;
      UNR for (int k = 0; k < 14; ++k) { const u32x2 a = TRRD(to + k * 32 * 56), c = TRRD(to + k * 32 * 56 + 4 * 56); W16[k] = (u32x4){a.x, a.y, c.x, c.y}; }
    } else {
      const int to = L_RK + (4 * hh + q4) * 896 + (gq & 1) * 32 + p4 * 8;
      if (n < 84) {
        UNR for (int j = 0; j < 4; ++j) { const int h = 4 * w + j; const u32x2 a = TRRD(to + h * 56), c = TRRD(to + h * 56 + 8 * 896);
          STC(((u32x4){a.x, a.y, c.x, c.y}), lo, WB_OFF + ((h * 3 + c3) * 28 + ks) * KB); }
        if (w == 0) { float s = 0.f;
          UNR for (int j = 0; j < 32; ++j) s += *(LAS float*)(lds + L_RK + 15 * KB + (j * 4 + gq) * 64 + (lane & 15) * 4);
          s = half_sum(row_sum(s));
          if (lane < 16) __builtin_amdgcn_raw_buffer_store_b32(__float_as_uint(s), rs, lane * 4, CS_OFF + n * 64, 16); }
      } else if (n < 116) {
        UNR for (int j = 0; j < 4; ++j) if (4 * j + w < NTL) { const int t = 4 * j + w;
          const u32x2 a = TRRD(to + t * 64), c = TRRD(to + t * 64 + 8 * 896), d = TRRD(to + t * 64 + 16 * 896), e = TRRD(to + t * 64 + 24 * 896);
          const int dst = (nw & 16 ? WOB_OFF : WOA_OFF) + (t * 32 + 2 * (nw & 15)) * KB;
          STC(((u32x4){a.x, a.y, c.x, c.y}), lo, dst); STC(((u32x4){d.x, d.y, hh ? 0u : e.x, hh ? 0u : e.y}), lo + KB, dst); }
      } else if (n == 116) STC(((u32x4){0u, 0u, 0u, 0u}), lo, ZERO_OFF + w * KB);
    }
    STAMP(66);
  }

#define X_GATHER(layer) { \
    STAMP((layer) * 16 + 0); \
    poll(flags + (FX_OFF + (((layer) * 256 + blk * 4 + w) * 128)) / 8, tag); AFTER_POLL(); \
    STAMP((layer) * 16 + 1); \
    int src_ = XT_OFF + (layer) * XT_L + blk * XT_BLK + w * XT_WG; OPQ(src_); \
    u32x4 t_[9]; \
    UNR for (int f = 0; f < 9; ++f) t_[f] = LDW(src_, f); \
    UNR for (int f = 0; f < 9; ++f) LDS128(L_X + w * XT_WG + f * KB + lo) = t_[f]; \
    __syncthreads(); \
    STAMP((layer) * 16 + 2); }
#define X_STATS_LD() f32x2 st_[7]; \
    UNR for (int k = 0; k < 7; ++k) st_[k] = *(LAS f32x2*)(lds + L_X + (k >> 1) * XT_WG + 8192 + (k & 1) * 512 + lane * 8)
#define X_STATS(mu, rstd) { \
    mu = half_sum(((st_[0].x + st_[1].x) + (st_[2].x + st_[3].x)) + ((st_[4].x + st_[5].x) + st_[6].x)) * (1.f / 14.f); \
    float q_ = 0.f, q2_ = 0.f; \
    UNR for (int k = 0; k < 7; ++k) { const float d = st_[k].x - mu; q_ = fmaf(d, d, q_); q2_ += st_[k].y; } \
    rstd = __builtin_amdgcn_rsqf(half_sum(fmaf(32.f, q_, q2_)) * (1.f / 448.f) + 1e-5f); \
    *(LAS float*)(lds + L_MS + w * 256 + lane * 4) = hh ? -mu * rstd : rstd; }
#define XFRAG(ks) LDS128(L_X + ((ks) >> 3) * XT_WG + ((ks) & 7) * KB + lo)
#define LDCS(cs, col) { int o_ = CS_OFF + (col) * 4; OPQ(o_); UNR for (int q4 = 0; q4 < 4; ++q4) { const f32x4 c_ = __builtin_bit_cast(f32x4, LDC(hh * 16 + q4 * 32, o_)); \
    cs[4 * q4] = c_.x; cs[4 * q4 + 1] = c_.y; cs[4 * q4 + 2] = c_.z; cs[4 * q4 + 3] = c_.w; } }
#define EPI_ALPHA(a, cs, mu, sc, n2) { const float nm_ = -(mu) * (sc); UNR for (int e = 0; e < 16; ++e) a[e] = fmaf(a[e], (sc), nm_ * cs[e]); \
    UNR for (int e = 12; e < 16; ++e) a[e] = hh ? 0.f : a[e]; \
    float n0_ = 0.f, n1_ = 0.f; UNR for (int e = 0; e < 8; ++e) { n0_ = fmaf(a[e], a[e], n0_); n1_ = fmaf(a[8 + e], a[8 + e], n1_); } n2 = half_sum(n0_ + n1_); }
#define PUBLISH(off) { DRAIN(); __syncthreads(); if (tid == 0) __hip_atomic_store(flags + (off) / 8, tag, RLX); }
#define O_STORE(layer, o, l) { const float inv_ = __builtin_amdgcn_rcpf(l); f32x16 v_; \
    UNR for (int e = 0; e < 16; ++e) v_[e] = o[e] * inv_; \
    u32x4 f0_, f1_; pack16(v_, f0_, f1_); if (hh) { f1_.z = 0u; f1_.w = 0u; } \
    const int dst_ = OT_OFF + (layer) * OT_L + blk * OT_BLK + head * 2 * KB; STC(f0_, lo, dst_); STC(f1_, lo + KB, dst_); }
#define Z_PHASE(layer, LAST, PRE, SIDE) { \
    STAMP((layer) * 16 + 9); \
    PRE; \
    SBAR(); \
    STAMP((layer) * 16 + 10); \
    poll(flags + (FO_OFF + (((layer) * 256 + blk * 4 + w) * 128)) / 8, tag); AFTER_POLL(); \
    STAMP((layer) * 16 + 11); \
    int src_ = OT_OFF + (layer) * OT_L + blk * OT_BLK + w * 8 * KB; OPQ(src_); \
    u32x4 t_[8]; \
    UNR for (int f = 0; f < 8; ++f) t_[f] = LDW(src_, f); \
    UNR for (int f = 0; f < 8; ++f) LDS128(L_O + (w * 8 + f) * KB + lo) = t_[f]; \
    __syncthreads(); \
    STAMP((layer) * 16 + 12); \
    u32x4 of_[32]; \
    UNR for (int k = 0; k < 6; ++k) of_[k] = LDS128(L_O + k * KB + lo); \
    SBAR(); \
    UNR for (int k = 0; k < 32; ++k) { if (k + 6 < 32) of_[k + 6] = LDS128(L_O + (k + 6) * KB + lo); SIDE; xres = mfma32(WZ[k], of_[k], xres); SBAR(); } \
    u32x4 f0, f1; pack16(xres, f0, f1); \
    STAMP((layer) * 16 + 13); \
    if (LAST && (layer) == 3) { \
      if (act) { u16* p = out + (32 * blk + r) * DM + 32 * tl + 4 * hh; \
        *(u32x2*)(p) = (u32x2){f0.x, f0.y}; *(u32x2*)(p + 8) = (u32x2){f0.z, f0.w}; *(u32x2*)(p + 16) = (u32x2){f1.x, f1.y}; *(u32x2*)(p + 24) = (u32x2){f1.z, f1.w}; } \
      if constexpr (LAST) asm volatile(".Lcode_last:"); \
      __builtin_amdgcn_endpgm(); \
    } \
    const int dst = XT_OFF + ((layer) + 1) * XT_L + blk * XT_BLK + g * XT_WG; \
    if (act) { STC(f0, lo, dst + w * 2048); STC(f1, lo + KB, dst + w * 2048); } \
    float mean, m2; unpack16(f0, f1, xres); tile_stats(xres, mean, m2); \
    if (act && hh == 0) __builtin_amdgcn_raw_buffer_store_b64((u32x2){__float_as_uint(mean), __float_as_uint(m2)}, rs, r * 8, dst + 8192 + w * 256, 16); \
    STAMP((layer) * 16 + 14); \
    PUBLISH(FX_OFF + (((layer) + 1) * 256 + blk * 4 + g) * 128); \
    STAMP((layer) * 16 + 15); }

#pragma unroll 1
  for (int pair = 0;; ++pair) {
    u32x4 WZ[32], Wv[28], Wk[28], WqB[28];
    {
      const int layer = 2 * pair;
      IDX();
      const int wbB = WB_OFF + head * 3 * 28 * KB;
      float mu, rstd;
      X_STATS_LD();
      const int G = lane >> 4, cc = lane & 15;
      if (g < 2) {
        const int xo = L_X + (G >> 1) * KB + (16 * th + cc + 32 * (G & 1)) * 16;
        u32x4 xf[14];
        UNR for (int k = 0; k < 14; ++k) xf[k] = LDS128(xo + (k >> 2) * XT_WG + (k & 3) * 2048);
        X_STATS(mu, rstd);
        f32x4 a = {0.f, 0.f, 0.f, 0.f};
        const int kvdst = KVA_OFF + pair * KVA_L + blk * 4 * KB;
        if (g == 0) {
          const float rs_ = *(LAS float*)(lds + L_MS + w * 256 + (16 * th + cc) * 4), nm_ = *(LAS float*)(lds + L_MS + w * 256 + 128 + (16 * th + cc) * 4);
          UNR for (int k = 0; k < 14; ++k) a = mfma16(W16[k], xf[k], a);
          const bool pad = dh == 1 && G == 3;
          float n2 = 0.f;
          UNR for (int e = 0; e < 4; ++e) { a[e] = pad ? 0.f : fmaf(a[e], rs_, nm_ * cskv[e]); n2 = fmaf(a[e], a[e], n2); }
          a[0] = pad ? 1.f : a[0];
          __builtin_amdgcn_raw_buffer_store_b64((u32x2){pk(a[0], a[1]), pk(a[2], a[3])}, rs, dh * KB + (16 * th + cc + 32 * (G & 1)) * 16 + 8 * (G >> 1), kvdst, 16);
          const unsigned nm = umax16(__float_as_uint(row_sum(half_sum(n2))));
          if (lane == 0) __builtin_amdgcn_raw_buffer_store_b32(nm, rs, 0, KM_OFF + (pair * 64 + blk) * 16 + w * 4, 16);
        } else {
          const f32x4 rs_ = *(LAS f32x4*)(lds + L_MS + w * 256 + (16 * th + 4 * G) * 4), nm_ = *(LAS f32x4*)(lds + L_MS + w * 256 + 128 + (16 * th + 4 * G) * 4);
          UNR for (int k = 0; k < 14; ++k) a = mfma16(xf[k], W16[k], a);
          UNR for (int e = 0; e < 4; ++e) a[e] = fmaf(a[e], rs_[e], nm_[e] * cskv.x);
          u32x2 o2 = {pk(a[0], a[1]), pk(a[2], a[3])};
          if (dh == 1 && cc >= 12) { o2.x = cc == 12 ? 0x3f803f80u : 0u; o2.y = o2.x; }
          __builtin_amdgcn_raw_buffer_store_b64(o2, rs, 2 * KB + th * KB + (16 * dh + cc + 32 * (G & 1)) * 16 + 8 * (G >> 1), kvdst, 16);
        }
        if (pair == 0) {
          const int to = L_RK + (16 * (G >> 1) + 4 * (G & 1) + (cc >> 2)) * 56 + dh * 32 + (cc & 3) * 8;
          UNR for (int k = 0; k < 14; ++k) { const u32x2 a_ = TRRD(to + k * 32 * 56), c_ = TRRD(to + k * 32 * 56 + 8 * 56); W16[k] = (u32x4){a_.x, a_.y, c_.x, c_.y}; } }
      } else {
        X_STATS(mu, rstd);
      }
      STAMP(layer * 16 + 3);
      { f32x16 a = {}, csq = {}; u32x4 xf[28], wf[28];
        const int to = L_RQ + ((pair ? 4 : 8) * hh + (cc >> 2)) * 224 + w * 56 + (G & 1) * 32 + (cc & 3) * 8, to2 = to + (pair ? 8 : 4) * 224;
#define WQF(k) { const u32x2 a_ = TRRD(to + (k) * 16 * 224), c_ = TRRD(to2 + (k) * 16 * 224); wf[k] = (u32x4){a_.x, a_.y, c_.x, c_.y}; }
#define PUBA() { asm volatile("s_waitcnt vmcnt(0) lgkmcnt(0)\n\ts_barrier" ::: "memory"); \
            if (tid < 8) __hip_atomic_store(flags + (FA_OFF + (pair * 8 + tid) * 2 * KB) / 8 + (g >> 1) * 128 + blk * 2 + (g & 1), tag, RLX); SBAR(); }
        u32x2 xo[4];
        if (pair == 0) { PUBA();
          UNR for (int a_ = 0; a_ < 4; ++a_) xo[a_] = *(const u32x2*)(x + (32 * blk + r) * DM + 32 * tlc + 8 * a_ + 4 * hh); }
        UNR for (int k = 0; k < 4; ++k) { WQF(k) xf[k] = XFRAG(k); }
        SBAR();
        UNR for (int k = 0; k < 28; ++k) { if (k + 4 < 28) { WQF(k + 4) xf[k + 4] = XFRAG(k + 4); }
          a = mfma32(wf[k], xf[k], a); SBAR();
          if (k == 11 && g < 2 && pair != 0) PUBA(); }
        if (pair != 0) { UNR for (int k = 0; k < 28; ++k) csq = mfma32(wf[k], ones, csq); }
        else unpack16((u32x4){xo[0].x, xo[0].y, xo[1].x, xo[1].y}, (u32x4){xo[2].x, xo[2].y, xo[3].x, xo[3].y}, xres);
        float n2; EPI_ALPHA(a, csq, mu, rstd * QSCALE, n2);
        u32x4 f0, f1; pack16(a, f0, f1);
        if (hh) f1.z = bf_ceil(__builtin_amdgcn_sqrtf(n2) * 1.02f);
        LDS128(L_Q + w * 2 * KB + lo) = f0; LDS128(L_Q + w * 2 * KB + KB + lo) = f1;
        if (pair == 0 && g < 2) { const int d_ = WKV_OFF + ((blk * 2 + g) * 4 + w) * 14 * KB; UNR for (int k = 0; k < 14; ++k) STC(W16[k], lo + (k & 3) * KB, d_ + (k >> 2) * 4 * KB); }
      }
      STAMP(layer * 16 + 4);
      if (w < 2 || pair == 0) { poll(flags + (FA_OFF + (pair * 8 + (b & 7)) * 2 * KB) / 8 + tid, tag); AFTER_POLL(); }
      STAMP(layer * 16 + 5);
      __syncthreads();
      STAMP(layer * 16 + 6);
      int base = KVA_OFF + pair * KVA_L + w * 64 * KB; OPQ(base);
      int woz = WOA_OFF + tlc * 32 * KB; OPQ(woz);
      int wvb = wbB + 56 * KB, wkb = wbB + 28 * KB; OPQ(wvb); OPQ(wkb);
      u32x4 q[4][2];
      const f32x4 km = __builtin_bit_cast(f32x4, LDC(lo, KM_OFF + pair * KB));
      if (b == 0 && tid == 0 && pair == 0) __hip_atomic_store(flags + EP_OFF / 8, tag, RLX);
      UNR for (int h = 0; h < 4; ++h) { const int qo = L_Q + ((w + 1 + h) & 3) * 2 * KB + lo; q[h][0] = LDS128(qo); q[h][1] = LDS128(qo + KB); }
#pragma unroll 1
      for (int attempt = 0; attempt < 2; ++attempt) {
        u32x4 Kt[16][2], Vt[16][2];
#define LDK(t) { Kt[t][0] = LDC(lo, base + (t) * 4 * KB); Kt[t][1] = LDC(lo + KB, base + (t) * 4 * KB); }
#define LDV(t) { Vt[t][0] = LDC(lo + 2 * KB, base + (t) * 4 * KB); Vt[t][1] = LDC(lo + 3 * KB, base + (t) * 4 * KB); }
        LDK(0) LDV(0) LDK(1) LDV(1) LDK(2)
        SBAR();
        if (attempt == 0) {
          const float kmax = __builtin_amdgcn_sqrtf(__uint_as_float(umax64(umax(__float_as_uint(km.x + km.y), __float_as_uint(km.z + km.w)))));
          UNR for (int h = 0; h < 4; ++h) { const unsigned m = bf_ceil(bflo(q[h][1].z) * kmax) ^ 0x8000u; q[h][1].z = hh ? m : q[h][1].z; }
        }
        float z = 0.f; PIN(z); const unsigned zu = __float_as_uint(z);
        f32x16 oo[4] = {z, z, z, z};
        f32x16 sA, sB; u32x4 pA0 = zu, pA1 = zu, pB0 = zu, pB1 = zu; f32x4 ea, eb = z;
        PIN(q[0][1]); PIN(q[1][1]); PIN(q[2][1]); PIN(q[3][1]); PIN(pA0); PIN(pA1); PIN(pB0); PIN(pB1); PIN(eb);
        asm volatile("s_nop 1" : "+a"(oo[0]), "+a"(oo[1]), "+a"(oo[2]), "+a"(oo[3]));
        SBAR();
        MS0(sA, Kt[0][0], q[0][0]); MS1(sA, Kt[0][1], q[0][1]);
        NOP16V(sA); SBAR();
#define WPF(n) { if ((n) < 32) WZ[(n) & 31] = LDW(woz, (n)); else if ((n) < 60) Wv[((n) - 32) % 28] = LDW(wvb, (n) - 32); }
#define PUTO(i) { asm volatile("" : "+a"(oo[i])); const int s_ = L_C + (((w + 1 + (i)) & 3) * 3 + (i)) * 4 * KB + lo; UNR for (int e = 0; e < 4; ++e) \
          *(LAS f32x4*)(lds + s_ + e * KB) = (f32x4){oo[i][4 * e], oo[i][4 * e + 1], oo[i][4 * e + 2], oo[i][4 * e + 3]}; }
        UNR for (int t = 0; t < 16; ++t) {
          const int tp = t > 0 ? t - 1 : 0, tn = t < 15 ? t + 1 : 15;
          STEP(sA, sB, Kt[t][0], Kt[t][1], q[1][0], q[1][1], oo[3], Vt[tp][0], Vt[tp][1], pB0, pB1, pA0, pA1, , , WPF(4 * t), WPF(4 * t + 1));
          STEP(sB, sA, Kt[t][0], Kt[t][1], q[2][0], q[2][1], oo[0], Vt[t][0], Vt[t][1], pA0, pA1, pB0, pB1, , ,
               if (t + 2 < 16) Vt[(t + 2) & 15][0] = LDC(lo + 2 * KB, base + (t + 2) * 4 * KB), if (t + 2 < 16) Vt[(t + 2) & 15][1] = LDC(lo + 3 * KB, base + (t + 2) * 4 * KB));
          STEP(sA, sB, Kt[t][0], Kt[t][1], q[3][0], q[3][1], oo[1], Vt[t][0], Vt[t][1], pB0, pB1, pA0, pA1, , , WPF(4 * t + 2), WPF(4 * t + 3) if (t == 15) PUTO(0));
          STEP(sB, sA, Kt[tn][0], Kt[tn][1], q[0][0], q[0][1], oo[2], Vt[t][0], Vt[t][1], pA0, pA1, pB0, pB1, , ,
               if (t + 3 < 16) Kt[(t + 3) & 15][0] = LDC(lo, base + (t + 3) * 4 * KB), if (t + 3 < 16) Kt[(t + 3) & 15][1] = LDC(lo + KB, base + (t + 3) * 4 * KB); if (t == 15) PUTO(1));
        }
        C2(pB1, 2, eb) asm volatile("s_nop 1");
        MO(oo[3], Vt[15][0], pB0); MO(oo[3], Vt[15][1], pB1);
        asm volatile("s_nop 3" : "+a"(oo[2])); SBAR();
        PUTO(2)
        STAMP(layer * 16 + 7);
        UNR for (int k = 0; k < 8; ++k) Wk[k] = LDW(wkb, k);
        NOP16A(oo[3]); SBAR();
        f32x16 o = oo[3];
        __syncthreads();
        UNR for (int s = 0; s < 3; ++s)
          UNR for (int e = 0; e < 4; ++e) { const f32x4 t = *(LAS f32x4*)(lds + L_C + (w * 3 + s) * 4 * KB + e * KB + lo);
            o[4 * e] += t.x; o[4 * e + 1] += t.y; o[4 * e + 2] += t.z; o[4 * e + 3] += t.w; }
        const float l = half_hi(o[12]);
        O_STORE(layer, o, l);
        const bool bad = !__all(l >= L_MIN);
        if (lane == 0) *(LAS unsigned*)(lds + L_MISC + w * 4) = bad ? 1u : 0u;
        STAMP(layer * 16 + 8);
        DRAIN();
        __syncthreads();
        const u32x4 bw = *(LAS u32x4*)(lds + L_MISC);
        if (__builtin_expect(attempt == 1 || __builtin_amdgcn_readfirstlane(bw.x | bw.y | bw.z | bw.w) == 0u, 1)) break;
        float mx[4] = {-INFINITY, -INFINITY, -INFINITY, -INFINITY};
        UNR for (int h = 0; h < 4; ++h) q[h][1].z = hh ? 0u : q[h][1].z;
#pragma unroll 1
        for (int t = 0; t < 16; ++t) {
          const u32x4 k0 = LDC(lo, base + t * 4 * KB), k1 = LDC(lo + KB, base + t * 4 * KB);
          UNR for (int h = 0; h < 4; ++h) { f32x16 s = {}; s = mfma32(k0, q[h][0], s); s = mfma32(k1, q[h][1], s);
            UNR for (int e = 0; e < 16; ++e) mx[h] = fmaxf(mx[h], s[e]); }
        }
        UNR for (int h = 0; h < 4; ++h) { const float m = half_max(mx[h]); if (hh == 0) *(LAS float*)(lds + L_MISC + 1024 + (w * 4 + ((w + 1 + h) & 3)) * 128 + r * 4) = m; }
        __syncthreads();
        UNR for (int h = 0; h < 4; ++h) { float m = -INFINITY;
          UNR for (int s = 0; s < 4; ++s) m = fmaxf(m, *(LAS float*)(lds + L_MISC + 1024 + (s * 4 + ((w + 1 + h) & 3)) * 128 + r * 4));
          q[h][1].z = hh ? (bf_ceil(m) ^ 0x8000u) : q[h][1].z; }
      }
      if (tid == 0) __hip_atomic_store(flags + (FO_OFF + (layer * 256 + blk * 4 + g) * 128) / 8, tag, RLX);
      Z_PHASE(layer, false,
              UNR for (int k = 8; k < 18; ++k) Wk[k] = LDW(wkb, k),
              if ((k & 1) && k < 20) Wk[18 + (k >> 1)] = LDW(wkb, 18 + (k >> 1)); else if ((k & 1) && k < 24) WqB[(k >> 1) - 10] = LDW(wbB, (k >> 1) - 10));
    }
    {
      const int layer = 2 * pair + 1;
      IDX();
      const int wbB = WB_OFF + head * 3 * 28 * KB;
      UNR for (int k = 2; k < 10; ++k) WqB[k] = LDW(wbB, k);
      int woz = WOB_OFF + tlc * 32 * KB; OPQ(woz);
      f32x16 csqB, cskB; LDCS(csqB, head * 28); LDCS(cskB, 448 + head * 28);
      const float csv = __uint_as_float(__builtin_amdgcn_raw_buffer_load_b32(rs, r * 4, CS_OFF + (896 + head * 28) * 4, 16));
      SBAR();
      X_GATHER(layer);
      float mu, rstd, qn;
      u32x4 K[9][2], V[9][2], q0, q1;
      unsigned kown;
      {
        const int dst = KVB_OFF + pair * KVB_L + blk * KVB_BLK + head * 4 * KB;
        f32x16 av = {}, ak = {}, aq = {}; u32x4 xf[84]; f32x4 rs_[4], nm_[4];
        X_STATS_LD();
        UNR for (int k = 0; k < 6; ++k) xf[k] = XFRAG(k);
        SBAR();
        sfor<0, 84>([&](auto kc) __attribute__((always_inline)) { constexpr int k = decltype(kc)::value;
          if constexpr (k + 6 < 84) xf[k + 6] = XFRAG((k + 6) % 28);
          if constexpr (k % 5 == 1 || k % 5 == 3) { constexpr int i = (k / 5) * 2 + (k % 5 == 3);
            if constexpr (i < 18) WqB[10 + i] = LDW(wbB, 10 + i); else if constexpr (i < 23) WZ[i - 18] = LDW(woz, i - 18); else if constexpr (k > 72) WZ[i - 24] = LDW(woz, i - 24); }
          if constexpr (k < 28) av = mfma32(xf[k], Wv[k], av); else if constexpr (k < 56) ak = mfma32(Wk[k - 28], xf[k], ak); else aq = mfma32(WqB[k - 56], xf[k], aq);
          if constexpr (k == 1) X_STATS(mu, rstd);
          if constexpr (k == 8) { UNR for (int i = 0; i < 4; ++i) { rs_[i] = *(LAS f32x4*)(lds + L_MS + w * 256 + (8 * i + 4 * hh) * 4); nm_[i] = *(LAS f32x4*)(lds + L_MS + w * 256 + 128 + (8 * i + 4 * hh) * 4); } }
          if constexpr (k == 30) asm volatile("" : "+a"(av));
          if constexpr (k >= 30 && k < 34) { constexpr int i = k - 30;
            unsigned p0 = pk(fmaf(av[4 * i], rs_[i].x, nm_[i].x * csv), fmaf(av[4 * i + 1], rs_[i].y, nm_[i].y * csv));
            unsigned p1 = pk(fmaf(av[4 * i + 2], rs_[i].z, nm_[i].z * csv), fmaf(av[4 * i + 3], rs_[i].w, nm_[i].w * csv));
            const unsigned pad = r == 28 ? 0x3f803f80u : 0u;
            V[8][i >> 1][2 * (i & 1)] = r < 28 ? p0 : pad; V[8][i >> 1][2 * (i & 1) + 1] = r < 28 ? p1 : pad; }
          if constexpr (k == 34) { STC(V[8][0], lo + 2 * KB, dst); STC(V[8][1], lo + 3 * KB, dst); }
          if constexpr (k == 57) { asm volatile("" : "+a"(ak));
            float kn2; EPI_ALPHA(ak, cskB, mu, rstd, kn2);
            if (hh) ak[12] = 1.f;
            pack16(ak, K[8][0], K[8][1]);
            STC(K[8][0], lo, dst); STC(K[8][1], lo + KB, dst);
            kown = umax32(__float_as_uint(kn2));
            if (lane == 0) __builtin_amdgcn_raw_buffer_store_b32(kown, rs, 0, KMB_OFF + ((pair * 64 + blk) * 16 + head) * 4, 16);
            STAMP(layer * 16 + 3); }
          SBAR();
          if constexpr (k == 72) { DRAIN(); __builtin_amdgcn_s_barrier();
            if (tid == 0) __hip_atomic_store(flags + (FB_OFF + (pair * 256 + blk * 4 + g) * 128) / 8, tag, RLX);
            SBAR(); } });
        float n2; EPI_ALPHA(aq, csqB, mu, rstd * QSCALE, n2);
        pack16(aq, q0, q1);
        qn = __builtin_amdgcn_sqrtf(n2) * 1.02f;
      }
      STAMP(layer * 16 + 4);
      const bool pv = lane < 8 && blk - 8 + lane >= 0;
      if (pv) poll(flags + (FB_OFF + (pair * 256 + (blk - 8 + lane) * 4 + g) * 128) / 8, tag);
      AFTER_POLL();
      STAMP(layer * 16 + 5);
      unsigned kmx = __builtin_amdgcn_raw_buffer_load_b32(rs, pv ? (blk - 8 + lane) * 64 : blk * 64, KMB_OFF + (pair * 64 * 16 + head) * 4, 16);
      const int kvb = KVB_OFF + pair * KVB_L + (blk - 8) * KVB_BLK + head * 4 * KB;
#define SRCB(j) (blk - 8 + (j) < 0 ? ZERO_OFF : kvb + (j) * KVB_BLK)
#define LKB(j) { const int s_ = SRCB(j); K[j][0] = LDC(lo, s_); K[j][1] = LDC(lo + KB, s_); }
#define LVB(j) { const int s_ = SRCB(j); V[j][0] = LDC(lo + 2 * KB, s_); V[j][1] = LDC(lo + 3 * KB, s_); }
      bool first = true;
      f32x16 o; float l;
#pragma unroll 1
      for (int attempt = 0; attempt < 2; ++attempt) {
        LKB(7) LKB(6) LVB(7) LKB(5) LVB(6) LKB(4)
        SBAR();
        if (first) { kmx = __builtin_amdgcn_readfirstlane(umax16(pv ? kmx : kown));
          q1.z = hh ? (bf_ceil(qn * __builtin_amdgcn_sqrtf(__uint_as_float(kmx))) ^ 0x8000u) : q1.z; first = false; }
        STAMP(layer * 16 + 6);
        float z = 0.f; PIN(z); const unsigned zu = __float_as_uint(z);
        o = z;
        f32x16 sA, sB; u32x4 pA0 = zu, pA1 = zu, pB0 = zu, pB1 = zu; f32x4 ea, eb = z;
        PIN(q0); PIN(q1); PIN(K[8][0]); PIN(K[8][1]); PIN(V[8][0]); PIN(V[8][1]); PIN(pA0); PIN(pA1); PIN(pB0); PIN(pB1); PIN(eb); asm volatile("s_nop 1" : "+a"(o)); SBAR();
        MS0(sA, K[8][0], q0); MS1(sA, K[8][1], q1);
        NOP16V(sA); SBAR();
#define MASKT(S, keep) { UNR for (int e = 0; e < 16; ++e) { const int row = (e & 3) + 8 * (e >> 2) + 4 * hh; S[e] = (keep) ? S[e] : -INFINITY; } PIN(S); SBAR(); }
#define WZ2(k) { WZ[k] = LDW(woz, k); WZ[k + 1] = LDW(woz, k + 1); }
        MASKT(sA, row <= r)
        STEP(sA, sB, K[7][0], K[7][1], q0, q1, o, V[8][0], V[8][1], pB0, pB1, pA0, pA1, , , LVB(5), LKB(3));
        STEP(sB, sA, K[6][0], K[6][1], q0, q1, o, V[8][0], V[8][1], pA0, pA1, pB0, pB1, , , LVB(4), LKB(2));
        STEP(sA, sB, K[5][0], K[5][1], q0, q1, o, V[7][0], V[7][1], pB0, pB1, pA0, pA1, , , LVB(3), LKB(1));
        STEP(sB, sA, K[4][0], K[4][1], q0, q1, o, V[6][0], V[6][1], pA0, pA1, pB0, pB1, , , LVB(2), LKB(0));
        STEP(sA, sB, K[3][0], K[3][1], q0, q1, o, V[5][0], V[5][1], pB0, pB1, pA0, pA1, , , LVB(1), LVB(0));
        STEP(sB, sA, K[2][0], K[2][1], q0, q1, o, V[4][0], V[4][1], pA0, pA1, pB0, pB1, , , WZ2(10), );
        STEP(sA, sB, K[1][0], K[1][1], q0, q1, o, V[3][0], V[3][1], pB0, pB1, pA0, pA1, , , WZ2(12), WZ2(14));
        STEP(sB, sA, K[0][0], K[0][1], q0, q1, o, V[2][0], V[2][1], pA0, pA1, pB0, pB1, , , WZ2(16), WZ2(18));
        MASKT(sA, row > r)
        STEP(sA, sB, K[0][0], K[0][1], q0, q1, o, V[1][0], V[1][1], pB0, pB1, pA0, pA1, , , , );
        C2(pA1, 2, eb) asm volatile("s_nop 1");
        MO(o, V[0][0], pA0); MO(o, V[0][1], pA1);
        NOP16A(o); SBAR();
        l = half_hi(o[12]);
        if (__builtin_expect(attempt == 1 || __all(l >= L_MIN), 1)) break;
        q1.z = hh ? 0u : q1.z;
        float mx = -INFINITY;
        UNR for (int j = 8; j >= 0; --j) {
          f32x16 s = {}; s = mfma32(K[j][0], q0, s); s = mfma32(K[j][1], q1, s); float m_ = -INFINITY;
          UNR for (int e = 0; e < 16; ++e) { const int row = (e & 3) + 8 * (e >> 2) + 4 * hh; float v = s[e];
            if (j == 8) v = (row <= r) ? v : -INFINITY; if (j == 0) v = (row > r) ? v : -INFINITY; m_ = fmaxf(m_, v); }
          mx = (blk - 8 + j >= 0) ? fmaxf(mx, m_) : mx; SBAR(); }
        mx = half_max(mx);
        q1.z = hh ? (bf_ceil(mx) ^ 0x8000u) : q1.z;
      }
      STAMP(layer * 16 + 7);
      O_STORE(layer, o, l);
      STAMP(layer * 16 + 8);
      PUBLISH(FO_OFF + (layer * 256 + blk * 4 + g) * 128);
      int wkv = g < 2 ? WKV_OFF + ((blk * 2 + g) * 4 + w) * 14 * KB : WS_BYTES; OPQ(wkv);
      Z_PHASE(layer, true,
              UNR for (int k = 20; k < 32; ++k) WZ[k] = LDW(woz, k),
              if ((k & 1) && k < 28) W16[k >> 1] = LDW(wkv, k >> 1));
      cskv = (f32x4){0.f, 0.f, 0.f, 0.f};
      if (g == 0) { UNR for (int k = 0; k < 14; ++k) cskv = mfma16(W16[k], ones, cskv); }
      else if (g == 1) { UNR for (int k = 0; k < 14; ++k) cskv = mfma16(ones, W16[k], cskv); }
      SBAR();
      X_GATHER(layer + 1);
    }
  }
}

extern "C" void kernel_launch(void* const* d_in, const int* in_sizes, int n_in, void* d_out, int out_size, void* d_ws, size_t ws_size, hipStream_t stream) {
  (void)in_sizes; (void)n_in; (void)out_size; (void)ws_size;
  (void)hipFuncSetAttribute((const void*)main_kernel, hipFuncAttributeMaxDynamicSharedMemorySize, L_BYTES);
  main_kernel<<<256, 256, L_BYTES, stream>>>((const u16*)d_in[0], (const u16*)d_in[1], (const u16*)d_in[2], (const u16*)d_in[3], (const u16*)d_in[4],
                                             (const u16*)d_in[6], (const u16*)d_in[7], (unsigned char*)d_ws, (u16*)d_out);
}
```

```cpp
#include <hip/hip_runtime.h>
#include <hip/hip_bf16.h>

typedef float f32x16 __attribute__((ext_vector_type(16)));
typedef float f32x4 __attribute__((ext_vector_type(4)));
typedef float f32x2 __attribute__((ext_vector_type(2)));
typedef unsigned u32x4 __attribute__((ext_vector_type(4)));
typedef unsigned u32x2 __attribute__((ext_vector_type(2)));
typedef short bf16x8 __attribute__((ext_vector_type(8)));
typedef short s16x4 __attribute__((ext_vector_type(4)));
typedef __bf16 bf2 __attribute__((ext_vector_type(2)));
typedef unsigned short u16;
typedef unsigned long long u64;
#define GAS __attribute__((address_space(1)))
#define LAS __attribute__((address_space(3)))
typedef GAS u64 gu64;
#define RLX __ATOMIC_RELAXED, __HIP_MEMORY_SCOPE_AGENT
#define SBAR() __builtin_amdgcn_sched_barrier(0)
#define PIN(x) asm volatile("" : "+v"(x))
#define OPQ(x) asm volatile("" : "+s"(x))
#define DRAIN() asm volatile("s_waitcnt vmcnt(0)" ::: "memory")
#define UNR _Pragma("unroll")

constexpr int DM = 448, NB = 64, NTL = 14, KB = 1024;
constexpr int EP_OFF = 0;
constexpr int FA_OFF = 1 * KB;
constexpr int FL_OFF = FA_OFF + 2 * 8 * 2 * KB;
constexpr int KM_OFF = FL_OFF + 256 * 128;
constexpr int KMB_OFF = KM_OFF + 2 * KB;
constexpr int CS_OFF = KMB_OFF + 4 * KB;
constexpr int ZERO_OFF = CS_OFF + 6 * KB;
constexpr int WKV_OFF = ZERO_OFF + 4 * KB;
constexpr int KVA_OFF = WKV_OFF + 56 * KB;
constexpr int KVA_L = NB * 4 * KB;
constexpr int WOA_OFF = KVA_OFF + 2 * KVA_L;
constexpr int WOB_OFF = WOA_OFF + 14 * 32 * KB;
constexpr int WB_OFF = WOB_OFF + 14 * 32 * KB;
constexpr int XT_OFF = WB_OFF + 16 * 3 * 28 * KB;
constexpr int XT_WG = 9 * KB, XT_BLK = 4 * XT_WG;
constexpr int OT_OFF = XT_OFF + NB * XT_BLK;
constexpr int OT_BLK = 32 * KB;
constexpr int KVB_OFF = OT_OFF + NB * OT_BLK;
constexpr int KVB_BLK = 64 * KB;
constexpr int WS_BYTES = KVB_OFF + NB * KVB_BLK;
#define FLAG(wg, word) (flags + (FL_OFF + (wg) * 128) / 8 + (word))
constexpr int L_X = 0;
constexpr int L_O = 0, L_C = 0;
constexpr int L_MISC = 48 * KB;
constexpr int L_Q = 52 * KB;
constexpr int L_RK = 36 * KB;
constexpr int L_RQ = L_RK + 448 * 56;
constexpr int L_MS = 159 * KB;
constexpr int L_BYTES = 160 * KB;
static_assert(L_RQ + 448 * 224 + 64 <= L_MS && L_RQ % 16 == 0, "lds");

#define STAMP(n) do {} while (0)
constexpr float QSCALE = 0.18898223650461361f * 1.4426950408889634f;
constexpr unsigned SPIN_CAP = 1u << 16;
constexpr float L_MIN = 1e-18f;

__device__ __forceinline__ unsigned pk(float a, float b) { f32x2 v = {a, b}; bf2 r = __builtin_convertvector(v, bf2); return __builtin_bit_cast(unsigned, r); }
__device__ __forceinline__ float bflo(unsigned u) { return __uint_as_float(u << 16); }
__device__ __forceinline__ float bfhi(unsigned u) { return __uint_as_float(u & 0xffff0000u); }
__device__ __forceinline__ f32x16 mfma32(u32x4 a, u32x4 b, f32x16 c) {
  return __builtin_amdgcn_mfma_f32_32x32x16_bf16(__builtin_bit_cast(bf16x8, a), __builtin_bit_cast(bf16x8, b), c, 0, 0, 0); }
__device__ __forceinline__ f32x4 mfma16(u32x4 a, u32x4 b, f32x4 c) {
  return __builtin_amdgcn_mfma_f32_16x16x32_bf16(__builtin_bit_cast(bf16x8, a), __builtin_bit_cast(bf16x8, b), c, 0, 0, 0); }
__device__ __forceinline__ float half_sum(float v) { auto r = __builtin_amdgcn_permlane32_swap(__float_as_uint(v), __float_as_uint(v), false, false); return __uint_as_float(r[0]) + __uint_as_float(r[1]); }
__device__ __forceinline__ float half_max(float v) { auto r = __builtin_amdgcn_permlane32_swap(__float_as_uint(v), __float_as_uint(v), false, false); return fmaxf(__uint_as_float(r[0]), __uint_as_float(r[1])); }
__device__ __forceinline__ float half_hi(float v) { auto r = __builtin_amdgcn_permlane32_swap(__float_as_uint(v), __float_as_uint(v), false, false); return __uint_as_float(r[1]); }
__device__ __forceinline__ float row_sum(float v) { auto r = __builtin_amdgcn_permlane16_swap(__float_as_uint(v), __float_as_uint(v), false, false); return __uint_as_float(r[0]) + __uint_as_float(r[1]); }
template <int C> __device__ __forceinline__ float dppf(float v) { return __uint_as_float(__builtin_amdgcn_update_dpp(0u, __float_as_uint(v), C, 0xf, 0xf, true)); }
__device__ __forceinline__ float sum8(float v) { v += dppf<0xb1>(v); v += dppf<0x4e>(v); return v + dppf<0x141>(v); }
template <int N> __device__ __forceinline__ unsigned ror(unsigned v) { return __builtin_amdgcn_update_dpp(0u, v, 0x120 + N, 0xf, 0xf, true); }
__device__ __forceinline__ unsigned umax(unsigned a, unsigned b) { return a > b ? a : b; }
__device__ __forceinline__ unsigned umax16(unsigned v) { v = umax(v, ror<1>(v)); v = umax(v, ror<2>(v)); v = umax(v, ror<4>(v)); return umax(v, ror<8>(v)); }
__device__ __forceinline__ unsigned umax32(unsigned v) { v = umax16(v); auto r = __builtin_amdgcn_permlane16_swap(v, v, false, false); return umax(r[0], r[1]); }
__device__ __forceinline__ unsigned umax64(unsigned v) { v = umax32(v); auto r = __builtin_amdgcn_permlane32_swap(v, v, false, false); return umax(r[0], r[1]); }
__device__ __forceinline__ unsigned bf_ceil(float v) { unsigned u = __float_as_uint(v); unsigned t = u >> 16; return (v > 0.f && (u & 0xffffu)) ? t + 1u : t; }
__device__ __forceinline__ void pack16(const f32x16& v, u32x4& f0, u32x4& f1) {
  f0 = (u32x4){pk(v[0], v[1]), pk(v[2], v[3]), pk(v[4], v[5]), pk(v[6], v[7])};
  f1 = (u32x4){pk(v[8], v[9]), pk(v[10], v[11]), pk(v[12], v[13]), pk(v[14], v[15])}; }
__device__ __forceinline__ void unpack16(const u32x4 f0, const u32x4 f1, f32x16& v) {
  UNR for (int i = 0; i < 4; ++i) { v[2 * i] = bflo(f0[i]); v[2 * i + 1] = bfhi(f0[i]); v[8 + 2 * i] = bflo(f1[i]); v[9 + 2 * i] = bfhi(f1[i]); } }
__device__ __forceinline__ void tile_stats(const f32x16& v, float& mean, float& m2) {
  const float s = (((v[0] + v[1]) + (v[2] + v[3])) + ((v[4] + v[5]) + (v[6] + v[7]))) + (((v[8] + v[9]) + (v[10] + v[11])) + ((v[12] + v[13]) + (v[14] + v[15])));
  mean = half_sum(s) * (1.f / 32.f);
  float q0 = 0.f, q1 = 0.f;
  UNR for (int i = 0; i < 8; ++i) { const float d0 = v[i] - mean, d1 = v[8 + i] - mean; q0 = fmaf(d0, d0, q0); q1 = fmaf(d1, d1, q1); }
  m2 = half_sum(q0 + q1);
}
template <int V> struct IC { static constexpr int value = V; };
template <int I, int N, class F> __device__ __forceinline__ void sfor(F&& f) { if constexpr (I < N) { f(IC<I>{}); sfor<I + 1, N>(f); } }
__device__ __forceinline__ u32x4 mkdesc(const void* p, unsigned bytes) { return (u32x4){(unsigned)(size_t)p, (unsigned)((size_t)p >> 32) & 0xffffu, bytes, 0x00020000u}; }

#define LDC(voff, soff) __builtin_amdgcn_raw_buffer_load_b128(rs, (voff), (soff), 16)
#define STC(v, voff, soff) __builtin_amdgcn_raw_buffer_store_b128((v), rs, (voff), (soff), 16)
#define LDS128(off) (*(LAS u32x4*)(lds + (off)))
#define LDW(base, k) LDC(lo + ((k) & 3) * KB, (base) + ((k) >> 2) * 4 * KB)
#define MS0(S, A, B) asm volatile("v_mfma_f32_32x32x16_bf16 %0, %1, %2, 0" : "=&v"(S) : "v"(A), "v"(B))
#define MS1(S, A, B) asm volatile("v_mfma_f32_32x32x16_bf16 %0, %1, %2, %0" : "+v"(S) : "v"(A), "v"(B))
#define MO(O, A, B) asm volatile("v_mfma_f32_32x32x16_bf16 %0, %1, %2, %0" : "+a"(O) : "v"(A), "v"(B))
#define EX(x) __builtin_amdgcn_exp2f(x)
#define E4(E, S, i) { E[0] = EX(S[i]); E[1] = EX(S[i + 1]); E[2] = EX(S[i + 2]); E[3] = EX(S[i + 3]); PIN(E); SBAR(); }
#define C2(P, j, E) { P[j] = pk(E[0], E[1]); P[j + 1] = pk(E[2], E[3]); PIN(P); SBAR(); }
#define STEP(SC, SN, KN0, KN1, QN0, QN1, OP, VP0, VP1, PP0, PP1, PC0, PC1, W0, W2, LDA, LDB) \
  W0; MS0(SN, KN0, QN0); SBAR(); E4(ea, SC, 0) C2(PP1, 2, eb) \
  MS1(SN, KN1, QN1); LDA; SBAR(); E4(eb, SC, 4) C2(PC0, 0, ea) \
  W2; MO(OP, VP0, PP0); SBAR(); E4(ea, SC, 8) C2(PC0, 2, eb) \
  MO(OP, VP1, PP1); LDB; SBAR(); E4(eb, SC, 12) C2(PC1, 0, ea)
#define NOP16V(x) asm volatile("s_nop 7\n\ts_nop 7" : "+v"(x))
#define NOP16A(x) asm volatile("s_nop 7\n\ts_nop 7" : "+a"(x))
#define VMW(n) asm volatile("s_waitcnt vmcnt(" #n ")")
#define ALD(dst, soff, imm) asm volatile("buffer_load_dwordx4 %0, %1, %2, %3 offen offset:" #imm " sc1" : "+v"(dst) : "v"(lo), "s"(desc), "s"(soff))
#define ALD0(dst, soff, imm) asm volatile("buffer_load_dwordx4 %0, %1, %2, %3 offen offset:" #imm " sc1" : "=v"(dst) : "v"(lo), "s"(desc), "s"(soff))
#define ALDA(dst, soff, imm) asm volatile("buffer_load_dwordx4 %0, %1, %2, %3 offen offset:%4 sc1" : "=a"(dst) : "v"(lo), "s"(desc), "s"(soff), "i"(imm))
#define LDWA(dst, base, k) { const int so_ = (base) + ((k) >> 2) * 4 * KB; ALDA(dst, so_, ((k) & 3) * KB); }
#define ADMA(ldsaddr, voff, dsc, soff) asm volatile("s_mov_b32 m0, %0\n\ts_nop 0\n\tbuffer_load_dwordx4 %1, %2, %3 offen lds" :: "s"(ldsaddr), "v"(voff), "s"(dsc), "s"(soff) : "memory")
#define DMAW(base, k) { const unsigned la_ = L_W + w * L_WW + (k) * KB; const int so_ = (base) + (k) * KB; ADMA(la_, lo, desc, so_); }
#define TRRD(off) __builtin_bit_cast(u32x2, __builtin_amdgcn_ds_read_tr16_b64_v4i16((LAS s16x4*)(lds + (off))))

__device__ __forceinline__ void poll(gu64* p, u64 tag) {
  unsigned spins = 0;
  while (__hip_atomic_load(p, RLX) != tag) { if (++spins > SPIN_CAP) break; }
}
#define AFTER_POLL() __builtin_amdgcn_fence(__ATOMIC_ACQUIRE, "wavefront")

__global__ __launch_bounds__(256, 1) void main_kernel(const u16* __restrict__ x, const u16* __restrict__ wq, const u16* __restrict__ wk, const u16* __restrict__ wv,
                                                      const u16* __restrict__ woa, const u16* __restrict__ wb, const u16* __restrict__ wob,
                                                      unsigned char* __restrict__ ws, u16* __restrict__ out) {
  extern __shared__ __attribute__((aligned(16))) unsigned char smem[];
  const __amdgpu_buffer_rsrc_t rs = __builtin_amdgcn_make_buffer_rsrc((void*)ws, 0, WS_BYTES, 0x00020000);
  const u32x4 desc = mkdesc(ws, WS_BYTES);
  LAS unsigned char* lds = (LAS unsigned char*)smem;
  gu64* flags = (gu64*)ws;
  int tid_ = threadIdx.x, b_ = blockIdx.x, w_ = __builtin_amdgcn_readfirstlane(threadIdx.x >> 6);
#define IDX() asm volatile("" : "+v"(tid_)); asm volatile("" : "+s"(b_), "+s"(w_)); \
  const int tid = tid_, b = b_, w = w_, lane = tid & 63, r = lane & 31, hh = lane >> 5, lo = lane * 16; \
  const int blk = (b & 7) * 8 + (b >> 5), g = (b >> 3) & 3, head = 4 * g + w, tl = head; const bool act = tl < NTL; const int tlc = act ? tl : 0; \
  const int dh = w & 1, th = w >> 1; (void)r; (void)hh; (void)dh; (void)th; (void)tlc; (void)blk; (void)lo
  u64 pc_; unsigned len_;
  asm volatile("s_getpc_b64 %0\n.Lcode_here:\n\ts_mov_b32 %1, .Lcode_last-.Lcode_here" : "=s"(pc_), "=s"(len_));
#define CODEPF(ldsoff) { const u32x4 dc = mkdesc((const void*)pc_, len_); const int vo = ((b >> 3) * 4 + w) * 512 + (lane & 3) * 128; const unsigned la = (ldsoff) + w * 256; \
    asm volatile("s_mov_b32 m0, %0\n\ts_nop 0\n\tbuffer_load_dword %1, %2, 0 offen lds" :: "s"(la), "v"(vo), "s"(dc) : "memory"); }
  STAMP(64);

  f32x16 xres;
  f32x4 cskv;
  u32x4 W16[14];
  u64 tag;
  const u32x4 ones = {0x3f803f80u, 0x3f803f80u, 0x3f803f80u, 0x3f803f80u};
  {
    IDX();
    u64 ep; asm volatile("global_load_dwordx2 %0, %1, %2 sc1" : "=v"(ep) : "v"(0), "s"(ws));
    u32x4 xr[7];
    { const u32x4 dx = mkdesc(x + 32 * blk * DM, 32 * 896); const int vo = (8 * w + (lane >> 3)) * 896 + (lane & 7) * 16;
      UNR for (int f = 0; f < 7; ++f) asm volatile("buffer_load_dwordx4 %0, %1, %2, 0 offen offset:%3" : "=v"(xr[f]) : "v"(vo), "s"(dx), "i"(f * 128)); }
    CODEPF(L_X + 31 * KB);
    {
      const int vo = lane < 5 ? ((b * 4 + w) * 5 + lane) * 128 + KB : lane * 192 * KB; const unsigned la = L_X + 33 * KB + w * 256;
      asm volatile("s_mov_b32 m0, %0\n\ts_nop 0\n\tbuffer_load_dword %1, %2, 0 offen sc1 lds" :: "s"(la), "v"(vo), "s"(desc) : "memory"); }
    const int n = blk * 2 + (g & 1), nw = n - 84;
    const int c3 = (n * 37) >> 10, ks = n - 28 * c3;
    u32x4 cl[4];
    if (g < 2 || n >= 84) { const u32x4 dk = g < 2 ? mkdesc(g ? wv : wk, 448 * 56) : mkdesc((nw & 16 ? wob : woa) + (nw & 15) * 28 * 448, n < 116 ? 448 * 56 : 0);
      UNR for (int k = 0; k < 7; ++k) if (4 * k + w < 25) { const unsigned la = L_RK + (4 * k + w) * KB; const int so = (4 * k + w) * KB;
        if (4 * k + w < 24 || lane < 32) ADMA(la, lo, dk, so); } }
    else { const u32x4 dk = mkdesc(wb + 16 * ks * 1344 + 448 * c3, 16 * 2688);
      UNR for (int k = 0; k < 4; ++k) if (4 * k + w < 14) { const int c = tid + 256 * k, row = (c * 1171) >> 16; const unsigned la = L_RK + (4 * k + w) * KB;
        ADMA(la, row * 2688 + (c - row * 56) * 16, dk, 0); }
      const u32x4 db = mkdesc(wb, 448 * 2688); const int vo = (tid >> 1) * 2688 + (tid & 1) * 16;
      UNR for (int j = 0; j < 4; ++j) { const int so = j * 128 * 2688 + n * 32; asm volatile("buffer_load_dwordx4 %0, %1, %2, %3 offen" : "=v"(cl[j]) : "v"(vo), "s"(db), "s"(so)); } }
    const u32x4 dq = mkdesc(wq, 448 * 448 * 2); int vq[7];
    UNR for (int k = 0; k < 7; ++k) { const int c = tid + 256 * k, row = (c * 4682) >> 16; vq[k] = row * 896 + (c - row * 14) * 16 + g * 224; }
#define WQDMA(u0) { const int u = ((u0) + blk) & 3; UNR for (int k = 0; k < 7; ++k) { const int i = 4 * (7 * u + k) + w; \
      const unsigned la = (k < 3 || i < 98) ? L_RQ + i * KB : L_X + 32 * KB; const int so = u * 128 * 896; ADMA(la, vq[k], dq, so); } }
    WQDMA(0) WQDMA(1)
    STAMP(67);
    asm volatile("s_waitcnt vmcnt(14)" : "+v"(xr[0]), "+v"(xr[1]), "+v"(xr[2]), "+v"(xr[3]), "+v"(xr[4]), "+v"(xr[5]), "+v"(xr[6]), "+v"(cl[0]), "+v"(cl[1]), "+v"(cl[2]), "+v"(cl[3]), "+v"(ep) :: "memory");
    { f32x2 v[28]; f32x2 s = {0.f, 0.f};
      UNR for (int f = 0; f < 7; ++f) UNR for (int c = 0; c < 4; ++c) { v[4 * f + c] = (f32x2){bflo(xr[f][c]), bfhi(xr[f][c])}; s += v[4 * f + c]; }
      const float mu = sum8(s.x + s.y) * (1.f / 448.f);
      s = (f32x2){0.f, 0.f};
      UNR for (int i = 0; i < 28; ++i) { v[i] -= mu; s += v[i] * v[i]; }
      const float rstd = __builtin_amdgcn_rsqf(sum8(s.x + s.y) * (1.f / 448.f) + 1e-5f);
      const int c = lane & 7;
      UNR for (int f = 0; f < 7; ++f) {
        u32x4 o; UNR for (int e = 0; e < 4; ++e) { const f32x2 t = v[4 * f + e] * rstd; o[e] = pk(t.x, t.y); }
        LDS128(L_X + (f >> 1) * XT_WG + (f & 1) * 4 * KB + (c >> 1) * KB + (8 * w + (lane >> 3) + 32 * (c & 1)) * 16) = o; }
      const float one = 32.f * (1.f - 1e-5f);
      *(LAS f32x4*)(lds + L_X + w * XT_WG + 8192 + lo) = (f32x4){0.f, one, 0.f, one};
    }
    if (g >= 2 && n < 84) { float s[8];
      UNR for (int c = 0; c < 4; ++c) { s[2 * c] = (bflo(cl[0][c]) + bflo(cl[1][c])) + (bflo(cl[2][c]) + bflo(cl[3][c])); s[2 * c + 1] = (bfhi(cl[0][c]) + bfhi(cl[1][c])) + (bfhi(cl[2][c]) + bfhi(cl[3][c])); }
      *(LAS f32x4*)(lds + L_RK + 15 * KB + tid * 32) = (f32x4){s[0], s[1], s[2], s[3]}; *(LAS f32x4*)(lds + L_RK + 15 * KB + tid * 32 + 16) = (f32x4){s[4], s[5], s[6], s[7]}; }
    SBAR();
    WQDMA(2) WQDMA(3)
    STAMP(68);
    asm volatile("s_waitcnt lgkmcnt(0)\n\ts_barrier" ::: "memory");
    STAMP(65);
    tag = (((u64)(unsigned)__builtin_amdgcn_readfirstlane((unsigned)(ep >> 32)) << 32) | (unsigned)__builtin_amdgcn_readfirstlane((unsigned)ep)) + 1ull;
    const int q4 = (lane & 15) >> 2, p4 = lane & 3, gq = lane >> 4;
    cskv = (f32x4){0.f, 0.f, 0.f, 0.f};
    if (g < 2) { const int to = L_RK + (8 * gq + q4) * 56 + dh * 32 + p4 * 8;
      UNR for (int k = 0; k < 14; ++k) { const u32x2 a = TRRD(to + k * 32 * 56), c = TRRD(to + k * 32 * 56 + 4 * 56); W16[k] = (u32x4){a.x, a.y, c.x, c.y}; }
    } else {
      const int to = L_RK + (4 * hh + q4) * 896 + (gq & 1) * 32 + p4 * 8;
      if (n < 84) {
        UNR for (int j = 0; j < 4; ++j) { const int h = 4 * w + j; const u32x2 a = TRRD(to + h * 56), c = TRRD(to + h * 56 + 8 * 896);
          STC(((u32x4){a.x, a.y, c.x, c.y}), lo, WB_OFF + ((h * 3 + c3) * 28 + ks) * KB); }
        if (w == 0) { float s = 0.f;
          UNR for (int j = 0; j < 32; ++j) s += *(LAS float*)(lds + L_RK + 15 * KB + (j * 4 + gq) * 64 + (lane & 15) * 4);
          s = half_sum(row_sum(s));
          if (lane < 16) __builtin_amdgcn_raw_buffer_store_b32(__float_as_uint(s), rs, lane * 4, CS_OFF + n * 64, 16); }
      } else if (n < 116) {
        UNR for (int j = 0; j < 4; ++j) if (4 * j + w < NTL) { const int t = 4 * j + w;
          const u32x2 a = TRRD(to + t * 64), c = TRRD(to + t * 64 + 8 * 896), d = TRRD(to + t * 64 + 16 * 896), e = TRRD(to + t * 64 + 24 * 896);
          const int dst = (nw & 16 ? WOB_OFF : WOA_OFF) + (t * 32 + 2 * (nw & 15)) * KB;
          STC(((u32x4){a.x, a.y, c.x, c.y}), lo, dst); STC(((u32x4){d.x, d.y, hh ? 0u : e.x, hh ? 0u : e.y}), lo + KB, dst); }
      } else if (n == 116) STC(((u32x4){0u, 0u, 0u, 0u}), lo, ZERO_OFF + w * KB);
    }
    STAMP(66);
  }

#define X_GATHER(layer) { \
    STAMP((layer) * 16 + 0); \
    poll(FLAG(blk * 4 + w, (layer)), tag); AFTER_POLL(); \
    STAMP((layer) * 16 + 1); \
    int src_ = XT_OFF + blk * XT_BLK + w * XT_WG; OPQ(src_); \
    u32x4 t_[9]; \
    UNR for (int f = 0; f < 9; ++f) t_[f] = LDW(src_, f); \
    UNR for (int f = 0; f < 9; ++f) LDS128(L_X + w * XT_WG + f * KB + lo) = t_[f]; \
    __syncthreads(); \
    STAMP((layer) * 16 + 2); }
#define X_STATS_LD() f32x2 st_[7]; \
    UNR for (int k = 0; k < 7; ++k) st_[k] = *(LAS f32x2*)(lds + L_X + (k >> 1) * XT_WG + 8192 + (k & 1) * 512 + lane * 8)
#define X_STATS(mu, rstd) { \
    mu = half_sum(((st_[0].x + st_[1].x) + (st_[2].x + st_[3].x)) + ((st_[4].x + st_[5].x) + st_[6].x)) * (1.f / 14.f); \
    float q_ = 0.f, q2_ = 0.f; \
    UNR for (int k = 0; k < 7; ++k) { const float d = st_[k].x - mu; q_ = fmaf(d, d, q_); q2_ += st_[k].y; } \
    rstd = __builtin_amdgcn_rsqf(half_sum(fmaf(32.f, q_, q2_)) * (1.f / 448.f) + 1e-5f); \
    *(LAS float*)(lds + L_MS + w * 256 + lane * 4) = hh ? -mu * rstd : rstd; }
#define XFRAG(ks) LDS128(L_X + ((ks) >> 3) * XT_WG + ((ks) & 7) * KB + lo)
#define LDCS(cs, col) { int o_ = CS_OFF + (col) * 4; OPQ(o_); UNR for (int q4 = 0; q4 < 4; ++q4) { const f32x4 c_ = __builtin_bit_cast(f32x4, LDC(hh * 16 + q4 * 32, o_)); \
    cs[4 * q4] = c_.x; cs[4 * q4 + 1] = c_.y; cs[4 * q4 + 2] = c_.z; cs[4 * q4 + 3] = c_.w; } }
#define EPI_ALPHA(a, cs, mu, sc, n2) { const float nm_ = -(mu) * (sc); UNR for (int e = 0; e < 16; ++e) a[e] = fmaf(a[e], (sc), nm_ * cs[e]); \
    UNR for (int e = 12; e < 16; ++e) a[e] = hh ? 0.f : a[e]; \
    float n0_ = 0.f, n1_ = 0.f; UNR for (int e = 0; e < 8; ++e) { n0_ = fmaf(a[e], a[e], n0_); n1_ = fmaf(a[8 + e], a[8 + e], n1_); } n2 = half_sum(n0_ + n1_); }
#define PUBLISH(word) { DRAIN(); __syncthreads(); if (tid == 0) __hip_atomic_store(FLAG(blk * 4 + g, (word)), tag, RLX); }
#define O_STORE(layer, o, l) { const float inv_ = __builtin_amdgcn_rcpf(l); f32x16 v_; \
    UNR for (int e = 0; e < 16; ++e) v_[e] = o[e] * inv_; \
    u32x4 f0_, f1_; pack16(v_, f0_, f1_); if (hh) { f1_.z = 0u; f1_.w = 0u; } \
    const int dst_ = OT_OFF + blk * OT_BLK + head * 2 * KB; STC(f0_, lo, dst_); STC(f1_, lo + KB, dst_); }
#define Z_PHASE(layer, LAST, PRE, SIDE) { \
    STAMP((layer) * 16 + 9); \
    PRE; \
    SBAR(); \
    STAMP((layer) * 16 + 10); \
    poll(FLAG(blk * 4 + w, 4 + (layer)), tag); AFTER_POLL(); \
    STAMP((layer) * 16 + 11); \
    int src_ = OT_OFF + blk * OT_BLK + w * 8 * KB; OPQ(src_); \
    u32x4 t_[8]; \
    UNR for (int f = 0; f < 8; ++f) t_[f] = LDW(src_, f); \
    UNR for (int f = 0; f < 8; ++f) LDS128(L_O + (w * 8 + f) * KB + lo) = t_[f]; \
    __syncthreads(); \
    STAMP((layer) * 16 + 12); \
    u32x4 of_[32]; \
    UNR for (int k = 0; k < 6; ++k) of_[k] = LDS128(L_O + k * KB + lo); \
    SBAR(); \
    UNR for (int k = 0; k < 32; ++k) { if (k + 6 < 32) of_[k + 6] = LDS128(L_O + (k + 6) * KB + lo); SIDE; xres = mfma32(WZ[k], of_[k], xres); SBAR(); } \
    u32x4 f0, f1; pack16(xres, f0, f1); \
    STAMP((layer) * 16 + 13); \
    if (LAST && (layer) == 3) { \
      if (act) { u16* p = out + (32 * blk + r) * DM + 32 * tl + 4 * hh; \
        *(u32x2*)(p) = (u32x2){f0.x, f0.y}; *(u32x2*)(p + 8) = (u32x2){f0.z, f0.w}; *(u32x2*)(p + 16) = (u32x2){f1.x, f1.y}; *(u32x2*)(p + 24) = (u32x2){f1.z, f1.w}; } \
      if constexpr (LAST) asm volatile(".Lcode_last:"); \
      __builtin_amdgcn_endpgm(); \
    } \
    const int dst = XT_OFF + blk * XT_BLK + g * XT_WG; \
    if (act) { STC(f0, lo, dst + w * 2048); STC(f1, lo + KB, dst + w * 2048); } \
    float mean, m2; unpack16(f0, f1, xres); tile_stats(xres, mean, m2); \
    if (act && hh == 0) __builtin_amdgcn_raw_buffer_store_b64((u32x2){__float_as_uint(mean), __float_as_uint(m2)}, rs, r * 8, dst + 8192 + w * 256, 16); \
    STAMP((layer) * 16 + 14); \
    PUBLISH((layer) + 1); \
    STAMP((layer) * 16 + 15); }

#pragma unroll 1
  for (int pair = 0;; ++pair) {
    u32x4 WZ[32], Wv[28], Wk[28], WqB[28];
    {
      const int layer = 2 * pair;
      IDX();
      const int wbB = WB_OFF + head * 3 * 28 * KB;
      float mu, rstd;
      X_STATS_LD();
      const int G = lane >> 4, cc = lane & 15;
      if (g < 2) {
        const int xo = L_X + (G >> 1) * KB + (16 * th + cc + 32 * (G & 1)) * 16;
        u32x4 xf[14];
        UNR for (int k = 0; k < 14; ++k) xf[k] = LDS128(xo + (k >> 2) * XT_WG + (k & 3) * 2048);
        X_STATS(mu, rstd);
        f32x4 a = {0.f, 0.f, 0.f, 0.f};
        const int kvdst = KVA_OFF + pair * KVA_L + blk * 4 * KB;
        if (g == 0) {
          const float rs_ = *(LAS float*)(lds + L_MS + w * 256 + (16 * th + cc) * 4), nm_ = *(LAS float*)(lds + L_MS + w * 256 + 128 + (16 * th + cc) * 4);
          UNR for (int k = 0; k < 14; ++k) a = mfma16(W16[k], xf[k], a);
          const bool pad = dh == 1 && G == 3;
          float n2 = 0.f;
          UNR for (int e = 0; e < 4; ++e) { a[e] = pad ? 0.f : fmaf(a[e], rs_, nm_ * cskv[e]); n2 = fmaf(a[e], a[e], n2); }
          a[0] = pad ? 1.f : a[0];
          __builtin_amdgcn_raw_buffer_store_b64((u32x2){pk(a[0], a[1]), pk(a[2], a[3])}, rs, dh * KB + (16 * th + cc + 32 * (G & 1)) * 16 + 8 * (G >> 1), kvdst, 16);
          const unsigned nm = umax16(__float_as_uint(row_sum(half_sum(n2))));
          if (lane == 0) __builtin_amdgcn_raw_buffer_store_b32(nm, rs, 0, KM_OFF + (pair * 64 + blk) * 16 + w * 4, 16);
        } else {
          const f32x4 rs_ = *(LAS f32x4*)(lds + L_MS + w * 256 + (16 * th + 4 * G) * 4), nm_ = *(LAS f32x4*)(lds + L_MS + w * 256 + 128 + (16 * th + 4 * G) * 4);
          UNR for (int k = 0; k < 14; ++k) a = mfma16(xf[k], W16[k], a);
          UNR for (int e = 0; e < 4; ++e) a[e] = fmaf(a[e], rs_[e], nm_[e] * cskv.x);
          u32x2 o2 = {pk(a[0], a[1]), pk(a[2], a[3])};
          if (dh == 1 && cc >= 12) { o2.x = cc == 12 ? 0x3f803f80u : 0u; o2.y = o2.x; }
          __builtin_amdgcn_raw_buffer_store_b64(o2, rs, 2 * KB + th * KB + (16 * dh + cc + 32 * (G & 1)) * 16 + 8 * (G >> 1), kvdst, 16);
        }
        if (pair == 0 && blk < 14 && th == 0) {
          const int to = L_RK + (32 * blk + 16 * (G >> 1) + 4 * (G & 1) + (cc >> 2)) * 56 + dh * 32 + (cc & 3) * 8;
          const u32x2 a_ = TRRD(to), c_ = TRRD(to + 8 * 56); STC(((u32x4){a_.x, a_.y, c_.x, c_.y}), lo, WKV_OFF + ((g * 2 + dh) * 14 + blk) * KB); }
      } else {
        X_STATS(mu, rstd);
      }
      STAMP(layer * 16 + 3);
      { f32x16 a = {}, csq = {}; u32x4 xf[28], wf[28];
        const int to = L_RQ + ((pair ? 4 : 8) * hh + (cc >> 2)) * 224 + w * 56 + (G & 1) * 32 + (cc & 3) * 8, to2 = to + (pair ? 8 : 4) * 224;
#define WQF(k) { const u32x2 a_ = TRRD(to + (k) * 16 * 224), c_ = TRRD(to2 + (k) * 16 * 224); wf[k] = (u32x4){a_.x, a_.y, c_.x, c_.y}; }
#define PUBA() { asm volatile("s_waitcnt vmcnt(0) lgkmcnt(0)\n\ts_barrier" ::: "memory"); \
            if (tid < 8) __hip_atomic_store(flags + (FA_OFF + (pair * 8 + tid) * 2 * KB) / 8 + (g >> 1) * 128 + blk * 2 + (g & 1), tag, RLX); SBAR(); }
        u32x2 xo[4];
        if (pair == 0) { PUBA();
          UNR for (int a_ = 0; a_ < 4; ++a_) xo[a_] = *(const u32x2*)(x + (32 * blk + r) * DM + 32 * tlc + 8 * a_ + 4 * hh); }
        UNR for (int k = 0; k < 4; ++k) { WQF(k) xf[k] = XFRAG(k); }
        SBAR();
        UNR for (int k = 0; k < 28; ++k) { if (k + 4 < 28) { WQF(k + 4) xf[k + 4] = XFRAG(k + 4); }
          a = mfma32(wf[k], xf[k], a); SBAR();
          if (k == 11 && g < 2 && pair != 0) PUBA(); }
        if (pair != 0) { UNR for (int k = 0; k < 28; ++k) csq = mfma32(wf[k], ones, csq); }
        else unpack16((u32x4){xo[0].x, xo[0].y, xo[1].x, xo[1].y}, (u32x4){xo[2].x, xo[2].y, xo[3].x, xo[3].y}, xres);
        float n2; EPI_ALPHA(a, csq, mu, rstd * QSCALE, n2);
        u32x4 f0, f1; pack16(a, f0, f1);
        if (hh) f1.z = bf_ceil(__builtin_amdgcn_sqrtf(n2) * 1.02f);
        LDS128(L_Q + w * 2 * KB + lo) = f0; LDS128(L_Q + w * 2 * KB + KB + lo) = f1;
      }
      STAMP(layer * 16 + 4);
      if (w < 2 || pair == 0) { poll(flags + (FA_OFF + (pair * 8 + (b & 7)) * 2 * KB) / 8 + tid, tag); AFTER_POLL(); }
      STAMP(layer * 16 + 5);
      __syncthreads();
      STAMP(layer * 16 + 6);
      int base = KVA_OFF + pair * KVA_L + w * 64 * KB; OPQ(base);
      int woz = WOA_OFF + tlc * 32 * KB; OPQ(woz);
      int wvb = wbB + 56 * KB, wkb = wbB + 28 * KB; OPQ(wvb); OPQ(wkb);
      u32x4 q[4][2];
      const f32x4 km = __builtin_bit_cast(f32x4, LDC(lo, KM_OFF + pair * KB));
      if (b == 0 && tid == 0 && pair == 0) __hip_atomic_store(flags + EP_OFF / 8, tag, RLX);
      UNR for (int h = 0; h < 4; ++h) { const int qo = L_Q + ((w + 1 + h) & 3) * 2 * KB + lo; q[h][0] = LDS128(qo); q[h][1] = LDS128(qo + KB); }
#pragma unroll 1
      for (int attempt = 0; attempt < 2; ++attempt) {
        u32x4 Kt[16][2], Vt[16][2];
#define LDK(t) { Kt[t][0] = LDC(lo, base + (t) * 4 * KB); Kt[t][1] = LDC(lo + KB, base + (t) * 4 * KB); }
#define LDV(t) { Vt[t][0] = LDC(lo + 2 * KB, base + (t) * 4 * KB); Vt[t][1] = LDC(lo + 3 * KB, base + (t) * 4 * KB); }
        LDK(0) LDV(0) LDK(1) LDV(1) LDK(2)
        SBAR();
        if (attempt == 0) {
          const float kmax = __builtin_amdgcn_sqrtf(__uint_as_float(umax64(umax(__float_as_uint(km.x + km.y), __float_as_uint(km.z + km.w)))));
          UNR for (int h = 0; h < 4; ++h) { const unsigned m = bf_ceil(bflo(q[h][1].z) * kmax) ^ 0x8000u; q[h][1].z = hh ? m : q[h][1].z; }
        }
        float z = 0.f; PIN(z); const unsigned zu = __float_as_uint(z);
        f32x16 oo[4] = {z, z, z, z};
        f32x16 sA, sB; u32x4 pA0 = zu, pA1 = zu, pB0 = zu, pB1 = zu; f32x4 ea, eb = z;
        PIN(q[0][1]); PIN(q[1][1]); PIN(q[2][1]); PIN(q[3][1]); PIN(pA0); PIN(pA1); PIN(pB0); PIN(pB1); PIN(eb);
        asm volatile("s_nop 1" : "+a"(oo[0]), "+a"(oo[1]), "+a"(oo[2]), "+a"(oo[3]));
        SBAR();
        MS0(sA, Kt[0][0], q[0][0]); MS1(sA, Kt[0][1], q[0][1]);
        NOP16V(sA); SBAR();
#define WPF(n) { if ((n) < 32) WZ[(n) & 31] = LDW(woz, (n)); else if ((n) < 60) Wv[((n) - 32) % 28] = LDW(wvb, (n) - 32); }
#define PUTO(i) { asm volatile("" : "+a"(oo[i])); const int s_ = L_C + (((w + 1 + (i)) & 3) * 3 + (i)) * 4 * KB + lo; UNR for (int e = 0; e < 4; ++e) \
          *(LAS f32x4*)(lds + s_ + e * KB) = (f32x4){oo[i][4 * e], oo[i][4 * e + 1], oo[i][4 * e + 2], oo[i][4 * e + 3]}; }
        UNR for (int t = 0; t < 16; ++t) {
          const int tp = t > 0 ? t - 1 : 0, tn = t < 15 ? t + 1 : 15;
          STEP(sA, sB, Kt[t][0], Kt[t][1], q[1][0], q[1][1], oo[3], Vt[tp][0], Vt[tp][1], pB0, pB1, pA0, pA1, , , WPF(4 * t), WPF(4 * t + 1));
          STEP(sB, sA, Kt[t][0], Kt[t][1], q[2][0], q[2][1], oo[0], Vt[t][0], Vt[t][1], pA0, pA1, pB0, pB1, , ,
               if (t + 2 < 16) Vt[(t + 2) & 15][0] = LDC(lo + 2 * KB, base + (t + 2) * 4 * KB), if (t + 2 < 16) Vt[(t + 2) & 15][1] = LDC(lo + 3 * KB, base + (t + 2) * 4 * KB));
          STEP(sA, sB, Kt[t][0], Kt[t][1], q[3][0], q[3][1], oo[1], Vt[t][0], Vt[t][1], pB0, pB1, pA0, pA1, , , WPF(4 * t + 2), WPF(4 * t + 3) if (t == 15) PUTO(0));
          STEP(sB, sA, Kt[tn][0], Kt[tn][1], q[0][0], q[0][1], oo[2], Vt[t][0], Vt[t][1], pA0, pA1, pB0, pB1, , ,
               if (t + 3 < 16) Kt[(t + 3) & 15][0] = LDC(lo, base + (t + 3) * 4 * KB), if (t + 3 < 16) Kt[(t + 3) & 15][1] = LDC(lo + KB, base + (t + 3) * 4 * KB); if (t == 15) PUTO(1));
        }
        C2(pB1, 2, eb) asm volatile("s_nop 1");
        MO(oo[3], Vt[15][0], pB0); MO(oo[3], Vt[15][1], pB1);
        asm volatile("s_nop 3" : "+a"(oo[2])); SBAR();
        PUTO(2)
        STAMP(layer * 16 + 7);
        UNR for (int k = 0; k < 8; ++k) Wk[k] = LDW(wkb, k);
        NOP16A(oo[3]); SBAR();
        f32x16 o = oo[3];
        __syncthreads();
        UNR for (int s = 0; s < 3; ++s)
          UNR for (int e = 0; e < 4; ++e) { const f32x4 t = *(LAS f32x4*)(lds + L_C + (w * 3 + s) * 4 * KB + e * KB + lo);
            o[4 * e] += t.x; o[4 * e + 1] += t.y; o[4 * e + 2] += t.z; o[4 * e + 3] += t.w; }
        const float l = half_hi(o[12]);
        O_STORE(layer, o, l);
        const bool bad = !__all(l >= L_MIN);
        if (lane == 0) *(LAS unsigned*)(lds + L_MISC + w * 4) = bad ? 1u : 0u;
        STAMP(layer * 16 + 8);
        DRAIN();
        __syncthreads();
        const u32x4 bw = *(LAS u32x4*)(lds + L_MISC);
        if (__builtin_expect(attempt == 1 || __builtin_amdgcn_readfirstlane(bw.x | bw.y | bw.z | bw.w) == 0u, 1)) break;
        float mx[4] = {-INFINITY, -INFINITY, -INFINITY, -INFINITY};
        UNR for (int h = 0; h < 4; ++h) q[h][1].z = hh ? 0u : q[h][1].z;
#pragma unroll 1
        for (int t = 0; t < 16; ++t) {
          const u32x4 k0 = LDC(lo, base + t * 4 * KB), k1 = LDC(lo + KB, base + t * 4 * KB);
          UNR for (int h = 0; h < 4; ++h) { f32x16 s = {}; s = mfma32(k0, q[h][0], s); s = mfma32(k1, q[h][1], s);
            UNR for (int e = 0; e < 16; ++e) mx[h] = fmaxf(mx[h], s[e]); }
        }
        UNR for (int h = 0; h < 4; ++h) { const float m = half_max(mx[h]); if (hh == 0) *(LAS float*)(lds + L_MISC + 1024 + (w * 4 + ((w + 1 + h) & 3)) * 128 + r * 4) = m; }
        __syncthreads();
        UNR for (int h = 0; h < 4; ++h) { float m = -INFINITY;
          UNR for (int s = 0; s < 4; ++s) m = fmaxf(m, *(LAS float*)(lds + L_MISC + 1024 + (s * 4 + ((w + 1 + h) & 3)) * 128 + r * 4));
          q[h][1].z = hh ? (bf_ceil(m) ^ 0x8000u) : q[h][1].z; }
      }
      if (tid == 0) __hip_atomic_store(FLAG(blk * 4 + g, 4 + layer), tag, RLX);
      Z_PHASE(layer, false,
              UNR for (int k = 8; k < 18; ++k) Wk[k] = LDW(wkb, k),
              if ((k & 1) && k < 20) Wk[18 + (k >> 1)] = LDW(wkb, 18 + (k >> 1)); else if ((k & 1) && k < 24) WqB[(k >> 1) - 10] = LDW(wbB, (k >> 1) - 10));
    }
    {
      const int layer = 2 * pair + 1;
      IDX();
      const int wbB = WB_OFF + head * 3 * 28 * KB;
      UNR for (int k = 2; k < 10; ++k) WqB[k] = LDW(wbB, k);
      int woz = WOB_OFF + tlc * 32 * KB; OPQ(woz);
      f32x16 csqB, cskB; LDCS(csqB, head * 28); LDCS(cskB, 448 + head * 28);
      const float csv = __uint_as_float(__builtin_amdgcn_raw_buffer_load_b32(rs, r * 4, CS_OFF + (896 + head * 28) * 4, 16));
      SBAR();
      X_GATHER(layer);
      float mu, rstd, qn;
      u32x4 K[9][2], V[9][2], q0, q1;
      unsigned kown;
      {
        const int dst = KVB_OFF + blk * KVB_BLK + head * 4 * KB;
        f32x16 av = {}, ak = {}, aq = {}; u32x4 xf[84]; f32x4 rs_[4], nm_[4];
        X_STATS_LD();
        UNR for (int k = 0; k < 6; ++k) xf[k] = XFRAG(k);
        SBAR();
        sfor<0, 84>([&](auto kc) __attribute__((always_inline)) { constexpr int k = decltype(kc)::value;
          if constexpr (k + 6 < 84) xf[k + 6] = XFRAG((k + 6) % 28);
          if constexpr (k % 5 == 1 || k % 5 == 3) { constexpr int i = (k / 5) * 2 + (k % 5 == 3);
            if constexpr (i < 18) WqB[10 + i] = LDW(wbB, 10 + i); else if constexpr (i < 23) WZ[i - 18] = LDW(woz, i - 18); else if constexpr (k > 72) WZ[i - 24] = LDW(woz, i - 24); }
          if constexpr (k < 28) av = mfma32(xf[k], Wv[k], av); else if constexpr (k < 56) ak = mfma32(Wk[k - 28], xf[k], ak); else aq = mfma32(WqB[k - 56], xf[k], aq);
          if constexpr (k == 1) X_STATS(mu, rstd);
          if constexpr (k == 8) { UNR for (int i = 0; i < 4; ++i) { rs_[i] = *(LAS f32x4*)(lds + L_MS + w * 256 + (8 * i + 4 * hh) * 4); nm_[i] = *(LAS f32x4*)(lds + L_MS + w * 256 + 128 + (8 * i + 4 * hh) * 4); } }
          if constexpr (k == 30) asm volatile("" : "+a"(av));
          if constexpr (k >= 30 && k < 34) { constexpr int i = k - 30;
            unsigned p0 = pk(fmaf(av[4 * i], rs_[i].x, nm_[i].x * csv), fmaf(av[4 * i + 1], rs_[i].y, nm_[i].y * csv));
            unsigned p1 = pk(fmaf(av[4 * i + 2], rs_[i].z, nm_[i].z * csv), fmaf(av[4 * i + 3], rs_[i].w, nm_[i].w * csv));
            const unsigned pad = r == 28 ? 0x3f803f80u : 0u;
            V[8][i >> 1][2 * (i & 1)] = r < 28 ? p0 : pad; V[8][i >> 1][2 * (i & 1) + 1] = r < 28 ? p1 : pad; }
          if constexpr (k == 34) { STC(V[8][0], lo + 2 * KB, dst); STC(V[8][1], lo + 3 * KB, dst); }
          if constexpr (k == 57) { asm volatile("" : "+a"(ak));
            float kn2; EPI_ALPHA(ak, cskB, mu, rstd, kn2);
            if (hh) ak[12] = 1.f;
            pack16(ak, K[8][0], K[8][1]);
            STC(K[8][0], lo, dst); STC(K[8][1], lo + KB, dst);
            kown = umax32(__float_as_uint(kn2));
            if (lane == 0) __builtin_amdgcn_raw_buffer_store_b32(kown, rs, 0, KMB_OFF + (blk * 16 + head) * 4, 16);
            STAMP(layer * 16 + 3); }
          SBAR();
          if constexpr (k == 72) { DRAIN(); __builtin_amdgcn_s_barrier();
            if (tid == 0) __hip_atomic_store(FLAG(blk * 4 + g, 8 + pair), tag, RLX);
            SBAR(); } });
        float n2; EPI_ALPHA(aq, csqB, mu, rstd * QSCALE, n2);
        pack16(aq, q0, q1);
        qn = __builtin_amdgcn_sqrtf(n2) * 1.02f;
      }
      STAMP(layer * 16 + 4);
      const bool pv = lane < 8 && blk - 8 + lane >= 0;
      if (pv) poll(FLAG((blk - 8 + lane) * 4 + g, 8 + pair), tag);
      AFTER_POLL();
      STAMP(layer * 16 + 5);
      unsigned kmx = __builtin_amdgcn_raw_buffer_load_b32(rs, pv ? (blk - 8 + lane) * 64 : blk * 64, KMB_OFF + head * 4, 16);
      const int kvb = KVB_OFF + (blk - 8) * KVB_BLK + head * 4 * KB;
#define SRCB(j) (blk - 8 + (j) < 0 ? ZERO_OFF : kvb + (j) * KVB_BLK)
#define LKB(j) { const int s_ = SRCB(j); K[j][0] = LDC(lo, s_); K[j][1] = LDC(lo + KB, s_); }
#define LVB(j) { const int s_ = SRCB(j); V[j][0] = LDC(lo + 2 * KB, s_); V[j][1] = LDC(lo + 3 * KB, s_); }
      bool first = true;
      f32x16 o; float l;
#pragma unroll 1
      for (int attempt = 0; attempt < 2; ++attempt) {
        LKB(7) LKB(6) LVB(7) LKB(5) LVB(6) LKB(4)
        SBAR();
        if (first) { kmx = __builtin_amdgcn_readfirstlane(umax16(pv ? kmx : kown));
          q1.z = hh ? (bf_ceil(qn * __builtin_amdgcn_sqrtf(__uint_as_float(kmx))) ^ 0x8000u) : q1.z; first = false; }
        STAMP(layer * 16 + 6);
        float z = 0.f; PIN(z); const unsigned zu = __float_as_uint(z);
        o = z;
        f32x16 sA, sB; u32x4 pA0 = zu, pA1 = zu, pB0 = zu, pB1 = zu; f32x4 ea, eb = z;
        PIN(q0); PIN(q1); PIN(K[8][0]); PIN(K[8][1]); PIN(V[8][0]); PIN(V[8][1]); PIN(pA0); PIN(pA1); PIN(pB0); PIN(pB1); PIN(eb); asm volatile("s_nop 1" : "+a"(o)); SBAR();
        MS0(sA, K[8][0], q0); MS1(sA, K[8][1], q1);
        NOP16V(sA); SBAR();
#define MASKT(S, keep) { UNR for (int e = 0; e < 16; ++e) { const int row = (e & 3) + 8 * (e >> 2) + 4 * hh; S[e] = (keep) ? S[e] : -INFINITY; } PIN(S); SBAR(); }
#define WZ2(k) { WZ[k] = LDW(woz, k); WZ[k + 1] = LDW(woz, k + 1); }
        MASKT(sA, row <= r)
        STEP(sA, sB, K[7][0], K[7][1], q0, q1, o, V[8][0], V[8][1], pB0, pB1, pA0, pA1, , , LVB(5), LKB(3));
        STEP(sB, sA, K[6][0], K[6][1], q0, q1, o, V[8][0], V[8][1], pA0, pA1, pB0, pB1, , , LVB(4), LKB(2));
        STEP(sA, sB, K[5][0], K[5][1], q0, q1, o, V[7][0], V[7][1], pB0, pB1, pA0, pA1, , , LVB(3), LKB(1));
        STEP(sB, sA, K[4][0], K[4][1], q0, q1, o, V[6][0], V[6][1], pA0, pA1, pB0, pB1, , , LVB(2), LKB(0));
        STEP(sA, sB, K[3][0], K[3][1], q0, q1, o, V[5][0], V[5][1], pB0, pB1, pA0, pA1, , , LVB(1), LVB(0));
        STEP(sB, sA, K[2][0], K[2][1], q0, q1, o, V[4][0], V[4][1], pA0, pA1, pB0, pB1, , , WZ2(10), );
        STEP(sA, sB, K[1][0], K[1][1], q0, q1, o, V[3][0], V[3][1], pB0, pB1, pA0, pA1, , , WZ2(12), WZ2(14));
        STEP(sB, sA, K[0][0], K[0][1], q0, q1, o, V[2][0], V[2][1], pA0, pA1, pB0, pB1, , , WZ2(16), WZ2(18));
        MASKT(sA, row > r)
        STEP(sA, sB, K[0][0], K[0][1], q0, q1, o, V[1][0], V[1][1], pB0, pB1, pA0, pA1, , , , );
        C2(pA1, 2, eb) asm volatile("s_nop 1");
        MO(o, V[0][0], pA0); MO(o, V[0][1], pA1);
        NOP16A(o); SBAR();
        l = half_hi(o[12]);
        if (__builtin_expect(attempt == 1 || __all(l >= L_MIN), 1)) break;
        q1.z = hh ? 0u : q1.z;
        float mx = -INFINITY;
        UNR for (int j = 8; j >= 0; --j) {
          f32x16 s = {}; s = mfma32(K[j][0], q0, s); s = mfma32(K[j][1], q1, s); float m_ = -INFINITY;
          UNR for (int e = 0; e < 16; ++e) { const int row = (e & 3) + 8 * (e >> 2) + 4 * hh; float v = s[e];
            if (j == 8) v = (row <= r) ? v : -INFINITY; if (j == 0) v = (row > r) ? v : -INFINITY; m_ = fmaxf(m_, v); }
          mx = (blk - 8 + j >= 0) ? fmaxf(mx, m_) : mx; SBAR(); }
        mx = half_max(mx);
        q1.z = hh ? (bf_ceil(mx) ^ 0x8000u) : q1.z;
      }
      STAMP(layer * 16 + 7);
      O_STORE(layer, o, l);
      STAMP(layer * 16 + 8);
      PUBLISH(4 + layer);
      int wkv = g < 2 ? WKV_OFF + (g * 2 + dh) * 14 * KB : WS_BYTES; OPQ(wkv);
      Z_PHASE(layer, true,
              UNR for (int k = 20; k < 32; ++k) WZ[k] = LDW(woz, k),
              if ((k & 1) && k < 28) W16[k >> 1] = LDW(wkv, k >> 1));
      cskv = (f32x4){0.f, 0.f, 0.f, 0.f};
      if (g == 0) { UNR for (int k = 0; k < 14; ++k) cskv = mfma16(W16[k], ones, cskv); }
      else if (g == 1) { UNR for (int k = 0; k < 14; ++k) cskv = mfma16(ones, W16[k], cskv); }
      SBAR();
      X_GATHER(layer + 1);
    }
  }
}

extern "C" void kernel_launch(void* const* d_in, const int* in_sizes, int n_in, void* d_out, int out_size, void* d_ws, size_t ws_size, hipStream_t stream) {
  (void)in_sizes; (void)n_in; (void)out_size; (void)ws_size;
  (void)hipFuncSetAttribute((const void*)main_kernel, hipFuncAttributeMaxDynamicSharedMemorySize, L_BYTES);
  main_kernel<<<256, 256, L_BYTES, stream>>>((const u16*)d_in[0], (const u16*)d_in[1], (const u16*)d_in[2], (const u16*)d_in[3], (const u16*)d_in[4],
                                             (const u16*)d_in[6], (const u16*)d_in[7], (unsigned char*)d_ws, (u16*)d_out);
}
```

```cpp
#include <hip/hip_runtime.h>
#include <hip/hip_bf16.h>

typedef float f32x16 __attribute__((ext_vector_type(16)));
typedef float f32x4 __attribute__((ext_vector_type(4)));
typedef float f32x2 __attribute__((ext_vector_type(2)));
typedef unsigned u32x4 __attribute__((ext_vector_type(4)));
typedef unsigned u32x2 __attribute__((ext_vector_type(2)));
typedef short bf16x8 __attribute__((ext_vector_type(8)));
typedef short s16x4 __attribute__((ext_vector_type(4)));
typedef __bf16 bf2 __attribute__((ext_vector_type(2)));
typedef unsigned short u16;
typedef unsigned long long u64;
#define GAS __attribute__((address_space(1)))
#define LAS __attribute__((address_space(3)))
typedef GAS u64 gu64;
#define RLX __ATOMIC_RELAXED, __HIP_MEMORY_SCOPE_AGENT
#define SBAR() __builtin_amdgcn_sched_barrier(0)
#define PIN(x) asm volatile("" : "+v"(x))
#define OPQ(x) asm volatile("" : "+s"(x))
#define DRAIN() asm volatile("s_waitcnt vmcnt(0)" ::: "memory")
#define UNR _Pragma("unroll")

constexpr int DM = 448, NB = 64, NTL = 14, KB = 1024;
constexpr int EP_OFF = 0;
constexpr int FA_OFF = 1 * KB;
constexpr int FL_OFF = FA_OFF + 2 * 8 * 4 * KB;
constexpr int KM_OFF = FL_OFF + 256 * 128;
constexpr int KMB_OFF = KM_OFF + 2 * KB;
constexpr int CS_OFF = KMB_OFF + 4 * KB;
constexpr int ZERO_OFF = CS_OFF + 6 * KB;
constexpr int WKV_OFF = ZERO_OFF + 4 * KB;
constexpr int KVA_OFF = WKV_OFF + 56 * KB;
constexpr int KVA_L = NB * 4 * KB;
constexpr int WOA_OFF = KVA_OFF + 2 * KVA_L;
constexpr int WOB_OFF = WOA_OFF + 14 * 32 * KB;
constexpr int WB_OFF = WOB_OFF + 14 * 32 * KB;
constexpr int XT_OFF = WB_OFF + 16 * 3 * 28 * KB;
constexpr int XT_WG = 9 * KB, XT_BLK = 4 * XT_WG, XT_L = NB * XT_BLK;
constexpr int OT_OFF = XT_OFF + 2 * XT_L;
constexpr int OT_BLK = 32 * KB, OT_L = NB * OT_BLK;
constexpr int KVB_OFF = OT_OFF + 2 * OT_L;
constexpr int KVB_BLK = 64 * KB;
constexpr int WS_BYTES = KVB_OFF + NB * KVB_BLK;
#define FLAG(wg, word) (flags + (FL_OFF + (wg) * 128) / 8 + (word))
constexpr int L_X = 0;
constexpr int L_O = 0, L_C = 0;
constexpr int L_MISC = 48 * KB;
constexpr int L_Q = 52 * KB;
constexpr int L_RK = 36 * KB;
constexpr int L_RQ = L_RK + 448 * 56;
constexpr int L_MS = 159 * KB;
constexpr int L_BYTES = 160 * KB;
static_assert(L_RQ + 448 * 224 + 64 <= L_MS && L_RQ % 16 == 0, "lds");

#define STAMP(n) do {} while (0)
constexpr float QSCALE = 0.18898223650461361f * 1.4426950408889634f;
constexpr unsigned SPIN_CAP = 1u << 16;
#define POLL_SLEEP 4
constexpr float L_MIN = 1e-18f;

__device__ __forceinline__ unsigned pk(float a, float b) { f32x2 v = {a, b}; bf2 r = __builtin_convertvector(v, bf2); return __builtin_bit_cast(unsigned, r); }
__device__ __forceinline__ float bflo(unsigned u) { return __uint_as_float(u << 16); }
__device__ __forceinline__ float bfhi(unsigned u) { return __uint_as_float(u & 0xffff0000u); }
__device__ __forceinline__ f32x16 mfma32(u32x4 a, u32x4 b, f32x16 c) {
  return __builtin_amdgcn_mfma_f32_32x32x16_bf16(__builtin_bit_cast(bf16x8, a), __builtin_bit_cast(bf16x8, b), c, 0, 0, 0); }
__device__ __forceinline__ f32x4 mfma16(u32x4 a, u32x4 b, f32x4 c) {
  return __builtin_amdgcn_mfma_f32_16x16x32_bf16(__builtin_bit_cast(bf16x8, a), __builtin_bit_cast(bf16x8, b), c, 0, 0, 0); }
__device__ __forceinline__ float half_sum(float v) { auto r = __builtin_amdgcn_permlane32_swap(__float_as_uint(v), __float_as_uint(v), false, false); return __uint_as_float(r[0]) + __uint_as_float(r[1]); }
__device__ __forceinline__ float half_max(float v) { auto r = __builtin_amdgcn_permlane32_swap(__float_as_uint(v), __float_as_uint(v), false, false); return fmaxf(__uint_as_float(r[0]), __uint_as_float(r[1])); }
__device__ __forceinline__ float half_hi(float v) { auto r = __builtin_amdgcn_permlane32_swap(__float_as_uint(v), __float_as_uint(v), false, false); return __uint_as_float(r[1]); }
__device__ __forceinline__ float row_sum(float v) { auto r = __builtin_amdgcn_permlane16_swap(__float_as_uint(v), __float_as_uint(v), false, false); return __uint_as_float(r[0]) + __uint_as_float(r[1]); }
template <int C> __device__ __forceinline__ float dppf(float v) { return __uint_as_float(__builtin_amdgcn_update_dpp(0u, __float_as_uint(v), C, 0xf, 0xf, true)); }
__device__ __forceinline__ float sum8(float v) { v += dppf<0xb1>(v); v += dppf<0x4e>(v); return v + dppf<0x141>(v); }
template <int N> __device__ __forceinline__ unsigned ror(unsigned v) { return __builtin_amdgcn_update_dpp(0u, v, 0x120 + N, 0xf, 0xf, true); }
__device__ __forceinline__ unsigned umax(unsigned a, unsigned b) { return a > b ? a : b; }
__device__ __forceinline__ unsigned umax16(unsigned v) { v = umax(v, ror<1>(v)); v = umax(v, ror<2>(v)); v = umax(v, ror<4>(v)); return umax(v, ror<8>(v)); }
__device__ __forceinline__ unsigned umax32(unsigned v) { v = umax16(v); auto r = __builtin_amdgcn_permlane16_swap(v, v, false, false); return umax(r[0], r[1]); }
__device__ __forceinline__ unsigned umax64(unsigned v) { v = umax32(v); auto r = __builtin_amdgcn_permlane32_swap(v, v, false, false); return umax(r[0], r[1]); }
__device__ __forceinline__ unsigned bf_ceil(float v) { unsigned u = __float_as_uint(v); unsigned t = u >> 16; return (v > 0.f && (u & 0xffffu)) ? t + 1u : t; }
__device__ __forceinline__ void pack16(const f32x16& v, u32x4& f0, u32x4& f1) {
  f0 = (u32x4){pk(v[0], v[1]), pk(v[2], v[3]), pk(v[4], v[5]), pk(v[6], v[7])};
  f1 = (u32x4){pk(v[8], v[9]), pk(v[10], v[11]), pk(v[12], v[13]), pk(v[14], v[15])}; }
__device__ __forceinline__ void unpack16(const u32x4 f0, const u32x4 f1, f32x16& v) {
  UNR for (int i = 0; i < 4; ++i) { v[2 * i] = bflo(f0[i]); v[2 * i + 1] = bfhi(f0[i]); v[8 + 2 * i] = bflo(f1[i]); v[9 + 2 * i] = bfhi(f1[i]); } }
__device__ __forceinline__ void tile_stats(const f32x16& v, float& mean, float& m2) {
  const float s = (((v[0] + v[1]) + (v[2] + v[3])) + ((v[4] + v[5]) + (v[6] + v[7]))) + (((v[8] + v[9]) + (v[10] + v[11])) + ((v[12] + v[13]) + (v[14] + v[15])));
  mean = half_sum(s) * (1.f / 32.f);
  float q0 = 0.f, q1 = 0.f;
  UNR for (int i = 0; i < 8; ++i) { const float d0 = v[i] - mean, d1 = v[8 + i] - mean; q0 = fmaf(d0, d0, q0); q1 = fmaf(d1, d1, q1); }
  m2 = half_sum(q0 + q1);
}
template <int V> struct IC { static constexpr int value = V; };
template <int I, int N, class F> __device__ __forceinline__ void sfor(F&& f) { if constexpr (I < N) { f(IC<I>{}); sfor<I + 1, N>(f); } }
__device__ __forceinline__ u32x4 mkdesc(const void* p, unsigned bytes) { return (u32x4){(unsigned)(size_t)p, (unsigned)((size_t)p >> 32) & 0xffffu, bytes, 0x00020000u}; }

#define LDC(voff, soff) __builtin_amdgcn_raw_buffer_load_b128(rs, (voff), (soff), 16)
#define STC(v, voff, soff) __builtin_amdgcn_raw_buffer_store_b128((v), rs, (voff), (soff), 16)
#define LDS128(off) (*(LAS u32x4*)(lds + (off)))
#define LDW(base, k) LDC(lo + ((k) & 3) * KB, (base) + ((k) >> 2) * 4 * KB)
#define MS0(S, A, B) asm volatile("v_mfma_f32_32x32x16_bf16 %0, %1, %2, 0" : "=&v"(S) : "v"(A), "v"(B))
#define MS1(S, A, B) asm volatile("v_mfma_f32_32x32x16_bf16 %0, %1, %2, %0" : "+v"(S) : "v"(A), "v"(B))
#define MO(O, A, B) asm volatile("v_mfma_f32_32x32x16_bf16 %0, %1, %2, %0" : "+a"(O) : "v"(A), "v"(B))
#define EX(x) __builtin_amdgcn_exp2f(x)
#define E4(E, S, i) { E[0] = EX(S[i]); E[1] = EX(S[i + 1]); E[2] = EX(S[i + 2]); E[3] = EX(S[i + 3]); PIN(E); SBAR(); }
#define C2(P, j, E) { P[j] = pk(E[0], E[1]); P[j + 1] = pk(E[2], E[3]); PIN(P); SBAR(); }
#define STEP(SC, SN, KN0, KN1, QN0, QN1, OP, VP0, VP1, PP0, PP1, PC0, PC1, W0, W2, LDA, LDB) \
  W0; MS0(SN, KN0, QN0); SBAR(); E4(ea, SC, 0) C2(PP1, 2, eb) \
  MS1(SN, KN1, QN1); LDA; SBAR(); E4(eb, SC, 4) C2(PC0, 0, ea) \
  W2; MO(OP, VP0, PP0); SBAR(); E4(ea, SC, 8) C2(PC0, 2, eb) \
  MO(OP, VP1, PP1); LDB; SBAR(); E4(eb, SC, 12) C2(PC1, 0, ea)
#define NOP16V(x) asm volatile("s_nop 7\n\ts_nop 7" : "+v"(x))
#define NOP16A(x) asm volatile("s_nop 7\n\ts_nop 7" : "+a"(x))
#define VMW(n) asm volatile("s_waitcnt vmcnt(" #n ")")
#define ALD(dst, soff, imm) asm volatile("buffer_load_dwordx4 %0, %1, %2, %3 offen offset:" #imm " sc1" : "+v"(dst) : "v"(lo), "s"(desc), "s"(soff))
#define ALD0(dst, soff, imm) asm volatile("buffer_load_dwordx4 %0, %1, %2, %3 offen offset:" #imm " sc1" : "=v"(dst) : "v"(lo), "s"(desc), "s"(soff))
#define ALDA(dst, soff, imm) asm volatile("buffer_load_dwordx4 %0, %1, %2, %3 offen offset:%4 sc1" : "=a"(dst) : "v"(lo), "s"(desc), "s"(soff), "i"(imm))
#define LDWA(dst, base, k) { const int so_ = (base) + ((k) >> 2) * 4 * KB; ALDA(dst, so_, ((k) & 3) * KB); }
#define ADMA(ldsaddr, voff, dsc, soff) asm volatile("s_mov_b32 m0, %0\n\ts_nop 0\n\tbuffer_load_dwordx4 %1, %2, %3 offen lds" :: "s"(ldsaddr), "v"(voff), "s"(dsc), "s"(soff) : "memory")
#define DMAW(base, k) { const unsigned la_ = L_W + w * L_WW + (k) * KB; const int so_ = (base) + (k) * KB; ADMA(la_, lo, desc, so_); }
#define TRRD(off) __builtin_bit_cast(u32x2, __builtin_amdgcn_ds_read_tr16_b64_v4i16((LAS s16x4*)(lds + (off))))

__device__ __forceinline__ void poll(gu64* p, u64 tag) {
  unsigned spins = 0;
  while (__hip_atomic_load(p, RLX) != tag) { if (++spins > SPIN_CAP) break; }
}
#define AFTER_POLL() __builtin_amdgcn_fence(__ATOMIC_ACQUIRE, "wavefront")

__global__ __launch_bounds__(256, 1) void main_kernel(const u16* __restrict__ x, const u16* __restrict__ wq, const u16* __restrict__ wk, const u16* __restrict__ wv,
                                                      const u16* __restrict__ woa, const u16* __restrict__ wb, const u16* __restrict__ wob,
                                                      unsigned char* __restrict__ ws, u16* __restrict__ out) {
  extern __shared__ __attribute__((aligned(16))) unsigned char smem[];
  const __amdgpu_buffer_rsrc_t rs = __builtin_amdgcn_make_buffer_rsrc((void*)ws, 0, WS_BYTES, 0x00020000);
  const u32x4 desc = mkdesc(ws, WS_BYTES);
  LAS unsigned char* lds = (LAS unsigned char*)smem;
  gu64* flags = (gu64*)ws;
  int tid_ = threadIdx.x, b_ = blockIdx.x, w_ = __builtin_amdgcn_readfirstlane(threadIdx.x >> 6);
#define IDX() asm volatile("" : "+v"(tid_)); asm volatile("" : "+s"(b_), "+s"(w_)); \
  const int tid = tid_, b = b_, w = w_, lane = tid & 63, r = lane & 31, hh = lane >> 5, lo = lane * 16; \
  const int blk = (b & 7) * 8 + (b >> 5), g = (b >> 3) & 3, head = 4 * g + w, tl = head; const bool act = tl < NTL; const int tlc = act ? tl : 0; \
  const int dh = w & 1, th = w >> 1; (void)r; (void)hh; (void)dh; (void)th; (void)tlc; (void)blk; (void)lo
  u64 pc_; unsigned len_;
  asm volatile("s_getpc_b64 %0\n.Lcode_here:\n\ts_mov_b32 %1, .Lcode_last-.Lcode_here" : "=s"(pc_), "=s"(len_));
#define CODEPF(ldsoff) { const u32x4 dc = mkdesc((const void*)pc_, len_); const int vo = ((b >> 3) * 4 + w) * 512 + (lane & 3) * 128; const unsigned la = (ldsoff) + w * 256; \
    asm volatile("s_mov_b32 m0, %0\n\ts_nop 0\n\tbuffer_load_dword %1, %2, 0 offen lds" :: "s"(la), "v"(vo), "s"(dc) : "memory"); }
  STAMP(64);

  f32x16 xres, csq;
  f32x4 cskv;
  u32x4 W16[14];
  u64 tag;
  const u32x4 ones = {0x3f803f80u, 0x3f803f80u, 0x3f803f80u, 0x3f803f80u};
  {
    IDX();
    u64 ep; asm volatile("global_load_dwordx2 %0, %1, %2 sc1" : "=v"(ep) : "v"(0), "s"(ws));
    u32x4 xr[7];
    { const u32x4 dx = mkdesc(x + 32 * blk * DM, 32 * 896); const int vo = (8 * w + (lane >> 3)) * 896 + (lane & 7) * 16;
      UNR for (int f = 0; f < 7; ++f) asm volatile("buffer_load_dwordx4 %0, %1, %2, 0 offen offset:%3" : "=v"(xr[f]) : "v"(vo), "s"(dx), "i"(f * 128)); }
    CODEPF(L_X + 31 * KB);
    const int n = blk * 2 + (g & 1), nw = n - 84;
    const int c3 = (n * 37) >> 10, ks = n - 28 * c3;
    const int t7 = tid & 127;
    u32x4 cl[7]; int vq[7];
    const u32x4 dq = mkdesc(wq, 448 * 448 * 2);
#define WQDMA(u0) { int u = (u0) + (blk & 7); u = u >= 7 ? u - 7 : u; UNR for (int k = 0; k < 7; ++k) { \
      const unsigned la = L_RQ + (2 * (7 * u + k) + dh) * KB; const int so = u * 64 * 896; ADMA(la, vq[k], dq, so); } }
    if (w >= 2) {
      UNR for (int k = 0; k < 7; ++k) { const int c = t7 + 128 * k, row = (c * 4682) >> 16; vq[k] = row * 896 + (c - row * 14) * 16 + g * 224; }
      WQDMA(0) WQDMA(1)
    } else if (g < 2 || n >= 84) { const u32x4 dk = g < 2 ? mkdesc(g ? wv : wk, 448 * 56) : mkdesc((nw & 16 ? wob : woa) + (nw & 15) * 28 * 448, n < 116 ? 448 * 56 : 0);
      UNR for (int k = 0; k < 14; ++k) { const int i = 2 * k + w; const unsigned la = i < 24 ? L_RK + i * KB : L_X + 32 * KB; const int so = i < 24 ? i * KB : 448 * 56;
        if (k == 12) { const unsigned l2 = L_RK + 24 * KB; const int s2 = 24 * KB; if (lane < 32) ADMA(l2, lo, dk, s2); } else ADMA(la, lo, dk, so); }
    } else { const u32x4 dk = mkdesc(wb + 16 * ks * 1344 + 448 * c3, 16 * 2688);
      const u32x4 db = mkdesc(wb, 448 * 2688); const int vo = (t7 >> 1) * 2688 + (t7 & 1) * 16;
      UNR for (int j = 0; j < 7; ++j) { const int so = j * 64 * 2688 + n * 32; asm volatile("buffer_load_dwordx4 %0, %1, %2, %3 offen" : "=v"(cl[j]) : "v"(vo), "s"(db), "s"(so)); }
      UNR for (int k = 0; k < 7; ++k) { const int c = t7 + 128 * k, row = (c * 1171) >> 16; const unsigned la = L_RK + (2 * k + w) * KB;
        ADMA(la, row * 2688 + (c - row * 56) * 16, dk, 0); } }
    STAMP(67);
    asm volatile("s_waitcnt vmcnt(15)" : "+v"(xr[0]), "+v"(xr[1]), "+v"(xr[2]), "+v"(xr[3]), "+v"(xr[4]), "+v"(xr[5]), "+v"(xr[6]), "+v"(ep) :: "memory");
    { f32x2 v[28]; f32x2 s = {0.f, 0.f};
      UNR for (int f = 0; f < 7; ++f) UNR for (int c = 0; c < 4; ++c) { v[4 * f + c] = (f32x2){bflo(xr[f][c]), bfhi(xr[f][c])}; s += v[4 * f + c]; }
      const float mu = sum8(s.x + s.y) * (1.f / 448.f);
      s = (f32x2){0.f, 0.f};
      UNR for (int i = 0; i < 28; ++i) { v[i] -= mu; s += v[i] * v[i]; }
      const float rstd = __builtin_amdgcn_rsqf(sum8(s.x + s.y) * (1.f / 448.f) + 1e-5f);
      const int c = lane & 7;
      UNR for (int f = 0; f < 7; ++f) {
        u32x4 o; UNR for (int e = 0; e < 4; ++e) { const f32x2 t = v[4 * f + e] * rstd; o[e] = pk(t.x, t.y); }
        LDS128(L_X + (f >> 1) * XT_WG + (f & 1) * 4 * KB + (c >> 1) * KB + (8 * w + (lane >> 3) + 32 * (c & 1)) * 16) = o; }
      const float one = 32.f * (1.f - 1e-5f);
      *(LAS f32x4*)(lds + L_X + w * XT_WG + 8192 + lo) = (f32x4){0.f, one, 0.f, one};
    }
    tag = (((u64)(unsigned)__builtin_amdgcn_readfirstlane((unsigned)(ep >> 32)) << 32) | (unsigned)__builtin_amdgcn_readfirstlane((unsigned)ep)) + 1ull;
    if (w < 2) {
      asm volatile("s_waitcnt vmcnt(0)" : "+v"(cl[0]), "+v"(cl[1]), "+v"(cl[2]), "+v"(cl[3]), "+v"(cl[4]), "+v"(cl[5]), "+v"(cl[6]) :: "memory");
      if (g >= 2 && n < 84) { f32x2 s[4];
        UNR for (int c = 0; c < 4; ++c) { s[c] = (f32x2){0.f, 0.f}; UNR for (int j = 0; j < 7; ++j) s[c] += (f32x2){bflo(cl[j][c]), bfhi(cl[j][c])}; }
        *(LAS f32x4*)(lds + L_RK + 15 * KB + t7 * 32) = (f32x4){s[0].x, s[0].y, s[1].x, s[1].y}; *(LAS f32x4*)(lds + L_RK + 15 * KB + t7 * 32 + 16) = (f32x4){s[2].x, s[2].y, s[3].x, s[3].y}; } }
    STAMP(68);
    asm volatile("s_waitcnt lgkmcnt(0)\n\ts_barrier" ::: "memory");
    STAMP(65);
    const int q4 = (lane & 15) >> 2, p4 = lane & 3, gq = lane >> 4;
    cskv = (f32x4){0.f, 0.f, 0.f, 0.f}; csq = (f32x16){};
    if (w >= 2) {
      WQDMA(2) WQDMA(3) WQDMA(4) WQDMA(5) WQDMA(6)
      {
        const int vo = lane < 11 ? ((b * 2 + dh) * 11 + lane) * 128 + KB : lane * 224 * KB; const unsigned la = L_X + 33 * KB + w * 256;
        asm volatile("s_mov_b32 m0, %0\n\ts_nop 0\n\tbuffer_load_dword %1, %2, 0 offen sc1 lds" :: "s"(la), "v"(vo), "s"(desc) : "memory"); }
    } else if (g < 2) { const int to = L_RK + (8 * gq + q4) * 56 + dh * 32 + p4 * 8;
      UNR for (int k = 0; k < 14; ++k) { const u32x2 a = TRRD(to + k * 32 * 56), c = TRRD(to + k * 32 * 56 + 4 * 56); W16[k] = (u32x4){a.x, a.y, c.x, c.y}; }
    } else {
      const int to = L_RK + (4 * hh + q4) * 896 + (gq & 1) * 32 + p4 * 8;
      if (n < 84) {
        UNR for (int j = 0; j < 8; ++j) { const int h = 8 * w + j; const u32x2 a = TRRD(to + h * 56), c = TRRD(to + h * 56 + 8 * 896);
          STC(((u32x4){a.x, a.y, c.x, c.y}), lo, WB_OFF + ((h * 3 + c3) * 28 + ks) * KB); }
        if (w == 0) { float s = 0.f;
          UNR for (int j = 0; j < 16; ++j) s += *(LAS float*)(lds + L_RK + 15 * KB + (j * 4 + gq) * 64 + (lane & 15) * 4);
          s = half_sum(row_sum(s));
          if (lane < 16) __builtin_amdgcn_raw_buffer_store_b32(__float_as_uint(s), rs, lane * 4, CS_OFF + n * 64, 16); }
      } else if (n < 116) {
        UNR for (int j = 0; j < 7; ++j) { const int t = 2 * j + w;
          const u32x2 a = TRRD(to + t * 64), c = TRRD(to + t * 64 + 8 * 896), d = TRRD(to + t * 64 + 16 * 896), e = TRRD(to + t * 64 + 24 * 896);
          const int dst = (nw & 16 ? WOB_OFF : WOA_OFF) + (t * 32 + 2 * (nw & 15)) * KB;
          STC(((u32x4){a.x, a.y, c.x, c.y}), lo, dst); STC(((u32x4){d.x, d.y, hh ? 0u : e.x, hh ? 0u : e.y}), lo + KB, dst); }
      } else if (n == 116) { STC(((u32x4){0u, 0u, 0u, 0u}), lo, ZERO_OFF + w * 2 * KB); STC(((u32x4){0u, 0u, 0u, 0u}), lo + KB, ZERO_OFF + w * 2 * KB); }
    }
    STAMP(66);
  }

#define GATHER(N, src, ldsoff) { u32x4 t_[N]; unsigned spins_ = 0; \
    for (;;) { asm volatile("" ::: "memory"); unsigned m_ = 0u; \
      UNR for (int f = 0; f < N; ++f) t_[f] = LDW(src, f); \
      UNR for (int f = 0; f < N; ++f) { LDS128((ldsoff) + f * KB + lo) = t_[f]; m_ = umax(umax(m_, t_[f].x), t_[f].y); m_ = umax(umax(m_, t_[f].z), t_[f].w); } \
      if (__builtin_expect(__all(m_ != ~0u) || ++spins_ > SPIN_CAP, 1)) break; } \
    __syncthreads(); }
#define SANE(f) { UNR for (int e_ = 0; e_ < 4; ++e_) f[e_] = f[e_] < 0xfffffffeu ? f[e_] : 0xfffffffeu; }
#define HINT(word) { __builtin_amdgcn_s_barrier(); if (tid == 0) __hip_atomic_store(FLAG(blk * 4 + g, (word)), tag, RLX); }
#define ARM(t) { const u32x4 a_ = {~0u, ~0u, ~0u, ~0u}; \
    if ((t) < 4) STC(a_, lo + ((t) & 1) * KB, armo + ((t) >> 1) * OT_L); \
    else if ((t) < 10) { const int d_ = (t) < 7 ? armx + XT_L : armx0; \
      if (((t) - 4) % 3 < 2) STC(a_, lo + (((t) - 4) % 3) * KB, d_ + w * 2048); else __builtin_amdgcn_raw_buffer_store_b32(~0u, rs, lane * 4, d_ + 8192 + w * 256, 16); } }
#define X_GATHER(layer) { \
    STAMP((layer) * 16 + 0); \
    poll(FLAG(blk * 4 + w, (layer)), tag); AFTER_POLL(); \
    STAMP((layer) * 16 + 1); \
    int src_ = XT_OFF + ((layer) & 1) * XT_L + blk * XT_BLK + w * XT_WG; OPQ(src_); \
    GATHER(9, src_, L_X + w * XT_WG) \
    STAMP((layer) * 16 + 2); }
#define X_STATS_LD() f32x2 st_[7]; \
    UNR for (int k = 0; k < 7; ++k) st_[k] = *(LAS f32x2*)(lds + L_X + (k >> 1) * XT_WG + 8192 + (k & 1) * 512 + lane * 8)
#define X_STATS(mu, rstd) { \
    mu = half_sum(((st_[0].x + st_[1].x) + (st_[2].x + st_[3].x)) + ((st_[4].x + st_[5].x) + st_[6].x)) * (1.f / 14.f); \
    float q_ = 0.f, q2_ = 0.f; \
    UNR for (int k = 0; k < 7; ++k) { const float d = st_[k].x - mu; q_ = fmaf(d, d, q_); q2_ += st_[k].y; } \
    rstd = __builtin_amdgcn_rsqf(half_sum(fmaf(32.f, q_, q2_)) * (1.f / 448.f) + 1e-5f); \
    *(LAS float*)(lds + L_MS + w * 256 + lane * 4) = hh ? -mu * rstd : rstd; }
#define XFRAG(ks) LDS128(L_X + ((ks) >> 3) * XT_WG + ((ks) & 7) * KB + lo)
#define LDCS(cs, col) { int o_ = CS_OFF + (col) * 4; OPQ(o_); UNR for (int q4 = 0; q4 < 4; ++q4) { const f32x4 c_ = __builtin_bit_cast(f32x4, LDC(hh * 16 + q4 * 32, o_)); \
    cs[4 * q4] = c_.x; cs[4 * q4 + 1] = c_.y; cs[4 * q4 + 2] = c_.z; cs[4 * q4 + 3] = c_.w; } }
#define EPI_ALPHA(a, cs, mu, sc, n2) { const float nm_ = -(mu) * (sc); UNR for (int e = 0; e < 16; ++e) a[e] = fmaf(a[e], (sc), nm_ * cs[e]); \
    UNR for (int e = 12; e < 16; ++e) a[e] = hh ? 0.f : a[e]; \
    float n0_ = 0.f, n1_ = 0.f; UNR for (int e = 0; e < 8; ++e) { n0_ = fmaf(a[e], a[e], n0_); n1_ = fmaf(a[8 + e], a[8 + e], n1_); } n2 = half_sum(n0_ + n1_); }
#define PUBLISH(word) { DRAIN(); __syncthreads(); if (tid == 0) __hip_atomic_store(FLAG(blk * 4 + g, (word)), tag, RLX); }
#define O_STORE(layer, o, l) { const float inv_ = __builtin_amdgcn_rcpf(l); f32x16 v_; \
    UNR for (int e = 0; e < 16; ++e) v_[e] = o[e] * inv_; \
    u32x4 f0_, f1_; pack16(v_, f0_, f1_); if (hh) { f1_.z = 0u; f1_.w = 0u; } SANE(f0_) SANE(f1_) \
    const int dst_ = OT_OFF + ((layer) & 1) * OT_L + blk * OT_BLK + head * 2 * KB; STC(f0_, lo, dst_); STC(f1_, lo + KB, dst_); }
#define Z_PHASE(layer, LAST, PRE, SIDE) { \
    STAMP((layer) * 16 + 9); \
    PRE; \
    SBAR(); \
    STAMP((layer) * 16 + 10); \
    poll(FLAG(blk * 4 + w, 4 + (layer)), tag); AFTER_POLL(); \
    STAMP((layer) * 16 + 11); \
    int src_ = OT_OFF + ((layer) & 1) * OT_L + blk * OT_BLK + w * 8 * KB; OPQ(src_); \
    GATHER(8, src_, L_O + w * 8 * KB) \
    STAMP((layer) * 16 + 12); \
    u32x4 of_[32]; \
    UNR for (int k = 0; k < 6; ++k) of_[k] = LDS128(L_O + k * KB + lo); \
    SBAR(); \
    UNR for (int k = 0; k < 32; ++k) { if (k + 6 < 32) of_[k + 6] = LDS128(L_O + (k + 6) * KB + lo); SIDE; xres = mfma32(WZ[k], of_[k], xres); SBAR(); } \
    u32x4 f0, f1; pack16(xres, f0, f1); \
    STAMP((layer) * 16 + 13); \
    if (LAST && (layer) == 3) { \
      if (act) { u16* p = out + (32 * blk + r) * DM + 32 * tl + 4 * hh; \
        *(u32x2*)(p) = (u32x2){f0.x, f0.y}; *(u32x2*)(p + 8) = (u32x2){f0.z, f0.w}; *(u32x2*)(p + 16) = (u32x2){f1.x, f1.y}; *(u32x2*)(p + 24) = (u32x2){f1.z, f1.w}; } \
      if constexpr (LAST) asm volatile(".Lcode_last:"); \
      __builtin_amdgcn_endpgm(); \
    } \
    const int dst = XT_OFF + (((layer) + 1) & 1) * XT_L + blk * XT_BLK + g * XT_WG; \
    SANE(f0) SANE(f1) STC(f0, lo, dst + w * 2048); STC(f1, lo + KB, dst + w * 2048); \
    float mean, m2; tile_stats(xres, mean, m2); \
    { const unsigned sv = __float_as_uint(hh ? m2 : mean); __builtin_amdgcn_raw_buffer_store_b32(sv < 0xfffffffeu ? sv : 0xfffffffeu, rs, r * 8 + hh * 4, dst + 8192 + w * 256, 16); } \
    STAMP((layer) * 16 + 14); \
    HINT((layer) + 1); \
    STAMP((layer) * 16 + 15); }

#pragma unroll 1
  for (int pair = 0;; ++pair) {
    u32x4 WZ[32], Wv[28], Wk[28], WqB[28];
    {
      const int layer = 2 * pair;
      IDX();
      const int wbB = WB_OFF + head * 3 * 28 * KB;
      float mu, rstd;
      X_STATS_LD();
      const int G = lane >> 4, cc = lane & 15;
      const bool role = g < 2 && w < 2;
      if (role) {
        const int xo = L_X + (G >> 1) * KB + (cc + 32 * (G & 1)) * 16;
        u32x4 xf[2][14];
        UNR for (int k = 0; k < 14; ++k) { xf[0][k] = LDS128(xo + (k >> 2) * XT_WG + (k & 3) * 2048); xf[1][k] = LDS128(xo + (k >> 2) * XT_WG + (k & 3) * 2048 + 256); }
        X_STATS(mu, rstd);
        f32x4 a[2] = {{0.f, 0.f, 0.f, 0.f}, {0.f, 0.f, 0.f, 0.f}};
        const int kvdst = KVA_OFF + pair * KVA_L + blk * 4 * KB;
        if (g == 0) {
          float rs_[2], nm_[2];
          UNR for (int t = 0; t < 2; ++t) { rs_[t] = *(LAS float*)(lds + L_MS + w * 256 + (16 * t + cc) * 4); nm_[t] = *(LAS float*)(lds + L_MS + w * 256 + 128 + (16 * t + cc) * 4); }
          UNR for (int k = 0; k < 14; ++k) { a[0] = mfma16(W16[k], xf[0][k], a[0]); a[1] = mfma16(W16[k], xf[1][k], a[1]); }
          const bool pad = dh == 1 && G == 3; float n2[2];
          UNR for (int t = 0; t < 2; ++t) { n2[t] = 0.f;
            UNR for (int e = 0; e < 4; ++e) { a[t][e] = pad ? 0.f : fmaf(a[t][e], rs_[t], nm_[t] * cskv[e]); n2[t] = fmaf(a[t][e], a[t][e], n2[t]); }
            a[t][0] = pad ? 1.f : a[t][0];
            __builtin_amdgcn_raw_buffer_store_b64((u32x2){pk(a[t][0], a[t][1]), pk(a[t][2], a[t][3])}, rs, dh * KB + (16 * t + cc + 32 * (G & 1)) * 16 + 8 * (G >> 1), kvdst, 16); }
          const unsigned n0 = umax16(__float_as_uint(row_sum(half_sum(n2[0])))), n1 = umax16(__float_as_uint(row_sum(half_sum(n2[1]))));
          if (lane < 2) __builtin_amdgcn_raw_buffer_store_b32(lane ? n1 : n0, rs, lane * 8, KM_OFF + (pair * 64 + blk) * 16 + dh * 4, 16);
        } else {
          f32x4 rs_[2], nm_[2];
          UNR for (int t = 0; t < 2; ++t) { rs_[t] = *(LAS f32x4*)(lds + L_MS + w * 256 + (16 * t + 4 * G) * 4); nm_[t] = *(LAS f32x4*)(lds + L_MS + w * 256 + 128 + (16 * t + 4 * G) * 4); }
          UNR for (int k = 0; k < 14; ++k) { a[0] = mfma16(xf[0][k], W16[k], a[0]); a[1] = mfma16(xf[1][k], W16[k], a[1]); }
          UNR for (int t = 0; t < 2; ++t) {
            UNR for (int e = 0; e < 4; ++e) a[t][e] = fmaf(a[t][e], rs_[t][e], nm_[t][e] * cskv.x);
            u32x2 o2 = {pk(a[t][0], a[t][1]), pk(a[t][2], a[t][3])};
            if (dh == 1 && cc >= 12) { o2.x = cc == 12 ? 0x3f803f80u : 0u; o2.y = o2.x; }
            __builtin_amdgcn_raw_buffer_store_b64(o2, rs, 2 * KB + t * KB + (16 * dh + cc + 32 * (G & 1)) * 16 + 8 * (G >> 1), kvdst, 16); }
        }
        if (pair == 0 && blk < 14) {
          const int to = L_RK + (32 * blk + 16 * (G >> 1) + 4 * (G & 1) + (cc >> 2)) * 56 + dh * 32 + (cc & 3) * 8;
          const u32x2 a_ = TRRD(to), c_ = TRRD(to + 8 * 56); STC(((u32x4){a_.x, a_.y, c_.x, c_.y}), lo, WKV_OFF + ((g * 2 + dh) * 14 + blk) * KB); }
      } else {
        X_STATS(mu, rstd);
      }
      STAMP(layer * 16 + 3);
      { f32x16 a = {}; u32x4 xf[28], wf[28];
        const int to = L_RQ + ((pair ? 4 : 8) * hh + (cc >> 2)) * 224 + w * 56 + (G & 1) * 32 + (cc & 3) * 8, to2 = to + (pair ? 8 : 4) * 224;
#define WQF(k) { const u32x2 a_ = TRRD(to + (k) * 16 * 224), c_ = TRRD(to2 + (k) * 16 * 224); wf[k] = (u32x4){a_.x, a_.y, c_.x, c_.y}; }
#define PUBA() { DRAIN(); if (lane == 0) __hip_atomic_store(flags + FA_OFF / 8 + ((g >> 1) * 256 + blk * 4 + (g & 1) * 2 + dh) * 16 + pair, tag, RLX); SBAR(); }
        u32x2 xo[4];
        if (pair == 0) { if (w < 2) PUBA() else DRAIN();
          asm volatile("s_waitcnt lgkmcnt(0)\n\ts_barrier" ::: "memory");
          UNR for (int a_ = 0; a_ < 4; ++a_) xo[a_] = *(const u32x2*)(x + (32 * blk + r) * DM + 32 * tlc + 8 * a_ + 4 * hh); }
        UNR for (int k = 0; k < 4; ++k) { WQF(k) xf[k] = XFRAG(k); }
        SBAR();
        UNR for (int k = 0; k < 28; ++k) { if (k + 4 < 28) { WQF(k + 4) xf[k + 4] = XFRAG(k + 4); }
          a = mfma32(wf[k], xf[k], a); SBAR();
          if (k == 13 && role && pair != 0) PUBA(); }
        if (pair == 0) unpack16((u32x4){xo[0].x, xo[0].y, xo[1].x, xo[1].y}, (u32x4){xo[2].x, xo[2].y, xo[3].x, xo[3].y}, xres);
        float n2; EPI_ALPHA(a, csq, mu, rstd * QSCALE, n2);
        u32x4 f0, f1; pack16(a, f0, f1);
        if (hh) f1.z = bf_ceil(__builtin_amdgcn_sqrtf(n2) * 1.02f);
        LDS128(L_Q + w * 2 * KB + lo) = f0; LDS128(L_Q + w * 2 * KB + KB + lo) = f1;
      }
      STAMP(layer * 16 + 4);
      { gu64* fp = flags + FA_OFF / 8 + tid * 16 + pair; poll(fp, tag); if (pair == 0) poll(fp + 256 * 16, tag); AFTER_POLL(); }
      STAMP(layer * 16 + 5);
      __syncthreads();
      STAMP(layer * 16 + 6);
      int base = KVA_OFF + pair * KVA_L + w * 64 * KB; OPQ(base);
      int woz = WOA_OFF + tlc * 32 * KB; OPQ(woz);
      int wvb = wbB + 56 * KB, wkb = wbB + 28 * KB; OPQ(wvb); OPQ(wkb);
      u32x4 q[4][2];
      const f32x4 km = __builtin_bit_cast(f32x4, LDC(lo, KM_OFF + pair * KB));
      if (b == 0 && tid == 0 && pair == 0) __hip_atomic_store(flags + EP_OFF / 8, tag, RLX);
      UNR for (int h = 0; h < 4; ++h) { const int qo = L_Q + ((w + 1 + h) & 3) * 2 * KB + lo; q[h][0] = LDS128(qo); q[h][1] = LDS128(qo + KB); }
      bool stored = false;
      int armo = OT_OFF + blk * OT_BLK + head * 2 * KB; const int armx = XT_OFF + blk * XT_BLK + g * XT_WG, armx0 = pair ? WS_BYTES : armx;
#pragma unroll 1
      for (int attempt = 0; attempt < 2; ++attempt) {
        u32x4 Kt[16][2], Vt[16][2];
#define LDK(t) { Kt[t][0] = LDC(lo, base + (t) * 4 * KB); Kt[t][1] = LDC(lo + KB, base + (t) * 4 * KB); }
#define LDV(t) { Vt[t][0] = LDC(lo + 2 * KB, base + (t) * 4 * KB); Vt[t][1] = LDC(lo + 3 * KB, base + (t) * 4 * KB); }
        LDK(0) LDV(0) LDK(1) LDV(1) LDK(2)
        SBAR();
        if (attempt == 0) {
          const float kmax = __builtin_amdgcn_sqrtf(__uint_as_float(umax64(umax(__float_as_uint(km.x + km.y), __float_as_uint(km.z + km.w)))));
          UNR for (int h = 0; h < 4; ++h) { const unsigned m = bf_ceil(bflo(q[h][1].z) * kmax) ^ 0x8000u; q[h][1].z = hh ? m : q[h][1].z; }
        }
        float z = 0.f; PIN(z); const unsigned zu = __float_as_uint(z);
        f32x16 oo[4] = {z, z, z, z};
        f32x16 sA, sB; u32x4 pA0 = zu, pA1 = zu, pB0 = zu, pB1 = zu; f32x4 ea, eb = z;
        PIN(q[0][1]); PIN(q[1][1]); PIN(q[2][1]); PIN(q[3][1]); PIN(pA0); PIN(pA1); PIN(pB0); PIN(pB1); PIN(eb);
        asm volatile("s_nop 1" : "+a"(oo[0]), "+a"(oo[1]), "+a"(oo[2]), "+a"(oo[3]));
        SBAR();
        MS0(sA, Kt[0][0], q[0][0]); MS1(sA, Kt[0][1], q[0][1]);
        NOP16V(sA); SBAR();
#define WPF(n) { if ((n) < 32) WZ[(n) & 31] = LDW(woz, (n)); else if ((n) < 60) Wv[((n) - 32) % 28] = LDW(wvb, (n) - 32); }
#define PUTO(i) { asm volatile("" : "+a"(oo[i])); const int s_ = L_C + (((w + 1 + (i)) & 3) * 3 + (i)) * 4 * KB + lo; UNR for (int e = 0; e < 4; ++e) \
          *(LAS f32x4*)(lds + s_ + e * KB) = (f32x4){oo[i][4 * e], oo[i][4 * e + 1], oo[i][4 * e + 2], oo[i][4 * e + 3]}; }
        UNR for (int t = 0; t < 16; ++t) {
          const int tp = t > 0 ? t - 1 : 0, tn = t < 15 ? t + 1 : 15;
          STEP(sA, sB, Kt[t][0], Kt[t][1], q[1][0], q[1][1], oo[3], Vt[tp][0], Vt[tp][1], pB0, pB1, pA0, pA1, , , WPF(4 * t), WPF(4 * t + 1));
          STEP(sB, sA, Kt[t][0], Kt[t][1], q[2][0], q[2][1], oo[0], Vt[t][0], Vt[t][1], pA0, pA1, pB0, pB1, , ,
               if (t + 2 < 16) Vt[(t + 2) & 15][0] = LDC(lo + 2 * KB, base + (t + 2) * 4 * KB), if (t + 2 < 16) Vt[(t + 2) & 15][1] = LDC(lo + 3 * KB, base + (t + 2) * 4 * KB); if (t >= 1) ARM(t - 1));
          STEP(sA, sB, Kt[t][0], Kt[t][1], q[3][0], q[3][1], oo[1], Vt[t][0], Vt[t][1], pB0, pB1, pA0, pA1, , , WPF(4 * t + 2), WPF(4 * t + 3) if (t == 15) PUTO(0));
          STEP(sB, sA, Kt[tn][0], Kt[tn][1], q[0][0], q[0][1], oo[2], Vt[t][0], Vt[t][1], pA0, pA1, pB0, pB1, , ,
               if (t + 3 < 16) Kt[(t + 3) & 15][0] = LDC(lo, base + (t + 3) * 4 * KB), if (t + 3 < 16) Kt[(t + 3) & 15][1] = LDC(lo + KB, base + (t + 3) * 4 * KB); if (t == 15) PUTO(1));
        }
        C2(pB1, 2, eb) asm volatile("s_nop 1");
        MO(oo[3], Vt[15][0], pB0); MO(oo[3], Vt[15][1], pB1);
        asm volatile("s_nop 3" : "+a"(oo[2])); SBAR();
        PUTO(2)
        DRAIN();
        armo = WS_BYTES;
        STAMP(layer * 16 + 7);
        UNR for (int k = 0; k < 8; ++k) Wk[k] = LDW(wkb, k);
        NOP16A(oo[3]); SBAR();
        f32x16 o = oo[3];
        __syncthreads();
        UNR for (int s = 0; s < 3; ++s)
          UNR for (int e = 0; e < 4; ++e) { const f32x4 t = *(LAS f32x4*)(lds + L_C + (w * 3 + s) * 4 * KB + e * KB + lo);
            o[4 * e] += t.x; o[4 * e + 1] += t.y; o[4 * e + 2] += t.z; o[4 * e + 3] += t.w; }
        const float l = half_hi(o[12]);
        const bool bad = !__all(l >= L_MIN);
        if (lane == 0) *(LAS unsigned*)(lds + L_MISC + w * 4) = bad ? 1u : 0u;
        if (attempt == 1 || !bad) { if (!stored) O_STORE(layer, o, l); stored = true; }
        STAMP(layer * 16 + 8);
        __syncthreads();
        const u32x4 bw = *(LAS u32x4*)(lds + L_MISC);
        if (__builtin_expect(attempt == 1 || __builtin_amdgcn_readfirstlane(bw.x | bw.y | bw.z | bw.w) == 0u, 1)) break;
        float mx[4] = {-INFINITY, -INFINITY, -INFINITY, -INFINITY};
        UNR for (int h = 0; h < 4; ++h) q[h][1].z = hh ? 0u : q[h][1].z;
#pragma unroll 1
        for (int t = 0; t < 16; ++t) {
          const u32x4 k0 = LDC(lo, base + t * 4 * KB), k1 = LDC(lo + KB, base + t * 4 * KB);
          UNR for (int h = 0; h < 4; ++h) { f32x16 s = {}; s = mfma32(k0, q[h][0], s); s = mfma32(k1, q[h][1], s);
            UNR for (int e = 0; e < 16; ++e) mx[h] = fmaxf(mx[h], s[e]); }
        }
        UNR for (int h = 0; h < 4; ++h) { const float m = half_max(mx[h]); if (hh == 0) *(LAS float*)(lds + L_MISC + 1024 + (w * 4 + ((w + 1 + h) & 3)) * 128 + r * 4) = m; }
        __syncthreads();
        UNR for (int h = 0; h < 4; ++h) { float m = -INFINITY;
          UNR for (int s = 0; s < 4; ++s) m = fmaxf(m, *(LAS float*)(lds + L_MISC + 1024 + (s * 4 + ((w + 1 + h) & 3)) * 128 + r * 4));
          q[h][1].z = hh ? (bf_ceil(m) ^ 0x8000u) : q[h][1].z; }
      }
      if (tid == 0) __hip_atomic_store(FLAG(blk * 4 + g, 4 + layer), tag, RLX);
      Z_PHASE(layer, false,
              UNR for (int k = 8; k < 18; ++k) Wk[k] = LDW(wkb, k),
              if ((k & 1) && k < 20) Wk[18 + (k >> 1)] = LDW(wkb, 18 + (k >> 1)); else if ((k & 1) && k < 24) WqB[(k >> 1) - 10] = LDW(wbB, (k >> 1) - 10));
    }
    {
      const int layer = 2 * pair + 1;
      IDX();
      const int wbB = WB_OFF + head * 3 * 28 * KB;
      UNR for (int k = 2; k < 10; ++k) WqB[k] = LDW(wbB, k);
      int woz = WOB_OFF + tlc * 32 * KB; OPQ(woz);
      f32x16 csqB, cskB; LDCS(csqB, head * 28); LDCS(cskB, 448 + head * 28);
      const float csv = __uint_as_float(__builtin_amdgcn_raw_buffer_load_b32(rs, r * 4, CS_OFF + (896 + head * 28) * 4, 16));
      SBAR();
      X_GATHER(layer);
      float mu, rstd, qn;
      u32x4 K[9][2], V[9][2], q0, q1;
      unsigned kown;
      {
        const int dst = KVB_OFF + blk * KVB_BLK + head * 4 * KB;
        f32x16 av = {}, ak = {}, aq = {}; u32x4 xf[84]; f32x4 rs_[4], nm_[4];
        X_STATS_LD();
        UNR for (int k = 0; k < 6; ++k) xf[k] = XFRAG(k);
        SBAR();
        sfor<0, 84>([&](auto kc) __attribute__((always_inline)) { constexpr int k = decltype(kc)::value;
          if constexpr (k + 6 < 84) xf[k + 6] = XFRAG((k + 6) % 28);
          if constexpr (k % 5 == 1 || k % 5 == 3) { constexpr int i = (k / 5) * 2 + (k % 5 == 3);
            if constexpr (i < 18) WqB[10 + i] = LDW(wbB, 10 + i); else if constexpr (i < 23) WZ[i - 18] = LDW(woz, i - 18); else if constexpr (k > 72) WZ[i - 24] = LDW(woz, i - 24); }
          if constexpr (k < 28) av = mfma32(xf[k], Wv[k], av); else if constexpr (k < 56) ak = mfma32(Wk[k - 28], xf[k], ak); else aq = mfma32(WqB[k - 56], xf[k], aq);
          if constexpr (k == 1) X_STATS(mu, rstd);
          if constexpr (k == 8) { UNR for (int i = 0; i < 4; ++i) { rs_[i] = *(LAS f32x4*)(lds + L_MS + w * 256 + (8 * i + 4 * hh) * 4); nm_[i] = *(LAS f32x4*)(lds + L_MS + w * 256 + 128 + (8 * i + 4 * hh) * 4); } }
          if constexpr (k == 30) asm volatile("" : "+a"(av));
          if constexpr (k >= 30 && k < 34) { constexpr int i = k - 30;
            unsigned p0 = pk(fmaf(av[4 * i], rs_[i].x, nm_[i].x * csv), fmaf(av[4 * i + 1], rs_[i].y, nm_[i].y * csv));
            unsigned p1 = pk(fmaf(av[4 * i + 2], rs_[i].z, nm_[i].z * csv), fmaf(av[4 * i + 3], rs_[i].w, nm_[i].w * csv));
            const unsigned pad = r == 28 ? 0x3f803f80u : 0u;
            V[8][i >> 1][2 * (i & 1)] = r < 28 ? p0 : pad; V[8][i >> 1][2 * (i & 1) + 1] = r < 28 ? p1 : pad; }
          if constexpr (k == 34) { STC(V[8][0], lo + 2 * KB, dst); STC(V[8][1], lo + 3 * KB, dst); }
          if constexpr (k == 57) { asm volatile("" : "+a"(ak));
            float kn2; EPI_ALPHA(ak, cskB, mu, rstd, kn2);
            if (hh) ak[12] = 1.f;
            pack16(ak, K[8][0], K[8][1]);
            STC(K[8][0], lo, dst); STC(K[8][1], lo + KB, dst);
            kown = umax32(__float_as_uint(kn2));
            if (lane == 0) __builtin_amdgcn_raw_buffer_store_b32(kown, rs, 0, KMB_OFF + (blk * 16 + head) * 4, 16);
            STAMP(layer * 16 + 3); }
          SBAR();
          if constexpr (k == 72) { DRAIN(); __builtin_amdgcn_s_barrier();
            if (tid == 0) __hip_atomic_store(FLAG(blk * 4 + g, 8 + pair), tag, RLX);
            SBAR(); } });
        float n2; EPI_ALPHA(aq, csqB, mu, rstd * QSCALE, n2);
        pack16(aq, q0, q1);
        qn = __builtin_amdgcn_sqrtf(n2) * 1.02f;
      }
      STAMP(layer * 16 + 4);
      const bool pv = lane < 8 && blk - 8 + lane >= 0;
      if (pv) poll(FLAG((blk - 8 + lane) * 4 + g, 8 + pair), tag);
      AFTER_POLL();
      STAMP(layer * 16 + 5);
      unsigned kmx = __builtin_amdgcn_raw_buffer_load_b32(rs, pv ? (blk - 8 + lane) * 64 : blk * 64, KMB_OFF + head * 4, 16);
      const int kvb = KVB_OFF + (blk - 8) * KVB_BLK + head * 4 * KB;
#define SRCB(j) (blk - 8 + (j) < 0 ? ZERO_OFF : kvb + (j) * KVB_BLK)
#define LKB(j) { const int s_ = SRCB(j); K[j][0] = LDC(lo, s_); K[j][1] = LDC(lo + KB, s_); }
#define LVB(j) { const int s_ = SRCB(j); V[j][0] = LDC(lo + 2 * KB, s_); V[j][1] = LDC(lo + 3 * KB, s_); }
      bool first = true;
      f32x16 o; float l;
#pragma unroll 1
      for (int attempt = 0; attempt < 2; ++attempt) {
        LKB(7) LKB(6) LVB(7) LKB(5) LVB(6) LKB(4)
        SBAR();
        if (first) { kmx = __builtin_amdgcn_readfirstlane(umax16(pv ? kmx : kown));
          q1.z = hh ? (bf_ceil(qn * __builtin_amdgcn_sqrtf(__uint_as_float(kmx))) ^ 0x8000u) : q1.z; first = false; }
        STAMP(layer * 16 + 6);
        float z = 0.f; PIN(z); const unsigned zu = __float_as_uint(z);
        o = z;
        f32x16 sA, sB; u32x4 pA0 = zu, pA1 = zu, pB0 = zu, pB1 = zu; f32x4 ea, eb = z;
        PIN(q0); PIN(q1); PIN(K[8][0]); PIN(K[8][1]); PIN(V[8][0]); PIN(V[8][1]); PIN(pA0); PIN(pA1); PIN(pB0); PIN(pB1); PIN(eb); asm volatile("s_nop 1" : "+a"(o)); SBAR();
        MS0(sA, K[8][0], q0); MS1(sA, K[8][1], q1);
        NOP16V(sA); SBAR();
#define MASKT(S, keep) { UNR for (int e = 0; e < 16; ++e) { const int row = (e & 3) + 8 * (e >> 2) + 4 * hh; S[e] = (keep) ? S[e] : -INFINITY; } PIN(S); SBAR(); }
#define WZ2(k) { WZ[k] = LDW(woz, k); WZ[k + 1] = LDW(woz, k + 1); }
        MASKT(sA, row <= r)
        STEP(sA, sB, K[7][0], K[7][1], q0, q1, o, V[8][0], V[8][1], pB0, pB1, pA0, pA1, , , LVB(5), LKB(3));
        STEP(sB, sA, K[6][0], K[6][1], q0, q1, o, V[8][0], V[8][1], pA0, pA1, pB0, pB1, , , LVB(4), LKB(2));
        STEP(sA, sB, K[5][0], K[5][1], q0, q1, o, V[7][0], V[7][1], pB0, pB1, pA0, pA1, , , LVB(3), LKB(1));
        STEP(sB, sA, K[4][0], K[4][1], q0, q1, o, V[6][0], V[6][1], pA0, pA1, pB0, pB1, , , LVB(2), LKB(0));
        STEP(sA, sB, K[3][0], K[3][1], q0, q1, o, V[5][0], V[5][1], pB0, pB1, pA0, pA1, , , LVB(1), LVB(0));
        STEP(sB, sA, K[2][0], K[2][1], q0, q1, o, V[4][0], V[4][1], pA0, pA1, pB0, pB1, , , WZ2(10), );
        STEP(sA, sB, K[1][0], K[1][1], q0, q1, o, V[3][0], V[3][1], pB0, pB1, pA0, pA1, , , WZ2(12), WZ2(14));
        STEP(sB, sA, K[0][0], K[0][1], q0, q1, o, V[2][0], V[2][1], pA0, pA1, pB0, pB1, , , WZ2(16), WZ2(18));
        MASKT(sA, row > r)
        STEP(sA, sB, K[0][0], K[0][1], q0, q1, o, V[1][0], V[1][1], pB0, pB1, pA0, pA1, , , , );
        C2(pA1, 2, eb) asm volatile("s_nop 1");
        MO(o, V[0][0], pA0); MO(o, V[0][1], pA1);
        NOP16A(o); SBAR();
        l = half_hi(o[12]);
        if (__builtin_expect(attempt == 1 || __all(l >= L_MIN), 1)) break;
        q1.z = hh ? 0u : q1.z;
        float mx = -INFINITY;
        UNR for (int j = 8; j >= 0; --j) {
          f32x16 s = {}; s = mfma32(K[j][0], q0, s); s = mfma32(K[j][1], q1, s); float m_ = -INFINITY;
          UNR for (int e = 0; e < 16; ++e) { const int row = (e & 3) + 8 * (e >> 2) + 4 * hh; float v = s[e];
            if (j == 8) v = (row <= r) ? v : -INFINITY; if (j == 0) v = (row > r) ? v : -INFINITY; m_ = fmaxf(m_, v); }
          mx = (blk - 8 + j >= 0) ? fmaxf(mx, m_) : mx; SBAR(); }
        mx = half_max(mx);
        q1.z = hh ? (bf_ceil(mx) ^ 0x8000u) : q1.z;
      }
      STAMP(layer * 16 + 7);
      O_STORE(layer, o, l);
      STAMP(layer * 16 + 8);
      HINT(4 + layer);
      int wkv = (g < 2 && w < 2) ? WKV_OFF + (g * 2 + dh) * 14 * KB : WS_BYTES; OPQ(wkv);
#define CSQ_CHAIN() { const int G = lane >> 4, cc = lane & 15, to = L_RQ + (4 * hh + (cc >> 2)) * 224 + w * 56 + (G & 1) * 32 + (cc & 3) * 8, to2 = to + 8 * 224; \
        u32x4 wf[28]; csq = (f32x16){}; UNR for (int k = 0; k < 6; ++k) WQF(k) SBAR(); \
        UNR for (int k = 0; k < 28; ++k) { if (k + 6 < 28) WQF(k + 6) if (!(k & 1) && k < 24) WZ[20 + (k >> 1)] = LDW(woz, 20 + (k >> 1)); csq = mfma32(wf[k], ones, csq); SBAR(); } }
      Z_PHASE(layer, true,
              CSQ_CHAIN(),
              if ((k & 1) && k < 28) W16[k >> 1] = LDW(wkv, k >> 1));
      cskv = (f32x4){0.f, 0.f, 0.f, 0.f};
      if (g == 0) { UNR for (int k = 0; k < 14; ++k) cskv = mfma16(W16[k], ones, cskv); }
      else if (g == 1) { UNR for (int k = 0; k < 14; ++k) cskv = mfma16(ones, W16[k], cskv); }
      SBAR();
      X_GATHER(layer + 1);
    }
  }
}

extern "C" void kernel_launch(void* const* d_in, const int* in_sizes, int n_in, void* d_out, int out_size, void* d_ws, size_t ws_size, hipStream_t stream) {
  (void)in_sizes; (void)n_in; (void)out_size; (void)ws_size;
  (void)hipFuncSetAttribute((const void*)main_kernel, hipFuncAttributeMaxDynamicSharedMemorySize, L_BYTES);
  main_kernel<<<256, 256, L_BYTES, stream>>>((const u16*)d_in[0], (const u16*)d_in[1], (const u16*)d_in[2], (const u16*)d_in[3], (const u16*)d_in[4],
                                             (const u16*)d_in[6], (const u16*)d_in[7], (unsigned char*)d_ws, (u16*)d_out);
}
```

```cpp
#include <hip/hip_runtime.h>
#include <hip/hip_bf16.h>

typedef float f32x16 __attribute__((ext_vector_type(16)));
typedef float f32x4 __attribute__((ext_vector_type(4)));
typedef float f32x2 __attribute__((ext_vector_type(2)));
typedef unsigned u32x4 __attribute__((ext_vector_type(4)));
typedef unsigned u32x2 __attribute__((ext_vector_type(2)));
typedef short bf16x8 __attribute__((ext_vector_type(8)));
typedef short s16x4 __attribute__((ext_vector_type(4)));
typedef __bf16 bf2 __attribute__((ext_vector_type(2)));
typedef unsigned short u16;
typedef unsigned long long u64;
#define GAS __attribute__((address_space(1)))
#define LAS __attribute__((address_space(3)))
typedef GAS u64 gu64;
#define RLX __ATOMIC_RELAXED, __HIP_MEMORY_SCOPE_AGENT
#define SBAR() __builtin_amdgcn_sched_barrier(0)
#define PIN(x) asm volatile("" : "+v"(x))
#define OPQ(x) asm volatile("" : "+s"(x))
#define DRAIN() asm volatile("s_waitcnt vmcnt(0)" ::: "memory")
#define UNR _Pragma("unroll")

constexpr int DM = 448, NB = 64, NTL = 14, KB = 1024;
constexpr int EP_OFF = 0;
constexpr int FA_OFF = 1 * KB;
constexpr int FL_OFF = FA_OFF + 2 * 8 * 4 * KB;
constexpr int KM_OFF = FL_OFF + 256 * 128;
constexpr int FB_OFF = KM_OFF + 2 * KB;
constexpr int CS_OFF = FB_OFF + 128 * KB;
constexpr int ZERO_OFF = CS_OFF + 6 * KB;
constexpr int WKV_OFF = ZERO_OFF + 4 * KB;
constexpr int KVA_OFF = WKV_OFF + 56 * KB;
constexpr int KVA_L = NB * 4 * KB;
constexpr int WOA_OFF = KVA_OFF + 2 * KVA_L;
constexpr int WOB_OFF = WOA_OFF + 14 * 32 * KB;
constexpr int WB_OFF = WOB_OFF + 14 * 32 * KB;
constexpr int XT_OFF = WB_OFF + 16 * 3 * 28 * KB;
constexpr int XT_WG = 9 * KB, XT_BLK = 4 * XT_WG, XT_L = NB * XT_BLK;
constexpr int OT_OFF = XT_OFF + 2 * XT_L;
constexpr int OT_BLK = 32 * KB, OT_L = NB * OT_BLK;
constexpr int KVB_OFF = OT_OFF + 2 * OT_L;
constexpr int KVB_BLK = 64 * KB;
constexpr int WS_BYTES = KVB_OFF + NB * KVB_BLK;
#define FLAG(wg, word) (flags + (FL_OFF + (wg) * 128) / 8 + (word))
constexpr int L_X = 0;
constexpr int L_O = 0, L_C = 0;
constexpr int L_MISC = 48 * KB;
constexpr int L_Q = 52 * KB;
constexpr int L_RK = 36 * KB;
constexpr int L_RQ = L_RK + 448 * 56;
constexpr int L_MS = 159 * KB;
constexpr int L_BYTES = 160 * KB;
static_assert(L_RQ + 448 * 224 + 64 <= L_MS && L_RQ % 16 == 0, "lds");

#define STAMP(n) do {} while (0)
#define STAMP_OUT()
constexpr float QSCALE = 0.18898223650461361f * 1.4426950408889634f;
constexpr unsigned SPIN_CAP = 1u << 16;
#define POLL_SLEEP 4
#define PUBV_K 52
#define PUBK_K 71
#define ROTM 0
#define PUBA_K 13
#define SPLIT_K 16
constexpr float L_MIN = 1e-18f;

__device__ __forceinline__ unsigned pk(float a, float b) { f32x2 v = {a, b}; bf2 r = __builtin_convertvector(v, bf2); return __builtin_bit_cast(unsigned, r); }
__device__ __forceinline__ float bflo(unsigned u) { return __uint_as_float(u << 16); }
__device__ __forceinline__ float bfhi(unsigned u) { return __uint_as_float(u & 0xffff0000u); }
__device__ __forceinline__ f32x16 mfma32(u32x4 a, u32x4 b, f32x16 c) {
  return __builtin_amdgcn_mfma_f32_32x32x16_bf16(__builtin_bit_cast(bf16x8, a), __builtin_bit_cast(bf16x8, b), c, 0, 0, 0); }
__device__ __forceinline__ f32x4 mfma16(u32x4 a, u32x4 b, f32x4 c) {
  return __builtin_amdgcn_mfma_f32_16x16x32_bf16(__builtin_bit_cast(bf16x8, a), __builtin_bit_cast(bf16x8, b), c, 0, 0, 0); }
__device__ __forceinline__ float half_sum(float v) { auto r = __builtin_amdgcn_permlane32_swap(__float_as_uint(v), __float_as_uint(v), false, false); return __uint_as_float(r[0]) + __uint_as_float(r[1]); }
__device__ __forceinline__ float half_max(float v) { auto r = __builtin_amdgcn_permlane32_swap(__float_as_uint(v), __float_as_uint(v), false, false); return fmaxf(__uint_as_float(r[0]), __uint_as_float(r[1])); }
__device__ __forceinline__ float half_hi(float v) { auto r = __builtin_amdgcn_permlane32_swap(__float_as_uint(v), __float_as_uint(v), false, false); return __uint_as_float(r[1]); }
__device__ __forceinline__ float row_sum(float v) { auto r = __builtin_amdgcn_permlane16_swap(__float_as_uint(v), __float_as_uint(v), false, false); return __uint_as_float(r[0]) + __uint_as_float(r[1]); }
__device__ __forceinline__ float dot2(unsigned a, unsigned b, float c) { return __builtin_amdgcn_fdot2_f32_bf16(__builtin_bit_cast(bf2, a), __builtin_bit_cast(bf2, b), c, false); }
template <int C> __device__ __forceinline__ float dppf(float v) { return __uint_as_float(__builtin_amdgcn_update_dpp(0u, __float_as_uint(v), C, 0xf, 0xf, true)); }
__device__ __forceinline__ float sum8(float v) { return half_sum(row_sum(v + dppf<0xb1>(v))); }
template <int N> __device__ __forceinline__ unsigned ror(unsigned v) { return __builtin_amdgcn_update_dpp(0u, v, 0x120 + N, 0xf, 0xf, true); }
__device__ __forceinline__ unsigned umax(unsigned a, unsigned b) { return a > b ? a : b; }
__device__ __forceinline__ unsigned umax16(unsigned v) { v = umax(v, ror<1>(v)); v = umax(v, ror<2>(v)); v = umax(v, ror<4>(v)); return umax(v, ror<8>(v)); }
__device__ __forceinline__ unsigned umax32(unsigned v) { v = umax16(v); auto r = __builtin_amdgcn_permlane16_swap(v, v, false, false); return umax(r[0], r[1]); }
__device__ __forceinline__ unsigned umax64(unsigned v) { v = umax32(v); auto r = __builtin_amdgcn_permlane32_swap(v, v, false, false); return umax(r[0], r[1]); }
__device__ __forceinline__ unsigned bf_ceil(float v) { unsigned u = __float_as_uint(v); unsigned t = u >> 16; return (v > 0.f && (u & 0xffffu)) ? t + 1u : t; }
__device__ __forceinline__ void pack16(const f32x16& v, u32x4& f0, u32x4& f1) {
  f0 = (u32x4){pk(v[0], v[1]), pk(v[2], v[3]), pk(v[4], v[5]), pk(v[6], v[7])};
  f1 = (u32x4){pk(v[8], v[9]), pk(v[10], v[11]), pk(v[12], v[13]), pk(v[14], v[15])}; }
__device__ __forceinline__ void unpack16(const u32x4 f0, const u32x4 f1, f32x16& v) {
  UNR for (int i = 0; i < 4; ++i) { v[2 * i] = bflo(f0[i]); v[2 * i + 1] = bfhi(f0[i]); v[8 + 2 * i] = bflo(f1[i]); v[9 + 2 * i] = bfhi(f1[i]); } }
__device__ __forceinline__ void tile_stats(const f32x16& v, float& mean, float& m2) {
  const float s = (((v[0] + v[1]) + (v[2] + v[3])) + ((v[4] + v[5]) + (v[6] + v[7]))) + (((v[8] + v[9]) + (v[10] + v[11])) + ((v[12] + v[13]) + (v[14] + v[15])));
  mean = half_sum(s) * (1.f / 32.f);
  float q0 = 0.f, q1 = 0.f;
  UNR for (int i = 0; i < 8; ++i) { const float d0 = v[i] - mean, d1 = v[8 + i] - mean; q0 = fmaf(d0, d0, q0); q1 = fmaf(d1, d1, q1); }
  m2 = half_sum(q0 + q1);
}
template <int V> struct IC { static constexpr int value = V; };
template <int I, int N, class F> __device__ __forceinline__ void sfor(F&& f) { if constexpr (I < N) { f(IC<I>{}); sfor<I + 1, N>(f); } }
__device__ __forceinline__ u32x4 mkdesc(const void* p, unsigned bytes) { return (u32x4){(unsigned)(size_t)p, (unsigned)((size_t)p >> 32) & 0xffffu, bytes, 0x00020000u}; }

#define LDC(voff, soff) __builtin_amdgcn_raw_buffer_load_b128(rs, (voff), (soff), 16)
#define STC(v, voff, soff) __builtin_amdgcn_raw_buffer_store_b128((v), rs, (voff), (soff), 16)
#define LDS128(off) (*(LAS u32x4*)(lds + (off)))
#define LDW(base, k) LDC(lo + ((k) & 3) * KB, (base) + ((k) >> 2) * 4 * KB)
#define MS0(S, A, B) asm volatile("v_mfma_f32_32x32x16_bf16 %0, %1, %2, 0" : "=&v"(S) : "v"(A), "v"(B))
#define MS1(S, A, B) asm volatile("v_mfma_f32_32x32x16_bf16 %0, %1, %2, %0" : "+v"(S) : "v"(A), "v"(B))
#define MO(O, A, B) { O = mfma32(A, B, O); asm volatile("" : "+a"(O)); }
#define EX(x) __builtin_amdgcn_exp2f(x)
#define E4(E, S, i) { E[0] = EX(S[i]); E[1] = EX(S[i + 1]); E[2] = EX(S[i + 2]); E[3] = EX(S[i + 3]); PIN(E); SBAR(); }
#define C2(P, j, E) { P[j] = pk(E[0], E[1]); P[j + 1] = pk(E[2], E[3]); PIN(P); SBAR(); }
#define STEP(MODE, SC, SN, KN0, KN1, QN0, QN1, OP, VP0, VP1, PP0, PP1, PC0, PC1, W0, LDA, LDB) \
  W0; if (!((MODE) & 4)) MS0(SN, KN0, QN0); SBAR(); E4(ea, SC, 0) if (!((MODE) & 1)) C2(PP1, 2, eb) \
  if (!((MODE) & 4)) MS1(SN, KN1, QN1); LDA; SBAR(); E4(eb, SC, 4) C2(PC0, 0, ea) \
  if ((MODE) & 2) OP = (f32x16){}; \
  if (!((MODE) & 1)) MO(OP, VP0, PP0); SBAR(); E4(ea, SC, 8) C2(PC0, 2, eb) \
  if (!((MODE) & 1)) MO(OP, VP1, PP1); LDB; SBAR(); E4(eb, SC, 12) C2(PC1, 0, ea)
#define NOP16V(x) asm volatile("s_nop 7\n\ts_nop 7" : "+v"(x))
#define NOP16A(x) asm volatile("s_nop 7\n\ts_nop 7" : "+a"(x))
#define VMW(n) asm volatile("s_waitcnt vmcnt(" #n ")")
#define ALD(dst, soff, imm) asm volatile("buffer_load_dwordx4 %0, %1, %2, %3 offen offset:" #imm " sc1" : "+v"(dst) : "v"(lo), "s"(desc), "s"(soff))
#define ALD0(dst, soff, imm) asm volatile("buffer_load_dwordx4 %0, %1, %2, %3 offen offset:" #imm " sc1" : "=v"(dst) : "v"(lo), "s"(desc), "s"(soff))
#define ALDA(dst, soff, imm) asm volatile("buffer_load_dwordx4 %0, %1, %2, %3 offen offset:%4 sc1" : "=a"(dst) : "v"(lo), "s"(desc), "s"(soff), "i"(imm))
#define LDWA(dst, base, k) { const int so_ = (base) + ((k) >> 2) * 4 * KB; ALDA(dst, so_, ((k) & 3) * KB); }
#define ADMA(ldsaddr, voff, dsc, soff) asm volatile("s_mov_b32 m0, %0\n\ts_nop 0\n\tbuffer_load_dwordx4 %1, %2, %3 offen lds" :: "s"(ldsaddr), "v"(voff), "s"(dsc), "s"(soff) : "memory")
#define DMAW(base, k) { const unsigned la_ = L_W + w * L_WW + (k) * KB; const int so_ = (base) + (k) * KB; ADMA(la_, lo, desc, so_); }
#define TRRD(off) __builtin_bit_cast(u32x2, __builtin_amdgcn_ds_read_tr16_b64_v4i16((LAS s16x4*)(lds + (off))))

__device__ __forceinline__ void poll(gu64* p, u64 tag) {
  unsigned spins = 0;
  while (__hip_atomic_load(p, RLX) != tag) { if (++spins > SPIN_CAP) break; }
}
#define OUTST(v, p) *(p) = v
#define AFTER_POLL() __builtin_amdgcn_fence(__ATOMIC_ACQUIRE, "wavefront")

__global__ __launch_bounds__(256, 1) void main_kernel(const u16* __restrict__ x, const u16* __restrict__ wq, const u16* __restrict__ wk, const u16* __restrict__ wv,
                                                      const u16* __restrict__ woa, const u16* __restrict__ wb, const u16* __restrict__ wob,
                                                      unsigned char* __restrict__ ws, u16* __restrict__ out) {
  extern __shared__ __attribute__((aligned(16))) unsigned char smem[];
  const __amdgpu_buffer_rsrc_t rs = __builtin_amdgcn_make_buffer_rsrc((void*)ws, 0, WS_BYTES, 0x00020000);
  const u32x4 desc = mkdesc(ws, WS_BYTES);
  LAS unsigned char* lds = (LAS unsigned char*)smem;
  gu64* flags = (gu64*)ws;
  int tid_ = threadIdx.x, b_ = blockIdx.x, w_ = __builtin_amdgcn_readfirstlane(threadIdx.x >> 6);
#define IDX() asm volatile("" : "+v"(tid_)); asm volatile("" : "+s"(b_), "+s"(w_)); \
  const int tid = tid_, b = b_, w = w_, lane = tid & 63, r = lane & 31, hh = lane >> 5, lo = lane * 16; \
  const int blk = (b & 7) * 8 + (b >> 5), g = (b >> 3) & 3, head = 4 * g + w, tl = head; const bool act = tl < NTL; const int tlc = act ? tl : 0; \
  const int dh = w & 1, th = w >> 1; (void)r; (void)hh; (void)dh; (void)th; (void)tlc; (void)blk; (void)lo
  u64 pc_; unsigned len_;
  asm volatile("s_getpc_b64 %0\n.Lcode_here:\n\ts_mov_b32 %1, .Lcode_last-.Lcode_here" : "=s"(pc_), "=s"(len_));
#define CODEPF(ldsoff) { const u32x4 dc = mkdesc((const void*)pc_, len_); const int vo = ((b >> 3) * 4 + w) * 512 + (lane & 3) * 128; const unsigned la = (ldsoff) + w * 256; \
    asm volatile("s_mov_b32 m0, %0\n\ts_nop 0\n\tbuffer_load_dword %1, %2, 0 offen lds" :: "s"(la), "v"(vo), "s"(dc) : "memory"); }
  STAMP(64);

  f32x16 xres, csq;
  f32x4 cskv;
  u32x4 W16[14];
  u64 tag;
  {
    IDX();
    u64 ep; asm volatile("global_load_dwordx2 %0, %1, %2 sc1" : "=v"(ep) : "v"(0), "s"(ws));
    u32x4 xr[7];
    { const u32x4 dx = mkdesc(x + 32 * blk * DM, 32 * 896); const int vo = (8 * w + ((lane >> 1) & 7)) * 896 + (lane >> 4) * 32 + (lane & 1) * 16;
      UNR for (int f = 0; f < 7; ++f) asm volatile("buffer_load_dwordx4 %0, %1, %2, 0 offen offset:%3" : "=v"(xr[f]) : "v"(vo), "s"(dx), "i"(f * 128)); }
    CODEPF(L_X + 31 * KB);
    const int n = blk * 2 + (g & 1), nw = n - 84;
    const int c3 = (n * 37) >> 10, ks = n - 28 * c3;
    const int t7 = tid & 127;
    int vq[7];
    const u32x4 dq = mkdesc(wq, 448 * 448 * 2);
#define WQDMA(u0) { int u = (u0) + (blk & 7); u = u >= 7 ? u - 7 : u; UNR for (int k = 0; k < 7; ++k) { \
      const unsigned la = L_RQ + (2 * (7 * u + k) + dh) * KB; const int so = u * 64 * 896; ADMA(la, vq[k], dq, so); } }
    if (w >= 2) {
      UNR for (int k = 0; k < 7; ++k) { const int c = t7 + 128 * k, row = (c * 4682) >> 16; vq[k] = row * 896 + (c - row * 14) * 16 + g * 224; }
      WQDMA(0) WQDMA(1)
    } else if (g < 2 || n >= 84) { const u32x4 dk = g < 2 ? mkdesc(g ? wv : wk, 448 * 56) : mkdesc((nw & 16 ? wob : woa) + (nw & 15) * 28 * 448, n < 116 ? 448 * 56 : 0);
      UNR for (int k = 0; k < 14; ++k) { const int i = 2 * k + w; const unsigned la = i < 24 ? L_RK + i * KB : L_X + 32 * KB; const int so = i < 24 ? i * KB : 448 * 56;
        if (k == 12) { const unsigned l2 = L_RK + 24 * KB; const int s2 = 24 * KB; if (lane < 32) ADMA(l2, lo, dk, s2); } else ADMA(la, lo, dk, so); }
    } else { const u32x4 dk = mkdesc(wb + 16 * ks * 1344 + 448 * c3, 16 * 2688);
      UNR for (int k = 0; k < 7; ++k) { const int c = t7 + 128 * k, row = (c * 1171) >> 16; const unsigned la = L_RK + (2 * k + w) * KB;
        ADMA(la, row * 2688 + (c - row * 56) * 16, dk, 0); }
      UNR for (int k = 0; k < 7; ++k) { const unsigned la = L_X + 32 * KB; const int so = 16 * 2688; ADMA(la, lo, dk, so); } }
    STAMP(67);
    asm volatile("s_waitcnt vmcnt(15)" : "+v"(xr[0]), "+v"(xr[1]), "+v"(xr[2]), "+v"(xr[3]), "+v"(xr[4]), "+v"(xr[5]), "+v"(xr[6]), "+v"(ep) :: "memory");
    { float s0 = 0.f, s1 = 0.f, q0 = 0.f, q1 = 0.f;
      UNR for (int f = 0; f < 7; ++f) UNR for (int c = 0; c < 4; c += 2) { s0 = dot2(xr[f][c], 0x3f803f80u, s0); q0 = dot2(xr[f][c], xr[f][c], q0); s1 = dot2(xr[f][c + 1], 0x3f803f80u, s1); q1 = dot2(xr[f][c + 1], xr[f][c + 1], q1); }
      SBAR(); if (w >= 2) WQDMA(2) SBAR();
      const float mu = sum8(s0 + s1) * (1.f / 448.f);
      const float rstd = __builtin_amdgcn_rsqf(fmaxf(fmaf(-mu, mu, sum8(q0 + q1) * (1.f / 448.f)), 0.f) + 1e-5f), nm = -mu * rstd;
      UNR for (int f = 0; f < 7; ++f) {
        u32x4 o; UNR for (int e = 0; e < 4; ++e) { const f32x2 t = (f32x2){bflo(xr[f][e]), bfhi(xr[f][e])} * rstd + nm; o[e] = pk(t.x, t.y); }
        LDS128(L_X + (f >> 1) * XT_WG + (f & 1) * 4 * KB + (lane >> 4) * KB + (8 * w + ((lane >> 1) & 7) + 32 * (lane & 1)) * 16) = o; }
      const float one = 32.f * (1.f - 1e-5f);
      *(LAS f32x4*)(lds + L_X + w * XT_WG + 8192 + lo) = (f32x4){0.f, one, 0.f, one};
    }
    tag = (((u64)(unsigned)__builtin_amdgcn_readfirstlane((unsigned)(ep >> 32)) << 32) | (unsigned)__builtin_amdgcn_readfirstlane((unsigned)ep)) + 1ull;
    if (w < 2) DRAIN();
    STAMP(68);
    asm volatile("s_waitcnt lgkmcnt(0)\n\ts_barrier" ::: "memory");
    STAMP(65);
    const int q4 = (lane & 15) >> 2, p4 = lane & 3, gq = lane >> 4;
    cskv = (f32x4){0.f, 0.f, 0.f, 0.f}; csq = (f32x16){};
    if (w >= 2) {
      WQDMA(3) WQDMA(4) WQDMA(5) WQDMA(6)
      {
        const int vo = lane < 13 ? ((b * 2 + dh) * 13 + lane) * 128 + KB : lane * 256 * KB; const unsigned la = L_X + 33 * KB + w * 256;
        asm volatile("s_mov_b32 m0, %0\n\ts_nop 0\n\tbuffer_load_dword %1, %2, 0 offen sc1 lds" :: "s"(la), "v"(vo), "s"(desc) : "memory"); }
    } else if (g < 2) { const int to = L_RK + (8 * gq + q4) * 56 + dh * 32 + p4 * 8;
      UNR for (int k = 0; k < 14; ++k) { const u32x2 a = TRRD(to + k * 32 * 56), c = TRRD(to + k * 32 * 56 + 4 * 56); W16[k] = (u32x4){a.x, a.y, c.x, c.y}; }
    } else {
      const int to = L_RK + (4 * hh + q4) * 896 + (gq & 1) * 32 + p4 * 8;
      if (n < 84) {
        UNR for (int j = 0; j < 8; ++j) { const int h = 8 * w + j; const u32x2 a = TRRD(to + h * 56), c = TRRD(to + h * 56 + 8 * 896);
          STC(((u32x4){a.x, a.y, c.x, c.y}), lo, WB_OFF + ((h * 3 + c3) * 28 + ks) * KB); }
      } else if (n < 116) {
        UNR for (int j = 0; j < 7; ++j) { const int t = 2 * j + w;
          const u32x2 a = TRRD(to + t * 64), c = TRRD(to + t * 64 + 8 * 896), d = TRRD(to + t * 64 + 16 * 896), e = TRRD(to + t * 64 + 24 * 896);
          const int dst = (nw & 16 ? WOB_OFF : WOA_OFF) + (t * 32 + 2 * (nw & 15)) * KB;
          STC(((u32x4){a.x, a.y, c.x, c.y}), lo, dst); STC(((u32x4){d.x, d.y, hh ? 0u : e.x, hh ? 0u : e.y}), lo + KB, dst); }
      } else if (n == 116) { STC(((u32x4){0u, 0u, 0u, 0u}), lo, ZERO_OFF + w * 2 * KB); STC(((u32x4){0u, 0u, 0u, 0u}), lo + KB, ZERO_OFF + w * 2 * KB); }
    }
    STAMP(66);
  }

#define GPUT(N, ldsoff) unsigned m_ = 0u; \
    UNR for (int f = 0; f < N; ++f) { LDS128((ldsoff) + f * KB + lo) = t_[f]; m_ = umax(umax(m_, t_[f].x), t_[f].y); m_ = umax(umax(m_, t_[f].z), t_[f].w); PIN(m_); SBAR(); }
#define GATHER(N, src, ldsoff, flagp, ...) { \
    { u32x4 t_[N]; UNR for (int f = 0; f < N; ++f) { t_[f] = LDW(src, f); SBAR(); } \
      SBAR(); __VA_ARGS__; SBAR(); \
      GPUT(N, ldsoff) \
      if (__builtin_expect(!__all(m_ != ~0u), 0)) { poll(flagp, tag); \
        for (unsigned spins_ = 0; spins_ < SPIN_CAP; ++spins_) { asm volatile("" ::: "memory"); \
          u32x4 t_[N]; UNR for (int f = 0; f < N; ++f) t_[f] = LDW(src, f); \
          GPUT(N, ldsoff) if (__all(m_ != ~0u)) break; } } } \
    asm volatile("s_waitcnt lgkmcnt(0)\n\ts_barrier" ::: "memory"); }
#define SANE(f) { UNR for (int e_ = 0; e_ < 4; ++e_) f[e_] = f[e_] < 0xfffffffeu ? f[e_] : 0xfffffffeu; }
#define HINT(word) { __builtin_amdgcn_s_barrier(); if (tid == 0) __hip_atomic_store(FLAG(blk * 4 + g, (word)), tag, RLX); }
#define ARM(t) { const u32x4 a_ = {~0u, ~0u, ~0u, ~0u}; \
    if ((t) < 4) STC(a_, lo + ((t) & 1) * KB, armo + ((t) >> 1) * OT_L); \
    else if ((t) < 10) { const int d_ = (t) < 7 ? armx + XT_L : armx0; \
      if (((t) - 4) % 3 < 2) STC(a_, lo + (((t) - 4) % 3) * KB, d_ + w * 2048); else __builtin_amdgcn_raw_buffer_store_b32(~0u, rs, lane * 4, d_ + 8192 + w * 256, 16); } }
#define X_GATHER(layer, WORK) { \
    STAMP((layer) * 16 + 0); \
    STAMP((layer) * 16 + 1); \
    int src_ = XT_OFF + ((layer) & 1) * XT_L + blk * XT_BLK + w * XT_WG; OPQ(src_); \
    GATHER(9, src_, L_X + w * XT_WG, FLAG(blk * 4 + w, (layer)), WORK) \
    STAMP((layer) * 16 + 2); }
#define X_STATS_LD() f32x2 st_[7]; \
    UNR for (int k = 0; k < 7; ++k) st_[k] = *(LAS f32x2*)(lds + L_X + (k >> 1) * XT_WG + 8192 + (k & 1) * 512 + lane * 8)
#define X_STATS(mu, rstd) { f32x2 s2_; STATS_STEP(0) STATS_STEP(1) STATS_STEP(2) STATS_STEP(3) }
#define STATS_STEP(k) \
    if constexpr (k == 0) { s2_ = ((st_[0] + st_[1]) + (st_[2] + st_[3])) + ((st_[4] + st_[5]) + st_[6]); PIN(s2_); } \
    if constexpr (k == 1) { mu = half_sum(s2_.x) * (1.f / 448.f); s2_.y = half_sum(s2_.y); PIN(mu); PIN(s2_); } \
    if constexpr (k == 2) { rstd = __builtin_amdgcn_rsqf(fmaxf(fmaf(-mu, mu, s2_.y * (1.f / 448.f)), 0.f) + 1e-5f); PIN(rstd); } \
    if constexpr (k == 3) *(LAS float*)(lds + L_MS + w * 256 + lane * 4) = hh ? -mu * rstd : rstd;
#define XFRAG(ks) LDS128(L_X + ((ks) >> 3) * XT_WG + ((ks) & 7) * KB + lo)
#define LDCS(cs, col) { int o_ = CS_OFF + (col) * 4; OPQ(o_); UNR for (int q4 = 0; q4 < 4; ++q4) { const f32x4 c_ = __builtin_bit_cast(f32x4, LDC(hh * 16 + q4 * 32, o_)); \
    cs[4 * q4] = c_.x; cs[4 * q4 + 1] = c_.y; cs[4 * q4 + 2] = c_.z; cs[4 * q4 + 3] = c_.w; } }
#define EPI_ALPHA(a, cs, mu, sc, n2) { const float nm_ = -(mu) * (sc); UNR for (int e = 0; e < 16; ++e) a[e] = fmaf(a[e], (sc), nm_ * cs[e]); \
    UNR for (int e = 12; e < 16; ++e) a[e] = hh ? 0.f : a[e]; \
    float n0_ = 0.f, n1_ = 0.f; UNR for (int e = 0; e < 8; ++e) { n0_ = fmaf(a[e], a[e], n0_); n1_ = fmaf(a[8 + e], a[8 + e], n1_); } n2 = half_sum(n0_ + n1_); }
#define PUBLISH(word) { DRAIN(); __syncthreads(); if (tid == 0) __hip_atomic_store(FLAG(blk * 4 + g, (word)), tag, RLX); }
#define O_STORE(layer, o, l) { const float inv_ = __builtin_amdgcn_rcpf(l); f32x16 v_; \
    UNR for (int e = 0; e < 16; ++e) v_[e] = o[e] * inv_; \
    u32x4 f0_, f1_; pack16(v_, f0_, f1_); if (hh) { f1_.z = 0u; f1_.w = 0u; } SANE(f0_) SANE(f1_) \
    const int dst_ = OT_OFF + ((layer) & 1) * OT_L + blk * OT_BLK + head * 2 * KB; STC(f0_, lo, dst_); STC(f1_, lo + KB, dst_); }
#define Z_PHASE(layer, LAST, PRE, PRE2, SIDE) { \
    STAMP((layer) * 16 + 9); \
    PRE; \
    SBAR(); \
    STAMP((layer) * 16 + 10); \
    if (!LAST) { poll(FLAG(blk * 4 + w, 4 + (layer)), tag); AFTER_POLL(); }       \
    STAMP((layer) * 16 + 11); \
    int src_ = OT_OFF + ((layer) & 1) * OT_L + blk * OT_BLK + w * 8 * KB; OPQ(src_); \
    GATHER(8, src_, L_O + w * 8 * KB, FLAG(blk * 4 + w, 4 + (layer)), PRE2) \
    STAMP((layer) * 16 + 12); \
    u32x4 of_[32]; \
    UNR for (int k = 0; k < 6; ++k) of_[k] = LDS128(L_O + k * KB + lo); \
    SBAR(); \
    UNR for (int k = 0; k < 32; ++k) { if (k + 6 < 32) of_[k + 6] = LDS128(L_O + (k + 6) * KB + lo); SIDE; xres = mfma32(WZ[k], of_[k], xres); SBAR(); } \
    u32x4 f0, f1; pack16(xres, f0, f1); \
    STAMP((layer) * 16 + 13); \
    if (LAST && (layer) == 3) { \
      __builtin_amdgcn_s_barrier(); \
      { LAS unsigned char* p = lds + L_O + r * 272 + w * 64 + 8 * hh; \
        *(LAS u32x2*)(p) = (u32x2){f0.x, f0.y}; *(LAS u32x2*)(p + 16) = (u32x2){f0.z, f0.w}; *(LAS u32x2*)(p + 32) = (u32x2){f1.x, f1.y}; *(LAS u32x2*)(p + 48) = (u32x2){f1.z, f1.w}; } \
      __syncthreads(); \
      UNR for (int j = 0; j < 2; ++j) { const int row = 16 * j + (tid >> 4), c = tid & 15; \
        if (g < 3 || c < 8) OUTST(*(LAS u32x4*)(lds + L_O + row * 272 + c * 16), (u32x4*)(out + (32 * blk + row) * DM + 128 * g + 8 * c)); } \
      STAMP_OUT() if constexpr (LAST) asm volatile(".Lcode_last:"); \
      __builtin_amdgcn_endpgm(); \
    } \
    const int dst = XT_OFF + (((layer) + 1) & 1) * XT_L + blk * XT_BLK + g * XT_WG; \
    SANE(f0) SANE(f1) STC(f0, lo, dst + w * 2048); STC(f1, lo + KB, dst + w * 2048); \
    { float s_ = 0.f, q_ = 0.f; UNR for (int e = 0; e < 4; ++e) { s_ = dot2(f0[e], 0x3f803f80u, s_); q_ = dot2(f0[e], f0[e], q_); s_ = dot2(f1[e], 0x3f803f80u, s_); q_ = dot2(f1[e], f1[e], q_); } \
      auto r_ = __builtin_amdgcn_permlane32_swap(__float_as_uint(s_), __float_as_uint(q_), false, false);         \
      const unsigned sv = __float_as_uint(__uint_as_float(r_[0]) + __uint_as_float(r_[1])); \
      __builtin_amdgcn_raw_buffer_store_b32(sv < 0xfffffffeu ? sv : 0xfffffffeu, rs, r * 8 + hh * 4, dst + 8192 + w * 256, 16); } \
    STAMP((layer) * 16 + 14); \
    HINT((layer) + 1); \
    STAMP((layer) * 16 + 15); }

#pragma unroll 1
  for (int pair = 0;; ++pair) {
    u32x4 WZ[32], Wv[28], Wk[28], WqB[28]; float csv;
    {
      const int layer = 2 * pair;
      IDX();
      const int wbB = WB_OFF + head * 3 * 28 * KB;
      float mu, rstd;
      X_STATS_LD();
      const int G = lane >> 4, cc = lane & 15;
      const bool role = g < 2 && w < 2;
      if (role) {
        const int xo = L_X + (G >> 1) * KB + (cc + 32 * (G & 1)) * 16;
        u32x4 xf[2][14];
        UNR for (int k = 0; k < 14; ++k) { xf[0][k] = LDS128(xo + (k >> 2) * XT_WG + (k & 3) * 2048); xf[1][k] = LDS128(xo + (k >> 2) * XT_WG + (k & 3) * 2048 + 256); }
        f32x4 a[2] = {{0.f, 0.f, 0.f, 0.f}, {0.f, 0.f, 0.f, 0.f}}, rs_[2], nm_[2]; f32x2 s2_;
        const int kvdst = KVA_OFF + pair * KVA_L + blk * 4 * KB, mo = L_MS + w * 256 + (g ? 16 * G : 4 * cc);
        SBAR();
#define KVCHAIN(MM) sfor<0, 14>([&](auto kc) __attribute__((always_inline)) { constexpr int k = decltype(kc)::value; MM; STATS_STEP(k) \
          if constexpr (k == 5) { UNR for (int t = 0; t < 2; ++t) { rs_[t] = *(LAS f32x4*)(lds + (mo & ~15) + 64 * t); nm_[t] = *(LAS f32x4*)(lds + (mo & ~15) + 128 + 64 * t); } } SBAR(); })
        if (g == 0) {
          KVCHAIN(a[0] = mfma16(W16[k], xf[0][k], a[0]); a[1] = mfma16(W16[k], xf[1][k], a[1]));
          const bool pad = dh == 1 && G == 3; float n2[2];
          UNR for (int t = 0; t < 2; ++t) { n2[t] = 0.f;
            const float r_ = (cc & 2) ? ((cc & 1) ? rs_[t].w : rs_[t].z) : ((cc & 1) ? rs_[t].y : rs_[t].x), m_ = (cc & 2) ? ((cc & 1) ? nm_[t].w : nm_[t].z) : ((cc & 1) ? nm_[t].y : nm_[t].x);
            UNR for (int e = 0; e < 4; ++e) { a[t][e] = pad ? 0.f : fmaf(a[t][e], r_, m_ * cskv[e]); n2[t] = fmaf(a[t][e], a[t][e], n2[t]); }
            a[t][0] = pad ? 1.f : a[t][0];
            __builtin_amdgcn_raw_buffer_store_b64((u32x2){pk(a[t][0], a[t][1]), pk(a[t][2], a[t][3])}, rs, dh * KB + (16 * t + cc + 32 * (G & 1)) * 16 + 8 * (G >> 1), kvdst, 16); }
          const unsigned n0 = umax16(__float_as_uint(row_sum(half_sum(n2[0])))), n1 = umax16(__float_as_uint(row_sum(half_sum(n2[1]))));
          if (lane < 2) __builtin_amdgcn_raw_buffer_store_b32(lane ? n1 : n0, rs, lane * 8, KM_OFF + (pair * 64 + blk) * 16 + dh * 4, 16);
        } else {
          KVCHAIN(a[0] = mfma16(xf[0][k], W16[k], a[0]); a[1] = mfma16(xf[1][k], W16[k], a[1]));
          UNR for (int t = 0; t < 2; ++t) {
            UNR for (int e = 0; e < 4; ++e) a[t][e] = fmaf(a[t][e], rs_[t][e], nm_[t][e] * cskv.x);
            u32x2 o2 = {pk(a[t][0], a[t][1]), pk(a[t][2], a[t][3])};
            if (dh == 1 && cc >= 12) { o2.x = cc == 12 ? 0x3f803f80u : 0u; o2.y = o2.x; }
            __builtin_amdgcn_raw_buffer_store_b64(o2, rs, 2 * KB + t * KB + (16 * dh + cc + 32 * (G & 1)) * 16 + 8 * (G >> 1), kvdst, 16); }
        }
        if (pair == 0 && blk < 14) {
          const int to = L_RK + (32 * blk + 16 * (G >> 1) + 4 * (G & 1) + (cc >> 2)) * 56 + dh * 32 + (cc & 3) * 8;
          const u32x2 a_ = TRRD(to), c_ = TRRD(to + 8 * 56); STC(((u32x4){a_.x, a_.y, c_.x, c_.y}), lo, WKV_OFF + ((g * 2 + dh) * 14 + blk) * KB); }
      } else {
        X_STATS(mu, rstd);
      }
      STAMP(layer * 16 + 3);
      { f32x16 a = {}; u32x4 xf[28], wf[28];
        const int to = L_RQ + ((pair ? 4 : 8) * hh + (cc >> 2)) * 224 + w * 56 + (G & 1) * 32 + (cc & 3) * 8, to2 = to + (pair ? 8 : 4) * 224;
#define WQF(k) { const u32x2 a_ = TRRD(to + (k) * 16 * 224), c_ = TRRD(to2 + (k) * 16 * 224); wf[k] = (u32x4){a_.x, a_.y, c_.x, c_.y}; }
#define PUBA() { DRAIN(); if (lane == 0) __hip_atomic_store(flags + FA_OFF / 8 + ((g >> 1) * 256 + blk * 4 + (g & 1) * 2 + dh) * 16 + pair, tag, RLX); SBAR(); }
        u32x2 xo[4];
        if (pair == 0) { if (w >= 2) DRAIN();
          STAMP(1);
          asm volatile("s_waitcnt lgkmcnt(0)\n\ts_barrier" ::: "memory");
          STAMP(2);
        }
        UNR for (int k = 0; k < 4; ++k) { WQF(k) xf[k] = XFRAG(k); }
        SBAR();
        UNR for (int k = 0; k < 28; ++k) { if (k + 4 < 28) { WQF(k + 4) xf[k + 4] = XFRAG(k + 4); }
          a = mfma32(wf[k], xf[k], a); SBAR();
          if (k == PUBA_K && w < 2 && (g < 2 || pair == 0)) PUBA();
          if (k == PUBA_K && pair == 0) { UNR for (int a_ = 0; a_ < 4; ++a_) xo[a_] = *(const u32x2*)(x + (32 * blk + r) * DM + 32 * tlc + 8 * a_ + 4 * hh); SBAR(); } }
        float n2; EPI_ALPHA(a, csq, mu, rstd * QSCALE, n2);
        u32x4 f0, f1; pack16(a, f0, f1);
        if (hh) f1.z = bf_ceil(__builtin_amdgcn_sqrtf(n2) * 1.02f);
        LDS128(L_Q + w * 2 * KB + lo) = f0; LDS128(L_Q + w * 2 * KB + KB + lo) = f1;
        SBAR();
        if (pair == 0) unpack16((u32x4){xo[0].x, xo[0].y, xo[1].x, xo[1].y}, (u32x4){xo[2].x, xo[2].y, xo[3].x, xo[3].y}, xres);
      }
      STAMP(layer * 16 + 4);
      { gu64* fp = flags + FA_OFF / 8 + tid * 16 + pair; { unsigned spins = 0; while (__hip_atomic_load(fp, RLX) != tag && ++spins < SPIN_CAP) __builtin_amdgcn_s_sleep(POLL_SLEEP); } AFTER_POLL(); }
      STAMP(layer * 16 + 5);
      __syncthreads();
      STAMP(layer * 16 + 6);
      int base = KVA_OFF + pair * KVA_L + w * 64 * KB; OPQ(base);
      int woz = WOA_OFF + tlc * 32 * KB; OPQ(woz);
      int wvb = wbB + 56 * KB, wkb = wbB + 28 * KB; OPQ(wvb); OPQ(wkb);
      u32x4 q[4][2];
      const f32x4 km = __builtin_bit_cast(f32x4, LDC(lo, KM_OFF + pair * KB));
      UNR for (int h = 0; h < 4; ++h) { const int qo = L_Q + ((w + 1 + h) & 3) * 2 * KB + lo; q[h][0] = LDS128(qo); q[h][1] = LDS128(qo + KB); }
      bool stored = false; const int rot = (b >> 3) * ROTM;
      int armo = OT_OFF + blk * OT_BLK + head * 2 * KB; const int armx = XT_OFF + blk * XT_BLK + g * XT_WG, armx0 = pair ? WS_BYTES : armx;
#pragma unroll 1
      for (int attempt = 0; attempt < 2; ++attempt) {
        u32x4 Kt[16][2], Vt[16][2]; u64 wfl[4] = {0ull, 0ull, 0ull, 0ull};
#define TADR(t) (base + ((((t) + rot) & 15) << 12))
#define LDK(t) { Kt[t][0] = LDC(lo, TADR(t)); Kt[t][1] = LDC(lo + KB, TADR(t)); }
#define LDV(t) { Vt[t][0] = LDC(lo + 2 * KB, TADR(t)); Vt[t][1] = LDC(lo + 3 * KB, TADR(t)); }
        LDK(0) SBAR(); LDV(0) SBAR(); LDK(1) SBAR(); LDV(1) SBAR(); LDK(2)
        SBAR();
        if (attempt == 0) {
          const float kmax = __builtin_amdgcn_sqrtf(__uint_as_float(umax64(umax(__float_as_uint(km.x + km.y), __float_as_uint(km.z + km.w)))));
          UNR for (int h = 0; h < 4; ++h) { const unsigned m = bf_ceil(bflo(q[h][1].z) * kmax) ^ 0x8000u; q[h][1].z = hh ? m : q[h][1].z; }
        }
        f32x16 oo[4], sA, sB; u32x4 pA0 = q[0][0], pA1 = pA0, pB0 = pA0, pB1 = pA0; f32x4 ea, eb;
        PIN(q[0][1]); PIN(q[1][1]); PIN(q[2][1]); PIN(q[3][1]);
        asm volatile("s_nop 1");
        SBAR();
        MS0(sA, Kt[0][0], q[0][0]); MS1(sA, Kt[0][1], q[0][1]);
        NOP16V(sA); SBAR();
#define WPF(n) { if ((n) < 0) {} else if ((n) < 32) WZ[(n) & 31] = LDW(woz, (n)); else if ((n) < 60) Wv[((n) - 32) % 28] = LDW(wvb, (n) - 32); }
#define PUTO(i) { asm volatile("" : "+a"(oo[i])); const int s_ = L_C + (((w + 1 + (i)) & 3) * 3 + (i)) * 4 * KB + lo; UNR for (int e = 0; e < 4; ++e) \
          *(LAS f32x4*)(lds + s_ + e * KB) = (f32x4){oo[i][4 * e], oo[i][4 * e + 1], oo[i][4 * e + 2], oo[i][4 * e + 3]}; }
        UNR for (int t = 0; t < 16; ++t) {
          const int tp = t > 0 ? t - 1 : 0, tn = t < 15 ? t + 1 : 15;
          STEP((t == 0 ? 1 : t == 1 ? 2 : 0), sA, sB, Kt[t][0], Kt[t][1], q[1][0], q[1][1], oo[3], Vt[tp][0], Vt[tp][1], pB0, pB1, pA0, pA1, , WPF(5 * t - 15), WPF(5 * t - 14));
          STEP((t == 0 ? 2 : 0), sB, sA, Kt[t][0], Kt[t][1], q[2][0], q[2][1], oo[0], Vt[t][0], Vt[t][1], pA0, pA1, pB0, pB1, ,
               if (t + 2 < 16) Vt[(t + 2) & 15][0] = LDC(lo + 2 * KB, TADR(t + 2)), if (t + 2 < 16) Vt[(t + 2) & 15][1] = LDC(lo + 3 * KB, TADR(t + 2)); if (t >= 1) ARM(t - 1));
          STEP((t == 0 ? 2 : 0), sA, sB, Kt[t][0], Kt[t][1], q[3][0], q[3][1], oo[1], Vt[t][0], Vt[t][1], pB0, pB1, pA0, pA1, , WPF(5 * t - 13), WPF(5 * t - 12) if (t == 15) PUTO(0));
          STEP((t == 0 ? 2 : t == 15 ? 4 : 0), sB, sA, Kt[tn][0], Kt[tn][1], q[0][0], q[0][1], oo[2], Vt[t][0], Vt[t][1], pA0, pA1, pB0, pB1, ,
               if (t + 3 < 16) Kt[(t + 3) & 15][0] = LDC(lo, TADR(t + 3)), if (t + 3 < 16) Kt[(t + 3) & 15][1] = LDC(lo + KB, TADR(t + 3)); WPF(5 * t - 11) if (t == 15) PUTO(1));
          if (t == 0 && pair == 0) { UNR for (int j = 0; j < 4; ++j) wfl[j] = __hip_atomic_load(flags + FA_OFF / 8 + (256 + j * 64 + lane) * 16, RLX); SBAR(); }
          if (t == 2 && pair == 0) {
            if (__builtin_expect(!__all(wfl[0] == tag && wfl[1] == tag && wfl[2] == tag && wfl[3] == tag), 0)) { UNR for (int j = 0; j < 4; ++j) poll(flags + FA_OFF / 8 + (256 + j * 64 + lane) * 16, tag); }
            AFTER_POLL();
            if (b == 0 && tid == 0) __hip_atomic_store(flags + EP_OFF / 8, tag, RLX);
            SBAR(); }
        }
        C2(pB1, 2, eb)
        MO(oo[3], Vt[15][0], pB0); MO(oo[3], Vt[15][1], pB1);
        SBAR();
        PUTO(2)
        DRAIN();
        armo = WS_BYTES;
        STAMP(layer * 16 + 7);
        UNR for (int k = 0; k < 8; ++k) Wk[k] = LDW(wkb, k);
        SBAR();
        f32x16 o = oo[3];
        __syncthreads();
        UNR for (int s = 0; s < 3; ++s)
          UNR for (int e = 0; e < 4; ++e) { const f32x4 t = *(LAS f32x4*)(lds + L_C + (w * 3 + s) * 4 * KB + e * KB + lo);
            o[4 * e] += t.x; o[4 * e + 1] += t.y; o[4 * e + 2] += t.z; o[4 * e + 3] += t.w; }
        const float l = half_hi(o[12]);
        const bool bad = !__all(l >= L_MIN);
        if (lane == 0) *(LAS unsigned*)(lds + L_MISC + w * 4) = bad ? 1u : 0u;
        if (attempt == 1 || !bad) { if (!stored) O_STORE(layer, o, l); stored = true; }
        STAMP(layer * 16 + 8);
        __syncthreads();
        const u32x4 bw = *(LAS u32x4*)(lds + L_MISC);
        if (__builtin_expect(attempt == 1 || __builtin_amdgcn_readfirstlane(bw.x | bw.y | bw.z | bw.w) == 0u, 1)) break;
        float mx[4] = {-INFINITY, -INFINITY, -INFINITY, -INFINITY};
        UNR for (int h = 0; h < 4; ++h) q[h][1].z = hh ? 0u : q[h][1].z;
#pragma unroll 1
        for (int t = 0; t < 16; ++t) {
          const u32x4 k0 = LDC(lo, base + t * 4 * KB), k1 = LDC(lo + KB, base + t * 4 * KB);
          UNR for (int h = 0; h < 4; ++h) { f32x16 s = {}; s = mfma32(k0, q[h][0], s); s = mfma32(k1, q[h][1], s);
            UNR for (int e = 0; e < 16; ++e) mx[h] = fmaxf(mx[h], s[e]); }
        }
        UNR for (int h = 0; h < 4; ++h) { const float m = half_max(mx[h]); if (hh == 0) *(LAS float*)(lds + L_MISC + 1024 + (w * 4 + ((w + 1 + h) & 3)) * 128 + r * 4) = m; }
        __syncthreads();
        UNR for (int h = 0; h < 4; ++h) { float m = -INFINITY;
          UNR for (int s = 0; s < 4; ++s) m = fmaxf(m, *(LAS float*)(lds + L_MISC + 1024 + (s * 4 + ((w + 1 + h) & 3)) * 128 + r * 4));
          q[h][1].z = hh ? (bf_ceil(m) ^ 0x8000u) : q[h][1].z; }
      }
      if (tid == 0) __hip_atomic_store(FLAG(blk * 4 + g, 4 + layer), tag, RLX);
#define ONES() unsigned one2; asm volatile("v_mov_b32 %0, 0x3f803f80" : "=v"(one2)); const u32x4 ones = one2
#define CSV_CHAIN(k0, k1) { UNR for (int k = k0; k < k1; ++k) { if (!(k & 1) && k < 20) Wk[8 + (k >> 1)] = LDW(wkb, 8 + (k >> 1)); cv_ = mfma32(ones, Wv[k], cv_); SBAR(); } }
      ONES(); f32x16 cv_ = {};
      Z_PHASE(layer, false,
              CSV_CHAIN(0, 12), CSV_CHAIN(12, 28) csv = cv_[0]; PIN(csv),
              if ((k & 1) && k < 20) Wk[18 + (k >> 1)] = LDW(wkb, 18 + (k >> 1)); else if ((k & 1) && k < 24) WqB[(k >> 1) - 10] = LDW(wbB, (k >> 1) - 10));
    }
    {
      const int layer = 2 * pair + 1;
      IDX();
      const int wbB = WB_OFF + head * 3 * 28 * KB;
      int woz = WOB_OFF + tlc * 32 * KB; OPQ(woz);
      f32x16 csqB = {}, cskB = {};
      ONES();
#define CSK_CHAIN(k0, k1) { UNR for (int k = k0; k < k1; ++k) { if (!(k & 1) && k < 20) WqB[2 + (k >> 1)] = LDW(wbB, 2 + (k >> 1)); cskB = mfma32(Wk[k], ones, cskB); SBAR(); } }
      CSK_CHAIN(0, SPLIT_K)
      X_GATHER(layer, CSK_CHAIN(SPLIT_K, 28));
      float mu, rstd, qn;
      u32x4 K[9][2], V[9][2], q0, q1;
      const bool pv = lane < 8 && blk - 8 + lane >= 0;
      const int fbo = FB_OFF + ((blk - 8) * 16 + head) * 128 + pair * 32, fl = pv ? lane * 16 * 128 : 8 * 16 * 128;
      {
        const int dst = KVB_OFF + blk * KVB_BLK + head * 4 * KB;
        f32x16 av = {}, ak = {}, aq = {}, kf; u32x4 xf[28]; unsigned vp[8]; f32x4 rs_[4], nm_[4]; f32x2 s2_; float n0 = 0.f, n1 = 0.f;
        X_STATS_LD();
        UNR for (int k = 0; k < 6; ++k) xf[k] = XFRAG(k);
        SBAR();
        sfor<0, 84>([&](auto kc) __attribute__((always_inline)) { constexpr int k = decltype(kc)::value;
          if constexpr (k + 6 < 28) xf[k + 6] = XFRAG(k + 6);
          if constexpr (k % 5 == 1 || k % 5 == 3) { constexpr int i = (k / 5) * 2 + (k % 5 == 3);
            if constexpr (i < 16) WqB[12 + i] = LDW(wbB, 12 + i); else if constexpr (i < 23) WZ[i - 16] = LDW(woz, i - 16); else if constexpr (k > PUBK_K && i < 32) WZ[i - 22] = LDW(woz, i - 22); }
          if constexpr (k < 28) av = mfma32(xf[k], Wv[k], av); else if constexpr (k < 56) ak = mfma32(Wk[k - 28], xf[k - 28], ak); else { aq = mfma32(WqB[k - 56], xf[k - 56], aq); csqB = mfma32(WqB[k - 56], ones, csqB); }
          STATS_STEP(k)
          if constexpr (k >= 7 && k < 11) { constexpr int i = k - 7; rs_[i] = *(LAS f32x4*)(lds + L_MS + w * 256 + (8 * i + 4 * hh) * 4); nm_[i] = *(LAS f32x4*)(lds + L_MS + w * 256 + 128 + (8 * i + 4 * hh) * 4); }
          if constexpr (k >= 23 && k < 27) { constexpr int i = k - 23; nm_[i] *= csv; PIN(nm_[i]); }
          if constexpr (k == 30) asm volatile("" : "+a"(av));
          if constexpr (k >= 30 && k < 38) { constexpr int e = 2 * (k - 30), i = e >> 2, c = e & 3;
            const unsigned p0 = pk(fmaf(av[e], rs_[i][c], nm_[i][c]), fmaf(av[e + 1], rs_[i][c + 1], nm_[i][c + 1]));
            vp[e >> 1] = r < 28 ? p0 : (r == 28 ? 0x3f803f80u : 0u); PIN(vp[e >> 1]); }
          if constexpr (k == 38) { V[8][0] = (u32x4){vp[0], vp[1], vp[2], vp[3]}; V[8][1] = (u32x4){vp[4], vp[5], vp[6], vp[7]}; STC(V[8][0], lo + 2 * KB, dst); STC(V[8][1], lo + 3 * KB, dst); }
          if constexpr (k == 40) { cskB *= -mu * rstd; PIN(cskB); }
          if constexpr (k == 57) { asm volatile("" : "+a"(ak));
            kf = ak * rstd + cskB; UNR for (int e = 12; e < 16; ++e) kf[e] = hh ? 0.f : kf[e];
            pack16(kf, K[8][0], K[8][1]); K[8][1].z = hh ? 0x3f80u : K[8][1].z;
            STC(K[8][0], lo, dst); STC(K[8][1], lo + KB, dst);
            STAMP(layer * 16 + 3); }
          if constexpr (k >= 58 && k < 61) { constexpr int e0 = (k - 58) * 3; UNR for (int e = e0; e < e0 + 3 && e < 8; ++e) { n0 = fmaf(kf[e], kf[e], n0); n1 = fmaf(kf[8 + e], kf[8 + e], n1); } PIN(n0); PIN(n1); }
          if constexpr (k == 61) { n0 = half_sum(n0 + n1); PIN(n0); }
          if constexpr (k == 62) { const unsigned kown = umax32(__float_as_uint(n0)); if (lane == 0) __builtin_amdgcn_raw_buffer_store_b32(kown, rs, 8, fbo + 8 * 16 * 128, 16); }
          SBAR();
          if constexpr (k == PUBK_K) { DRAIN(); if (lane == 0) __hip_atomic_store(flags + (fbo + 8 * 16 * 128) / 8, tag, RLX); SBAR(); } });
        aq = (aq - csqB * mu) * (rstd * QSCALE); UNR for (int e = 12; e < 16; ++e) aq[e] = hh ? 0.f : aq[e];
        pack16(aq, q0, q1);
        n0 = 0.f; n1 = 0.f; UNR for (int e = 0; e < 8; ++e) { n0 = fmaf(aq[e], aq[e], n0); n1 = fmaf(aq[8 + e], aq[8 + e], n1); }
        qn = __builtin_amdgcn_sqrtf(half_sum(n0 + n1)) * 1.02f;
      }
      STAMP(layer * 16 + 4);
      const int kvb = KVB_OFF + (blk - 8) * KVB_BLK + head * 4 * KB;
#define SRCB(j) (blk - 8 + (j) < 0 ? ZERO_OFF : kvb + (j) * KVB_BLK)
#define LKB(j) { const int s_ = SRCB(j); K[j][0] = LDC(lo, s_); K[j][1] = LDC(lo + KB, s_); SBAR(); }
#define LVB(j) { const int s_ = SRCB(j); V[j][0] = LDC(lo + 2 * KB, s_); V[j][1] = LDC(lo + 3 * KB, s_); SBAR(); }
      { u32x4 fk; unsigned spins = 0;
        do { asm volatile("" ::: "memory"); fk = LDC(fl, fbo); } while (!__all(fk.x == (unsigned)tag && fk.y == (unsigned)(tag >> 32)) && ++spins < SPIN_CAP);
        AFTER_POLL();
        STAMP(layer * 16 + 5);
        LKB(7) LKB(6) LVB(7) LKB(5) LVB(6) LKB(4)
        const unsigned kmx = __builtin_amdgcn_readfirstlane(umax16(fk.z));
        q1.z = hh ? (bf_ceil(qn * __builtin_amdgcn_sqrtf(__uint_as_float(kmx))) ^ 0x8000u) : q1.z; }
      f32x16 o; float l;
#pragma unroll 1
      for (int attempt = 0; attempt < 2; ++attempt) {
        STAMP(layer * 16 + 6);
        f32x16 sA, sB; u32x4 pA0 = q0, pA1 = pA0, pB0 = pA0, pB1 = pA0; f32x4 ea, eb;
        PIN(q0); PIN(q1); PIN(K[8][0]); PIN(K[8][1]); asm volatile("s_nop 1"); SBAR();
        MS0(sA, K[8][0], q0); MS1(sA, K[8][1], q1);
        NOP16V(sA); SBAR();
#define MASKT(S, keep) { UNR for (int e = 0; e < 16; ++e) { const int row = (e & 3) + 8 * (e >> 2) + 4 * hh; S[e] = (keep) ? S[e] : -INFINITY; } PIN(S); SBAR(); }
#define WZ2(k) { WZ[k] = LDW(woz, k); WZ[k + 1] = LDW(woz, k + 1); }
        MASKT(sA, row <= r)
        STEP(1, sA, sB, K[7][0], K[7][1], q0, q1, o, V[8][0], V[8][1], pB0, pB1, pA0, pA1, LVB(5) LKB(3), LVB(4), LKB(2));
        STEP(2, sB, sA, K[6][0], K[6][1], q0, q1, o, V[8][0], V[8][1], pA0, pA1, pB0, pB1, , LVB(3), LKB(1));
        STEP(0, sA, sB, K[5][0], K[5][1], q0, q1, o, V[7][0], V[7][1], pB0, pB1, pA0, pA1, , LVB(2), LKB(0));
        STEP(0, sB, sA, K[4][0], K[4][1], q0, q1, o, V[6][0], V[6][1], pA0, pA1, pB0, pB1, , LVB(1), LVB(0));
        STEP(0, sA, sB, K[3][0], K[3][1], q0, q1, o, V[5][0], V[5][1], pB0, pB1, pA0, pA1, , , );
        STEP(0, sB, sA, K[2][0], K[2][1], q0, q1, o, V[4][0], V[4][1], pA0, pA1, pB0, pB1, , WZ2(10), );
        STEP(0, sA, sB, K[1][0], K[1][1], q0, q1, o, V[3][0], V[3][1], pB0, pB1, pA0, pA1, , WZ2(12), WZ2(14));
        STEP(0, sB, sA, K[0][0], K[0][1], q0, q1, o, V[2][0], V[2][1], pA0, pA1, pB0, pB1, , WZ2(16), WZ2(18));
        MASKT(sA, row > r)
        STEP(4, sA, sB, K[0][0], K[0][1], q0, q1, o, V[1][0], V[1][1], pB0, pB1, pA0, pA1, , , );
        C2(pA1, 2, eb)
        MO(o, V[0][0], pA0); MO(o, V[0][1], pA1);
        SBAR();
        l = half_hi(o[12]);
        if (__builtin_expect(attempt == 1 || __all(l >= L_MIN), 1)) break;
        q1.z = hh ? 0u : q1.z;
        float mx = -INFINITY;
        UNR for (int j = 8; j >= 0; --j) {
          f32x16 s = {}; s = mfma32(K[j][0], q0, s); s = mfma32(K[j][1], q1, s); float m_ = -INFINITY;
          UNR for (int e = 0; e < 16; ++e) { const int row = (e & 3) + 8 * (e >> 2) + 4 * hh; float v = s[e];
            if (j == 8) v = (row <= r) ? v : -INFINITY; if (j == 0) v = (row > r) ? v : -INFINITY; m_ = fmaxf(m_, v); }
          mx = (blk - 8 + j >= 0) ? fmaxf(mx, m_) : mx; SBAR(); }
        mx = half_max(mx);
        q1.z = hh ? (bf_ceil(mx) ^ 0x8000u) : q1.z;
      }
      STAMP(layer * 16 + 7);
      O_STORE(layer, o, l);
      STAMP(layer * 16 + 8);
      HINT(4 + layer);
      int wkv = (g < 2 && w < 2) ? WKV_OFF + (g * 2 + dh) * 14 * KB : WS_BYTES; OPQ(wkv);
      const int G = lane >> 4, cc = lane & 15, to = L_RQ + (4 * hh + (cc >> 2)) * 224 + w * 56 + (G & 1) * 32 + (cc & 3) * 8, to2 = to + 8 * 224;
      u32x4 wf[28]; csq = (f32x16){};
#define CSQ_CHAIN(k0, k1) { if (k0 == 0) { UNR for (int k = 0; k < 6; ++k) WQF(k) SBAR(); } \
        UNR for (int k = k0; k < k1; ++k) { if (k + 6 < 28) WQF(k + 6) if (!(k & 1) && k < 24) WZ[20 + (k >> 1)] = LDW(woz, 20 + (k >> 1)); csq = mfma32(wf[k], ones, csq); SBAR(); } }
      Z_PHASE(layer, true,
              CSQ_CHAIN(0, SPLIT_K - 2), CSQ_CHAIN(SPLIT_K - 2, 28),
              if ((k & 1) && k < 28) W16[k >> 1] = LDW(wkv, k >> 1));
      cskv = (f32x4){0.f, 0.f, 0.f, 0.f};
#define CSKV_CHAIN() { if (g == 0) { UNR for (int k = 0; k < 14; ++k) cskv = mfma16(W16[k], ones, cskv); } else { UNR for (int k = 0; k < 14; ++k) cskv = mfma16(ones, W16[k], cskv); } }
      CSKV_CHAIN(); DRAIN();
      X_GATHER(layer + 1, );
    }
  }
}

extern "C" void kernel_launch(void* const* d_in, const int* in_sizes, int n_in, void* d_out, int out_size, void* d_ws, size_t ws_size, hipStream_t stream) {
  (void)in_sizes; (void)n_in; (void)out_size; (void)ws_size;
  (void)hipFuncSetAttribute((const void*)main_kernel, hipFuncAttributeMaxDynamicSharedMemorySize, L_BYTES);
  main_kernel<<<256, 256, L_BYTES, stream>>>((const u16*)d_in[0], (const u16*)d_in[1], (const u16*)d_in[2], (const u16*)d_in[3], (const u16*)d_in[4],
                                             (const u16*)d_in[6], (const u16*)d_in[7], (unsigned char*)d_ws, (u16*)d_out);
}
```

```cpp
#include <hip/hip_runtime.h>
#include <hip/hip_bf16.h>

typedef float f32x16 __attribute__((ext_vector_type(16)));
typedef float f32x4 __attribute__((ext_vector_type(4)));
typedef float f32x2 __attribute__((ext_vector_type(2)));
typedef unsigned u32x4 __attribute__((ext_vector_type(4)));
typedef unsigned u32x2 __attribute__((ext_vector_type(2)));
typedef short bf16x8 __attribute__((ext_vector_type(8)));
typedef short s16x4 __attribute__((ext_vector_type(4)));
typedef __bf16 bf2 __attribute__((ext_vector_type(2)));
typedef unsigned short u16;
typedef unsigned long long u64;
#define GAS __attribute__((address_space(1)))
#define LAS __attribute__((address_space(3)))
typedef GAS u64 gu64;
#define RLX __ATOMIC_RELAXED, __HIP_MEMORY_SCOPE_AGENT
#define SBAR() __builtin_amdgcn_sched_barrier(0)
#define PIN(x) asm volatile("" : "+v"(x))
#define OPQ(x) asm volatile("" : "+s"(x))
#define DRAIN() asm volatile("s_waitcnt vmcnt(0)" ::: "memory")
#define UNR _Pragma("unroll")

constexpr int DM = 448, NB = 64, NTL = 14, KB = 1024;
constexpr int EP_OFF = 0;
constexpr int FA_OFF = 1 * KB;
constexpr int FL_OFF = FA_OFF + 2 * 8 * 4 * KB;
constexpr int KM_OFF = FL_OFF + 256 * 128;
constexpr int FB_OFF = KM_OFF + 2 * KB;
constexpr int CS_OFF = FB_OFF + 128 * KB;
constexpr int ZERO_OFF = CS_OFF + 6 * KB;
constexpr int WKV_OFF = ZERO_OFF + 4 * KB;
constexpr int KVA_OFF = WKV_OFF + 56 * KB;
constexpr int KVA_L = NB * 4 * KB;
constexpr int WOA_OFF = KVA_OFF + 2 * KVA_L;
constexpr int WOB_OFF = WOA_OFF + 14 * 32 * KB;
constexpr int WB_OFF = WOB_OFF + 14 * 32 * KB;
constexpr int XT_OFF = WB_OFF + 16 * 3 * 28 * KB;
constexpr int XT_WG = 9 * KB, XT_BLK = 4 * XT_WG, XT_L = NB * XT_BLK;
constexpr int OT_OFF = XT_OFF + 2 * XT_L;
constexpr int OT_BLK = 32 * KB, OT_L = NB * OT_BLK;
constexpr int KVB_OFF = OT_OFF + 2 * OT_L;
constexpr int KVB_BLK = 64 * KB;
constexpr int WS_BYTES = KVB_OFF + NB * KVB_BLK;
#define FLAG(wg, word) (flags + (FL_OFF + (wg) * 128) / 8 + (word))
constexpr int L_X = 0;
constexpr int L_O = 0, L_C = 0;
constexpr int L_MISC = 48 * KB;
constexpr int L_Q = 52 * KB;
constexpr int L_RK = 36 * KB;
constexpr int L_RQ = L_RK + 448 * 56;
constexpr int L_MS = 159 * KB;
constexpr int L_BYTES = 160 * KB;
static_assert(L_RQ + 448 * 224 + 64 <= L_MS && L_RQ % 16 == 0, "lds");

#define STAMP(n) do {} while (0)
#define STAMP_OUT()
constexpr float QSCALE = 0.18898223650461361f * 1.4426950408889634f;
constexpr unsigned SPIN_CAP = 1u << 16;
#define POLL_SLEEP 4
#define PUBV_K 52
#define PUBK_K 71
#define ROTM 0
#define PUBA_K 13
#define SPLIT_K 16
constexpr float L_MAX = 1e36f;
constexpr float L_MIN = 1e-18f;

__device__ __forceinline__ unsigned pk(float a, float b) { f32x2 v = {a, b}; bf2 r = __builtin_convertvector(v, bf2); return __builtin_bit_cast(unsigned, r); }
__device__ __forceinline__ float bflo(unsigned u) { return __uint_as_float(u << 16); }
__device__ __forceinline__ float bfhi(unsigned u) { return __uint_as_float(u & 0xffff0000u); }
__device__ __forceinline__ f32x16 mfma32(u32x4 a, u32x4 b, f32x16 c) {
  return __builtin_amdgcn_mfma_f32_32x32x16_bf16(__builtin_bit_cast(bf16x8, a), __builtin_bit_cast(bf16x8, b), c, 0, 0, 0); }
__device__ __forceinline__ f32x4 mfma16(u32x4 a, u32x4 b, f32x4 c) {
  return __builtin_amdgcn_mfma_f32_16x16x32_bf16(__builtin_bit_cast(bf16x8, a), __builtin_bit_cast(bf16x8, b), c, 0, 0, 0); }
__device__ __forceinline__ float half_sum(float v) { auto r = __builtin_amdgcn_permlane32_swap(__float_as_uint(v), __float_as_uint(v), false, false); return __uint_as_float(r[0]) + __uint_as_float(r[1]); }
__device__ __forceinline__ float half_max(float v) { auto r = __builtin_amdgcn_permlane32_swap(__float_as_uint(v), __float_as_uint(v), false, false); return fmaxf(__uint_as_float(r[0]), __uint_as_float(r[1])); }
__device__ __forceinline__ float half_hi(float v) { auto r = __builtin_amdgcn_permlane32_swap(__float_as_uint(v), __float_as_uint(v), false, false); return __uint_as_float(r[1]); }
__device__ __forceinline__ float row_sum(float v) { auto r = __builtin_amdgcn_permlane16_swap(__float_as_uint(v), __float_as_uint(v), false, false); return __uint_as_float(r[0]) + __uint_as_float(r[1]); }
__device__ __forceinline__ float dot2(unsigned a, unsigned b, float c) { return __builtin_amdgcn_fdot2_f32_bf16(__builtin_bit_cast(bf2, a), __builtin_bit_cast(bf2, b), c, false); }
template <int C> __device__ __forceinline__ float dppf(float v) { return __uint_as_float(__builtin_amdgcn_update_dpp(0u, __float_as_uint(v), C, 0xf, 0xf, true)); }
__device__ __forceinline__ float sum8(float v) { return half_sum(row_sum(v + dppf<0xb1>(v))); }
template <int N> __device__ __forceinline__ unsigned ror(unsigned v) { return __builtin_amdgcn_update_dpp(0u, v, 0x120 + N, 0xf, 0xf, true); }
__device__ __forceinline__ unsigned umax(unsigned a, unsigned b) { return a > b ? a : b; }
__device__ __forceinline__ unsigned umax16(unsigned v) { v = umax(v, ror<1>(v)); v = umax(v, ror<2>(v)); v = umax(v, ror<4>(v)); return umax(v, ror<8>(v)); }
__device__ __forceinline__ unsigned umax32(unsigned v) { v = umax16(v); auto r = __builtin_amdgcn_permlane16_swap(v, v, false, false); return umax(r[0], r[1]); }
__device__ __forceinline__ unsigned umax64(unsigned v) { v = umax32(v); auto r = __builtin_amdgcn_permlane32_swap(v, v, false, false); return umax(r[0], r[1]); }
__device__ __forceinline__ unsigned bf_ceil(float v) { unsigned u = __float_as_uint(v); unsigned t = u >> 16; return (v > 0.f && (u & 0xffffu)) ? t + 1u : t; }
__device__ __forceinline__ void pack16(const f32x16& v, u32x4& f0, u32x4& f1) {
  f0 = (u32x4){pk(v[0], v[1]), pk(v[2], v[3]), pk(v[4], v[5]), pk(v[6], v[7])};
  f1 = (u32x4){pk(v[8], v[9]), pk(v[10], v[11]), pk(v[12], v[13]), pk(v[14], v[15])}; }
__device__ __forceinline__ void unpack16(const u32x4 f0, const u32x4 f1, f32x16& v) {
  UNR for (int i = 0; i < 4; ++i) { v[2 * i] = bflo(f0[i]); v[2 * i + 1] = bfhi(f0[i]); v[8 + 2 * i] = bflo(f1[i]); v[9 + 2 * i] = bfhi(f1[i]); } }
__device__ __forceinline__ void tile_stats(const f32x16& v, float& mean, float& m2) {
  const float s = (((v[0] + v[1]) + (v[2] + v[3])) + ((v[4] + v[5]) + (v[6] + v[7]))) + (((v[8] + v[9]) + (v[10] + v[11])) + ((v[12] + v[13]) + (v[14] + v[15])));
  mean = half_sum(s) * (1.f / 32.f);
  float q0 = 0.f, q1 = 0.f;
  UNR for (int i = 0; i < 8; ++i) { const float d0 = v[i] - mean, d1 = v[8 + i] - mean; q0 = fmaf(d0, d0, q0); q1 = fmaf(d1, d1, q1); }
  m2 = half_sum(q0 + q1);
}
template <int V> struct IC { static constexpr int value = V; };
template <int I, int N, class F> __device__ __forceinline__ void sfor(F&& f) { if constexpr (I < N) { f(IC<I>{}); sfor<I + 1, N>(f); } }
__device__ __forceinline__ u32x4 mkdesc(const void* p, unsigned bytes) { return (u32x4){(unsigned)(size_t)p, (unsigned)((size_t)p >> 32) & 0xffffu, bytes, 0x00020000u}; }

#define LDC(voff, soff) __builtin_amdgcn_raw_buffer_load_b128(rs, (voff), (soff), 16)
#define STC(v, voff, soff) __builtin_amdgcn_raw_buffer_store_b128((v), rs, (voff), (soff), 16)
#define LDS128(off) (*(LAS u32x4*)(lds + (off)))
#define LDW(base, k) LDC(lo + ((k) & 3) * KB, (base) + ((k) >> 2) * 4 * KB)
#define MS0(S, A, B) asm volatile("v_mfma_f32_32x32x16_bf16 %0, %1, %2, 0" : "=&v"(S) : "v"(A), "v"(B))
#define MS1(S, A, B) asm volatile("v_mfma_f32_32x32x16_bf16 %0, %1, %2, %0" : "+v"(S) : "v"(A), "v"(B))
#define MO(O, A, B) { O = mfma32(A, B, O); asm volatile("" : "+a"(O)); }
#define EX(x) __builtin_amdgcn_exp2f(x)
#define E4(E, S, i) { E[0] = EX(S[i]); E[1] = EX(S[i + 1]); E[2] = EX(S[i + 2]); E[3] = EX(S[i + 3]); PIN(E); SBAR(); }
#define C2(P, j, E) { P[j] = pk(E[0], E[1]); P[j + 1] = pk(E[2], E[3]); PIN(P); SBAR(); }
#define STEP(MODE, SC, SN, KN0, KN1, QN0, QN1, OP, VP0, VP1, PP0, PP1, PC0, PC1, W0, LDA, LDB) \
  W0; if (!((MODE) & 4)) MS0(SN, KN0, QN0); SBAR(); E4(ea, SC, 0) if (!((MODE) & 1)) C2(PP1, 2, eb) \
  if (!((MODE) & 4)) MS1(SN, KN1, QN1); LDA; SBAR(); E4(eb, SC, 4) C2(PC0, 0, ea) \
  if ((MODE) & 2) OP = (f32x16){}; \
  if (!((MODE) & 1)) MO(OP, VP0, PP0); SBAR(); E4(ea, SC, 8) C2(PC0, 2, eb) \
  if (!((MODE) & 1)) MO(OP, VP1, PP1); LDB; SBAR(); E4(eb, SC, 12) C2(PC1, 0, ea)
#define NOP16V(x) asm volatile("s_nop 7\n\ts_nop 7" : "+v"(x))
#define NOP16A(x) asm volatile("s_nop 7\n\ts_nop 7" : "+a"(x))
#define VMW(n) asm volatile("s_waitcnt vmcnt(" #n ")")
#define ALD(dst, soff, imm) asm volatile("buffer_load_dwordx4 %0, %1, %2, %3 offen offset:" #imm " sc1" : "+v"(dst) : "v"(lo), "s"(desc), "s"(soff))
#define ALD0(dst, soff, imm) asm volatile("buffer_load_dwordx4 %0, %1, %2, %3 offen offset:" #imm " sc1" : "=v"(dst) : "v"(lo), "s"(desc), "s"(soff))
#define ALDA(dst, soff, imm) asm volatile("buffer_load_dwordx4 %0, %1, %2, %3 offen offset:%4 sc1" : "=a"(dst) : "v"(lo), "s"(desc), "s"(soff), "i"(imm))
#define LDWA(dst, base, k) { const int so_ = (base) + ((k) >> 2) * 4 * KB; ALDA(dst, so_, ((k) & 3) * KB); }
#define ADMA(ldsaddr, voff, dsc, soff) asm volatile("s_mov_b32 m0, %0\n\ts_nop 0\n\tbuffer_load_dwordx4 %1, %2, %3 offen lds" :: "s"(ldsaddr), "v"(voff), "s"(dsc), "s"(soff) : "memory")
#define DMAW(base, k) { const unsigned la_ = L_W + w * L_WW + (k) * KB; const int so_ = (base) + (k) * KB; ADMA(la_, lo, desc, so_); }
#define TRRD(off) __builtin_bit_cast(u32x2, __builtin_amdgcn_ds_read_tr16_b64_v4i16((LAS s16x4*)(lds + (off))))

__device__ __forceinline__ void poll(gu64* p, u64 tag) {
  unsigned spins = 0;
  while (__hip_atomic_load(p, RLX) != tag) { if (++spins > SPIN_CAP) break; }
}
#define OUTST(v, p) *(p) = v
#define AFTER_POLL() __builtin_amdgcn_fence(__ATOMIC_ACQUIRE, "wavefront")

__global__ __launch_bounds__(256, 1) void main_kernel(const u16* __restrict__ x, const u16* __restrict__ wq, const u16* __restrict__ wk, const u16* __restrict__ wv,
                                                      const u16* __restrict__ woa, const u16* __restrict__ wb, const u16* __restrict__ wob,
                                                      unsigned char* __restrict__ ws, u16* __restrict__ out) {
  extern __shared__ __attribute__((aligned(16))) unsigned char smem[];
  const __amdgpu_buffer_rsrc_t rs = __builtin_amdgcn_make_buffer_rsrc((void*)ws, 0, WS_BYTES, 0x00020000);
  const u32x4 desc = mkdesc(ws, WS_BYTES);
  LAS unsigned char* lds = (LAS unsigned char*)smem;
  gu64* flags = (gu64*)ws;
  int tid_ = threadIdx.x, b_ = blockIdx.x, w_ = __builtin_amdgcn_readfirstlane(threadIdx.x >> 6);
#define IDX() asm volatile("" : "+v"(tid_)); asm volatile("" : "+s"(b_), "+s"(w_)); \
  const int tid = tid_, b = b_, w = w_, lane = tid & 63, r = lane & 31, hh = lane >> 5, lo = lane * 16; \
  const int blk = (b & 7) * 8 + (b >> 5), g = (b >> 3) & 3, head = 4 * g + w, tl = head; const bool act = tl < NTL; const int tlc = act ? tl : 0; \
  const int dh = w & 1, th = w >> 1; (void)r; (void)hh; (void)dh; (void)th; (void)tlc; (void)blk; (void)lo
  u64 pc_; unsigned len_;
  asm volatile("s_getpc_b64 %0\n.Lcode_here:\n\ts_mov_b32 %1, .Lcode_last-.Lcode_here" : "=s"(pc_), "=s"(len_));
#define CODEPF(ldsoff) { const u32x4 dc = mkdesc((const void*)pc_, len_); const int vo = ((b >> 3) * 4 + w) * 512 + (lane & 3) * 128; const unsigned la = (ldsoff) + w * 256; \
    asm volatile("s_mov_b32 m0, %0\n\ts_nop 0\n\tbuffer_load_dword %1, %2, 0 offen lds" :: "s"(la), "v"(vo), "s"(dc) : "memory"); }
  STAMP(64);

  f32x16 xres, csq;
  f32x4 cskv;
  u32x4 W16[14];
  int rtb = 0, rta = 0, rts = 0, rtn = 0;
  u64 tag;
  {
    IDX();
    u64 ep; asm volatile("global_load_dwordx2 %0, %1, %2 sc1" : "=v"(ep) : "v"(0), "s"(ws));
    u32x4 xr[7];
    { const u32x4 dx = mkdesc(x + 32 * blk * DM, 32 * 896); const int vo = (8 * w + ((lane >> 1) & 7)) * 896 + (lane >> 4) * 32 + (lane & 1) * 16;
      UNR for (int f = 0; f < 7; ++f) asm volatile("buffer_load_dwordx4 %0, %1, %2, 0 offen offset:%3" : "=v"(xr[f]) : "v"(vo), "s"(dx), "i"(f * 128)); }
    CODEPF(L_X + 31 * KB);
    const int n = blk * 2 + (g & 1), nw = n - 84;
    const int c3 = (n * 37) >> 10, ks = n - 28 * c3;
    const int t7 = tid & 127;
    int vq[7];
    const u32x4 dq = mkdesc(wq, 448 * 448 * 2);
#define WQDMA(u0) { int u = (u0) + (blk & 7); u = u >= 7 ? u - 7 : u; UNR for (int k = 0; k < 7; ++k) { \
      const unsigned la = L_RQ + (2 * (7 * u + k) + dh) * KB; const int so = u * 64 * 896; ADMA(la, vq[k], dq, so); } }
    if (w >= 2) {
      UNR for (int k = 0; k < 7; ++k) { const int c = t7 + 128 * k, row = (c * 4682) >> 16; vq[k] = row * 896 + (c - row * 14) * 16 + g * 224; }
      WQDMA(0) WQDMA(1)
    } else if (g < 2 || n >= 84) { const u32x4 dk = g < 2 ? mkdesc(g ? wv : wk, 448 * 56) : mkdesc((nw & 16 ? wob : woa) + (nw & 15) * 28 * 448, n < 116 ? 448 * 56 : 0);
      UNR for (int k = 0; k < 14; ++k) { const int i = 2 * k + w; const unsigned la = i < 24 ? L_RK + i * KB : L_X + 32 * KB; const int so = i < 24 ? i * KB : 448 * 56;
        if (k == 12) { const unsigned l2 = L_RK + 24 * KB; const int s2 = 24 * KB; if (lane < 32) ADMA(l2, lo, dk, s2); } else ADMA(la, lo, dk, so); }
    } else { const u32x4 dk = mkdesc(wb + 16 * ks * 1344 + 448 * c3, 16 * 2688);
      UNR for (int k = 0; k < 7; ++k) { const int c = t7 + 128 * k, row = (c * 1171) >> 16; const unsigned la = L_RK + (2 * k + w) * KB;
        ADMA(la, row * 2688 + (c - row * 56) * 16, dk, 0); }
      UNR for (int k = 0; k < 7; ++k) { const unsigned la = L_X + 32 * KB; const int so = 16 * 2688; ADMA(la, lo, dk, so); } }
    STAMP(67);
    asm volatile("s_waitcnt vmcnt(15)" : "+v"(xr[0]), "+v"(xr[1]), "+v"(xr[2]), "+v"(xr[3]), "+v"(xr[4]), "+v"(xr[5]), "+v"(xr[6]), "+v"(ep) :: "memory");
    { float s0 = 0.f, s1 = 0.f, q0 = 0.f, q1 = 0.f;
      UNR for (int f = 0; f < 7; ++f) UNR for (int c = 0; c < 4; c += 2) { s0 = dot2(xr[f][c], 0x3f803f80u, s0); q0 = dot2(xr[f][c], xr[f][c], q0); s1 = dot2(xr[f][c + 1], 0x3f803f80u, s1); q1 = dot2(xr[f][c + 1], xr[f][c + 1], q1); }
      SBAR(); if (w >= 2) WQDMA(2) SBAR();
      const float mu = sum8(s0 + s1) * (1.f / 448.f);
      const float rstd = __builtin_amdgcn_rsqf(fmaxf(fmaf(-mu, mu, sum8(q0 + q1) * (1.f / 448.f)), 0.f) + 1e-5f), nm = -mu * rstd;
      UNR for (int f = 0; f < 7; ++f) {
        u32x4 o; UNR for (int e = 0; e < 4; ++e) { const f32x2 t = (f32x2){bflo(xr[f][e]), bfhi(xr[f][e])} * rstd + nm; o[e] = pk(t.x, t.y); }
        LDS128(L_X + (f >> 1) * XT_WG + (f & 1) * 4 * KB + (lane >> 4) * KB + (8 * w + ((lane >> 1) & 7) + 32 * (lane & 1)) * 16) = o; }
      const float one = 32.f * (1.f - 1e-5f);
      *(LAS f32x4*)(lds + L_X + w * XT_WG + 8192 + lo) = (f32x4){0.f, one, 0.f, one};
    }
    tag = (((u64)(unsigned)__builtin_amdgcn_readfirstlane((unsigned)(ep >> 32)) << 32) | (unsigned)__builtin_amdgcn_readfirstlane((unsigned)ep)) + 1ull;
    if (w < 2) DRAIN();
    STAMP(68);
    asm volatile("s_waitcnt lgkmcnt(0)\n\ts_barrier" ::: "memory");
    STAMP(65);
    const int q4 = (lane & 15) >> 2, p4 = lane & 3, gq = lane >> 4;
    cskv = (f32x4){0.f, 0.f, 0.f, 0.f}; csq = (f32x16){};
    if (w >= 2) {
      WQDMA(3) WQDMA(4) WQDMA(5) WQDMA(6)
      {
        const int vo = lane < 13 ? ((b * 2 + dh) * 13 + lane) * 128 + KB : lane * 256 * KB; const unsigned la = L_X + 33 * KB + w * 256;
        asm volatile("s_mov_b32 m0, %0\n\ts_nop 0\n\tbuffer_load_dword %1, %2, 0 offen sc1 lds" :: "s"(la), "v"(vo), "s"(desc) : "memory"); }
    } else if (g < 2) { const int to = L_RK + (8 * gq + q4) * 56 + dh * 32 + p4 * 8;
      UNR for (int k = 0; k < 14; ++k) { const u32x2 a = TRRD(to + k * 32 * 56), c = TRRD(to + k * 32 * 56 + 4 * 56); W16[k] = (u32x4){a.x, a.y, c.x, c.y}; }
    } else {
      const int to = L_RK + (4 * hh + q4) * 896 + (gq & 1) * 32 + p4 * 8;
      if (n < 84) {
        UNR for (int j = 0; j < 8; ++j) { const int h = 8 * w + j; const u32x2 a = TRRD(to + h * 56), c = TRRD(to + h * 56 + 8 * 896); W16[j] = (u32x4){a.x, a.y, c.x, c.y}; }
        rtb = WB_OFF + ((24 * w + c3) * 28 + ks) * KB; rta = 168 * KB; rts = 84 * KB; rtn = 8;
      } else {
        UNR for (int j = 0; j < 7; ++j) { const int t = 2 * j + w;
          const u32x2 a = TRRD(to + t * 64), c = TRRD(to + t * 64 + 8 * 896), d = TRRD(to + t * 64 + 16 * 896), e = TRRD(to + t * 64 + 24 * 896);
          W16[2 * j] = (u32x4){a.x, a.y, c.x, c.y}; W16[2 * j + 1] = (u32x4){d.x, d.y, hh ? 0u : e.x, hh ? 0u : e.y}; }
        rtb = n < 116 ? (nw & 16 ? WOB_OFF : WOA_OFF) + (w * 32 + 2 * (nw & 15)) * KB : ZERO_OFF + w * 2 * KB;
        rta = 64 * KB; rts = KB; rtn = n < 116 ? 14 : n == 116 ? 2 : 0;
      }
    }
    STAMP(66);
  }

#define GPUT(N, ldsoff) unsigned m_ = 0u; \
    UNR for (int f = 0; f < N; ++f) { LDS128((ldsoff) + f * KB + lo) = t_[f]; m_ = umax(umax(m_, t_[f].x), t_[f].y); m_ = umax(umax(m_, t_[f].z), t_[f].w); PIN(m_); SBAR(); }
#define GATHER(N, src, ldsoff, flagp, ...) { \
    { u32x4 t_[N]; UNR for (int f = 0; f < N; ++f) { t_[f] = LDW(src, f); SBAR(); } \
      SBAR(); __VA_ARGS__; SBAR(); \
      GPUT(N, ldsoff) \
      if (__builtin_expect(!__all(m_ != ~0u), 0)) { poll(flagp, tag); \
        for (unsigned spins_ = 0; spins_ < SPIN_CAP; ++spins_) { asm volatile("" ::: "memory"); \
          u32x4 t_[N]; UNR for (int f = 0; f < N; ++f) t_[f] = LDW(src, f); \
          GPUT(N, ldsoff) if (__all(m_ != ~0u)) break; } } } \
    asm volatile("s_waitcnt lgkmcnt(0)\n\ts_barrier" ::: "memory"); }
#define SANE(f) { UNR for (int e_ = 0; e_ < 4; ++e_) f[e_] = f[e_] < 0xfffffffeu ? f[e_] : 0xfffffffeu; }
#define HINT(word) { __builtin_amdgcn_s_barrier(); if (tid == 0) __hip_atomic_store(FLAG(blk * 4 + g, (word)), tag, RLX); }
#define ARM(t) { const u32x4 a_ = {~0u, ~0u, ~0u, ~0u}; \
    if ((t) < 4) STC(a_, lo + ((t) & 1) * KB, armo + ((t) >> 1) * OT_L); \
    else if ((t) < 10) { const int d_ = (t) < 7 ? armx + XT_L : armx0; \
      if (((t) - 4) % 3 < 2) STC(a_, lo + (((t) - 4) % 3) * KB, d_ + w * 2048); else __builtin_amdgcn_raw_buffer_store_b32(~0u, rs, lane * 4, d_ + 8192 + w * 256, 16); } }
#define X_GATHER(layer, WORK) { \
    STAMP((layer) * 16 + 0); \
    STAMP((layer) * 16 + 1); \
    int src_ = XT_OFF + ((layer) & 1) * XT_L + blk * XT_BLK + w * XT_WG; OPQ(src_); \
    GATHER(9, src_, L_X + w * XT_WG, FLAG(blk * 4 + w, (layer)), WORK) \
    STAMP((layer) * 16 + 2); }
#define X_STATS_LD() f32x2 st_[7]; \
    UNR for (int k = 0; k < 7; ++k) st_[k] = *(LAS f32x2*)(lds + L_X + (k >> 1) * XT_WG + 8192 + (k & 1) * 512 + lane * 8)
#define X_STATS(mu, rstd) { f32x2 s2_; STATS_STEP(0) STATS_STEP(1) STATS_STEP(2) STATS_STEP(3) }
#define STATS_STEP(k) \
    if constexpr (k == 0) { s2_ = ((st_[0] + st_[1]) + (st_[2] + st_[3])) + ((st_[4] + st_[5]) + st_[6]); PIN(s2_); } \
    if constexpr (k == 1) { mu = half_sum(s2_.x) * (1.f / 448.f); s2_.y = half_sum(s2_.y); PIN(mu); PIN(s2_); } \
    if constexpr (k == 2) { rstd = __builtin_amdgcn_rsqf(fmaxf(fmaf(-mu, mu, s2_.y * (1.f / 448.f)), 0.f) + 1e-5f); PIN(rstd); } \
    if constexpr (k == 3) *(LAS float*)(lds + L_MS + w * 256 + lane * 4) = hh ? -mu * rstd : rstd;
#define XFRAG(ks) LDS128(L_X + ((ks) >> 3) * XT_WG + ((ks) & 7) * KB + lo)
#define LDCS(cs, col) { int o_ = CS_OFF + (col) * 4; OPQ(o_); UNR for (int q4 = 0; q4 < 4; ++q4) { const f32x4 c_ = __builtin_bit_cast(f32x4, LDC(hh * 16 + q4 * 32, o_)); \
    cs[4 * q4] = c_.x; cs[4 * q4 + 1] = c_.y; cs[4 * q4 + 2] = c_.z; cs[4 * q4 + 3] = c_.w; } }
#define EPI_ALPHA(a, cs, mu, sc, n2) { const float nm_ = -(mu) * (sc); UNR for (int e = 0; e < 16; ++e) a[e] = fmaf(a[e], (sc), nm_ * cs[e]); \
    UNR for (int e = 12; e < 16; ++e) a[e] = hh ? 0.f : a[e]; \
    float n0_ = 0.f, n1_ = 0.f; UNR for (int e = 0; e < 8; ++e) { n0_ = fmaf(a[e], a[e], n0_); n1_ = fmaf(a[8 + e], a[8 + e], n1_); } n2 = half_sum(n0_ + n1_); }
#define PUBLISH(word) { DRAIN(); __syncthreads(); if (tid == 0) __hip_atomic_store(FLAG(blk * 4 + g, (word)), tag, RLX); }
#define DELAY()
#define O_STORE(layer, o, l) { DELAY() const float inv_ = __builtin_amdgcn_rcpf(l); f32x16 v_; \
    UNR for (int e = 0; e < 16; ++e) v_[e] = o[e] * inv_; \
    u32x4 f0_, f1_; pack16(v_, f0_, f1_); if (hh) { f1_.z = 0u; f1_.w = 0u; } SANE(f0_) SANE(f1_) \
    const int dst_ = OT_OFF + ((layer) & 1) * OT_L + blk * OT_BLK + head * 2 * KB; STC(f0_, lo, dst_); STC(f1_, lo + KB, dst_); }
#define Z_PHASE(layer, LAST, PRE, PRE2, SIDE) { \
    STAMP((layer) * 16 + 9); \
    PRE; \
    SBAR(); \
    STAMP((layer) * 16 + 10); \
    if (!LAST) { poll(FLAG(blk * 4 + w, 4 + (layer)), tag); AFTER_POLL(); }       \
    STAMP((layer) * 16 + 11); \
    int src_ = OT_OFF + ((layer) & 1) * OT_L + blk * OT_BLK + w * 8 * KB; OPQ(src_); \
    GATHER(8, src_, L_O + w * 8 * KB, FLAG(blk * 4 + w, 4 + (layer)), PRE2) \
    STAMP((layer) * 16 + 12); \
    u32x4 of_[32]; \
    UNR for (int k = 0; k < 6; ++k) of_[k] = LDS128(L_O + k * KB + lo); \
    SBAR(); \
    UNR for (int k = 0; k < 32; ++k) { if (k + 6 < 32) of_[k + 6] = LDS128(L_O + (k + 6) * KB + lo); SIDE; xres = mfma32(WZ[k], of_[k], xres); SBAR(); } \
    u32x4 f0, f1; pack16(xres, f0, f1); \
    STAMP((layer) * 16 + 13); \
    if (LAST && (layer) == 3) { \
      __builtin_amdgcn_s_barrier(); \
      { LAS unsigned char* p = lds + L_O + r * 272 + w * 64 + 8 * hh; \
        *(LAS u32x2*)(p) = (u32x2){f0.x, f0.y}; *(LAS u32x2*)(p + 16) = (u32x2){f0.z, f0.w}; *(LAS u32x2*)(p + 32) = (u32x2){f1.x, f1.y}; *(LAS u32x2*)(p + 48) = (u32x2){f1.z, f1.w}; } \
      __syncthreads(); \
      UNR for (int j = 0; j < 2; ++j) { const int row = 16 * j + (tid >> 4), c = tid & 15; \
        if (g < 3 || c < 8) OUTST(*(LAS u32x4*)(lds + L_O + row * 272 + c * 16), (u32x4*)(out + (32 * blk + row) * DM + 128 * g + 8 * c)); } \
      STAMP_OUT() if constexpr (LAST) asm volatile(".Lcode_last:"); \
      __builtin_amdgcn_endpgm(); \
    } \
    const int dst = XT_OFF + (((layer) + 1) & 1) * XT_L + blk * XT_BLK + g * XT_WG; \
    DELAY() SANE(f0) SANE(f1) STC(f0, lo, dst + w * 2048); STC(f1, lo + KB, dst + w * 2048); \
    { float s_ = 0.f, q_ = 0.f; UNR for (int e = 0; e < 4; ++e) { s_ = dot2(f0[e], 0x3f803f80u, s_); q_ = dot2(f0[e], f0[e], q_); s_ = dot2(f1[e], 0x3f803f80u, s_); q_ = dot2(f1[e], f1[e], q_); } \
      auto r_ = __builtin_amdgcn_permlane32_swap(__float_as_uint(s_), __float_as_uint(q_), false, false);         \
      const unsigned sv = __float_as_uint(__uint_as_float(r_[0]) + __uint_as_float(r_[1])); \
      __builtin_amdgcn_raw_buffer_store_b32(sv < 0xfffffffeu ? sv : 0xfffffffeu, rs, r * 8 + hh * 4, dst + 8192 + w * 256, 16); } \
    STAMP((layer) * 16 + 14); \
    HINT((layer) + 1); \
    STAMP((layer) * 16 + 15); }

#pragma unroll 1
  for (int pair = 0;; ++pair) {
    u32x4 WZ[32], Wv[28], Wk[28], WqB[28]; float csv;
    {
      const int layer = 2 * pair;
      IDX();
      const int wbB = WB_OFF + head * 3 * 28 * KB;
      float mu, rstd;
      X_STATS_LD();
      const int G = lane >> 4, cc = lane & 15;
      const bool role = g < 2 && w < 2;
      if (role) {
        const int xo = L_X + (G >> 1) * KB + (cc + 32 * (G & 1)) * 16;
        u32x4 xf[2][14];
        UNR for (int k = 0; k < 14; ++k) { xf[0][k] = LDS128(xo + (k >> 2) * XT_WG + (k & 3) * 2048); xf[1][k] = LDS128(xo + (k >> 2) * XT_WG + (k & 3) * 2048 + 256); }
        f32x4 a[2] = {{0.f, 0.f, 0.f, 0.f}, {0.f, 0.f, 0.f, 0.f}}, rs_[2], nm_[2]; f32x2 s2_;
        const int kvdst = KVA_OFF + pair * KVA_L + blk * 4 * KB, mo = L_MS + w * 256 + (g ? 16 * G : 4 * cc);
        SBAR();
#define KVCHAIN(MM) sfor<0, 14>([&](auto kc) __attribute__((always_inline)) { constexpr int k = decltype(kc)::value; MM; STATS_STEP(k) \
          if constexpr (k == 5) { UNR for (int t = 0; t < 2; ++t) { rs_[t] = *(LAS f32x4*)(lds + (mo & ~15) + 64 * t); nm_[t] = *(LAS f32x4*)(lds + (mo & ~15) + 128 + 64 * t); } } SBAR(); })
        if (g == 0) {
          KVCHAIN(a[0] = mfma16(W16[k], xf[0][k], a[0]); a[1] = mfma16(W16[k], xf[1][k], a[1]));
          const bool pad = dh == 1 && G == 3; float n2[2];
          UNR for (int t = 0; t < 2; ++t) { n2[t] = 0.f;
            const float r_ = (cc & 2) ? ((cc & 1) ? rs_[t].w : rs_[t].z) : ((cc & 1) ? rs_[t].y : rs_[t].x), m_ = (cc & 2) ? ((cc & 1) ? nm_[t].w : nm_[t].z) : ((cc & 1) ? nm_[t].y : nm_[t].x);
            UNR for (int e = 0; e < 4; ++e) { a[t][e] = pad ? 0.f : fmaf(a[t][e], r_, m_ * cskv[e]); n2[t] = fmaf(a[t][e], a[t][e], n2[t]); }
            a[t][0] = pad ? 1.f : a[t][0];
            __builtin_amdgcn_raw_buffer_store_b64((u32x2){pk(a[t][0], a[t][1]), pk(a[t][2], a[t][3])}, rs, dh * KB + (16 * t + cc + 32 * (G & 1)) * 16 + 8 * (G >> 1), kvdst, 16); }
          const unsigned n0 = umax16(__float_as_uint(row_sum(half_sum(n2[0])))), n1 = umax16(__float_as_uint(row_sum(half_sum(n2[1]))));
          if (lane == 0) __builtin_amdgcn_raw_buffer_store_b64((u32x2){n0, n1}, rs, 8, FA_OFF + (blk * 4 + dh) * 128 + pair * 16, 16);
        } else {
          KVCHAIN(a[0] = mfma16(xf[0][k], W16[k], a[0]); a[1] = mfma16(xf[1][k], W16[k], a[1]));
          UNR for (int t = 0; t < 2; ++t) {
            UNR for (int e = 0; e < 4; ++e) a[t][e] = fmaf(a[t][e], rs_[t][e], nm_[t][e] * cskv.x);
            u32x2 o2 = {pk(a[t][0], a[t][1]), pk(a[t][2], a[t][3])};
            if (dh == 1 && cc >= 12) { o2.x = cc == 12 ? 0x3f803f80u : 0u; o2.y = o2.x; }
            __builtin_amdgcn_raw_buffer_store_b64(o2, rs, 2 * KB + t * KB + (16 * dh + cc + 32 * (G & 1)) * 16 + 8 * (G >> 1), kvdst, 16); }
        }
        if (pair == 0 && blk < 14) {
          const int to = L_RK + (32 * blk + 16 * (G >> 1) + 4 * (G & 1) + (cc >> 2)) * 56 + dh * 32 + (cc & 3) * 8;
          const u32x2 a_ = TRRD(to), c_ = TRRD(to + 8 * 56); STC(((u32x4){a_.x, a_.y, c_.x, c_.y}), lo, WKV_OFF + ((g * 2 + dh) * 14 + blk) * KB); }
      } else {
        X_STATS(mu, rstd);
      }
      STAMP(layer * 16 + 3);
      { f32x16 a = {}; u32x4 xf[28], wf[28];
        const int to = L_RQ + ((pair ? 4 : 8) * hh + (cc >> 2)) * 224 + w * 56 + (G & 1) * 32 + (cc & 3) * 8, to2 = to + (pair ? 8 : 4) * 224;
#define WQF(k) { const u32x2 a_ = TRRD(to + (k) * 16 * 224), c_ = TRRD(to2 + (k) * 16 * 224); wf[k] = (u32x4){a_.x, a_.y, c_.x, c_.y}; }
#define PUBA() { DELAY() DRAIN(); if (lane == 0) __hip_atomic_store(flags + FA_OFF / 8 + (g < 2 ? (blk * 4 + g * 2 + dh) * 16 + pair * 2 : 256 * 16 + blk * 4 + (g & 1) * 2 + dh), tag, RLX); SBAR(); }
        u32x2 xo[4] = {{0u, 0u}, {0u, 0u}, {0u, 0u}, {0u, 0u}};
        if (pair == 0) { if (w >= 2) DRAIN();
          STAMP(1);
          asm volatile("s_waitcnt lgkmcnt(0)\n\ts_barrier" ::: "memory");
          STAMP(2);
          if (rtn) { UNR for (int j = 0; j < 14; ++j) STC(W16[j], lo, j < rtn ? rtb + (j >> 1) * rta + (j & 1) * rts : WS_BYTES); }
        }
        UNR for (int k = 0; k < 4; ++k) { WQF(k) xf[k] = XFRAG(k); }
        SBAR();
        UNR for (int k = 0; k < 28; ++k) { if (k + 4 < 28) { WQF(k + 4) xf[k + 4] = XFRAG(k + 4); }
          a = mfma32(wf[k], xf[k], a); SBAR();
          if (k == PUBA_K && role) PUBA();
          if (k == PUBA_K && pair == 0) { UNR for (int a_ = 0; a_ < 4; ++a_) xo[a_] = *(const u32x2*)(x + (32 * blk + r) * DM + 32 * tlc + 8 * a_ + 4 * hh); SBAR(); } }
        float n2; EPI_ALPHA(a, csq, mu, rstd * QSCALE, n2);
        u32x4 f0, f1; pack16(a, f0, f1);
        if (hh) f1.z = bf_ceil(__builtin_amdgcn_sqrtf(n2) * 1.02f);
        LDS128(L_Q + w * 2 * KB + lo) = f0; LDS128(L_Q + w * 2 * KB + KB + lo) = f1;
        SBAR();
        if (pair == 0) unpack16((u32x4){xo[0].x, xo[0].y, xo[1].x, xo[1].y}, (u32x4){xo[2].x, xo[2].y, xo[3].x, xo[3].y}, xres);
      }
      if (pair == 0 && g >= 2 && w < 2) PUBA();
      STAMP(layer * 16 + 4);
      int xg = blk >> 3;
      float kref;
      { const int fo = (lane < 8 ? (xg * 8 + 2 * w) * 4 + lane : blk * 4 + (lane & 1)) * 128; u32x4 fk; unsigned spins = 0;
        do { asm volatile("" ::: "memory"); fk = LDC(fo, FA_OFF + pair * 16); } while (!__all(fk.x == (unsigned)tag && fk.y == (unsigned)(tag >> 32)) && ++spins < SPIN_CAP);
        AFTER_POLL();
        const float z0 = __uint_as_float(fk.z), z1 = __uint_as_float(fk.w);
        kref = __builtin_amdgcn_sqrtf(fmaxf(z0 + dppf<0xb1>(z0), z1 + dppf<0xb1>(z1))); }
      STAMP(layer * 16 + 5);
      int base = KVA_OFF + pair * KVA_L + w * 8 * KB; OPQ(base);
      int woz = WOA_OFF + tlc * 32 * KB; OPQ(woz);
      int wvb = wbB + 56 * KB, wkb = wbB + 28 * KB; OPQ(wvb); OPQ(wkb);
      u32x4 q[4][2] = {};
      bool stored = false;
      gu64* fall = flags + FA_OFF / 8 + ((((xg + (lane >> 3)) & 7) * 8 + 2 * w) * 4 + (lane & 7)) * 16 + pair * 2;
      int armo = OT_OFF + blk * OT_BLK + head * 2 * KB; int armx = XT_OFF + blk * XT_BLK + g * XT_WG;
#pragma unroll 1
      for (int attempt = 0; attempt < 2; ++attempt) {
        u32x4 Kt[16][2], Vt[16][2]; u32x4 wfl[2] = {};
        OPQ(base); OPQ(woz); OPQ(wvb); OPQ(wkb); OPQ(xg); OPQ(armx);
        const int armx0 = pair ? WS_BYTES : armx;
#define TADR(t) (base + (((xg + ((t) >> 1)) & 7) << 15) + (((t) & 1) << 12))
#define LDK(t) { Kt[t][0] = LDC(lo, TADR(t)); Kt[t][1] = LDC(lo + KB, TADR(t)); }
#define LDV(t) { Vt[t][0] = LDC(lo + 2 * KB, TADR(t)); Vt[t][1] = LDC(lo + 3 * KB, TADR(t)); }
        LDK(0) SBAR(); LDV(0) SBAR(); LDK(1) SBAR(); LDV(1) SBAR();
        if (attempt == 0) {
          __syncthreads();
          STAMP(layer * 16 + 6);
          UNR for (int h = 0; h < 4; ++h) { const int qo = L_Q + ((w + 1 + h) & 3) * 2 * KB + lo; q[h][0] = LDS128(qo); q[h][1] = LDS128(qo + KB);
            const unsigned m = bf_ceil(bflo(q[h][1].z) * kref) ^ 0x8000u; q[h][1].z = hh ? m : q[h][1].z; }
        }
        const u64 fa = __hip_atomic_load(fall, RLX);
        f32x16 oo[4], sA, sB; u32x4 pA0 = q[0][0], pA1 = pA0, pB0 = pA0, pB1 = pA0; f32x4 ea, eb;
        PIN(q[0][1]); PIN(q[1][1]); PIN(q[2][1]); PIN(q[3][1]);
        asm volatile("s_nop 1");
        SBAR();
        MS0(sA, Kt[0][0], q[0][0]); MS1(sA, Kt[0][1], q[0][1]);
        NOP16V(sA); SBAR();
#define WPF(n) { if ((n) < 0) {} else if ((n) < 32) WZ[(n) & 31] = LDW(woz, (n)); else if ((n) < 60) Wv[((n) + 24) % 28] = LDW(wvb, (n) - 32); }
#define PUTO(i) { asm volatile("" : "+a"(oo[i])); const int s_ = L_C + (((w + 1 + (i)) & 3) * 3 + (i)) * 4 * KB + lo; UNR for (int e = 0; e < 4; ++e) \
          *(LAS f32x4*)(lds + s_ + e * KB) = (f32x4){oo[i][4 * e], oo[i][4 * e + 1], oo[i][4 * e + 2], oo[i][4 * e + 3]}; }
        sfor<0, 16>([&](auto tc) __attribute__((always_inline)) { constexpr int t = decltype(tc)::value;
          const int tp = t > 0 ? t - 1 : 0, tn = t < 15 ? t + 1 : 15;
          STEP((t == 0 ? 1 : t == 1 ? 2 : 0), sA, sB, Kt[t][0], Kt[t][1], q[1][0], q[1][1], oo[3], Vt[tp][0], Vt[tp][1], pB0, pB1, pA0, pA1, , WPF(5 * t - 15), WPF(5 * t - 14));
          STEP((t == 0 ? 2 : 0), sB, sA, Kt[t][0], Kt[t][1], q[2][0], q[2][1], oo[0], Vt[t][0], Vt[t][1], pA0, pA1, pB0, pB1, ,
               if (t > 0 && t + 2 < 16) Vt[(t + 2) & 15][0] = LDC(lo + 2 * KB, TADR(t + 2)), if (t > 0 && t + 2 < 16) Vt[(t + 2) & 15][1] = LDC(lo + 3 * KB, TADR(t + 2)); if (t >= 1) ARM(t - 1));
          if (t == 0) { if (fa != tag) { unsigned spins = 0; while (__hip_atomic_load(fall, RLX) != tag && ++spins < SPIN_CAP) __builtin_amdgcn_s_sleep(POLL_SLEEP); }
            AFTER_POLL(); LDK(2) SBAR(); LDV(2) SBAR(); }
          STEP((t == 0 ? 2 : 0), sA, sB, Kt[t][0], Kt[t][1], q[3][0], q[3][1], oo[1], Vt[t][0], Vt[t][1], pB0, pB1, pA0, pA1, , WPF(5 * t - 13), WPF(5 * t - 12) if (t == 15) PUTO(0));
          STEP((t == 0 ? 2 : t == 15 ? 4 : 0), sB, sA, Kt[tn][0], Kt[tn][1], q[0][0], q[0][1], oo[2], Vt[t][0], Vt[t][1], pA0, pA1, pB0, pB1, ,
               if (t + 3 < 16) Kt[(t + 3) & 15][0] = LDC(lo, TADR(t + 3)), if (t + 3 < 16) Kt[(t + 3) & 15][1] = LDC(lo + KB, TADR(t + 3)); WPF(5 * t - 11) if (t == 15) PUTO(1));
          if (t == 1 && pair == 0) { wfl[0] = LDC(lo, FA_OFF + 32 * KB); wfl[1] = LDC(lo + KB, FA_OFF + 32 * KB); SBAR(); }
          if (t == 2 && pair == 0) {
            const u32x4 tg = {(unsigned)tag, (unsigned)(tag >> 32), (unsigned)tag, (unsigned)(tag >> 32)}; const u32x4 df = (wfl[0] ^ tg) | (wfl[1] ^ tg);
            if (__builtin_expect(!__all((df.x | df.y | df.z | df.w) == 0u), 0)) { UNR for (int j = 0; j < 4; ++j) { unsigned spins = 0;
                while (__hip_atomic_load(flags + (FA_OFF + 32 * KB) / 8 + j * 64 + lane, RLX) != tag && ++spins < SPIN_CAP) __builtin_amdgcn_s_sleep(8); } }
            AFTER_POLL();
            SBAR(); }
        });
        C2(pB1, 2, eb)
        MO(oo[3], Vt[15][0], pB0); MO(oo[3], Vt[15][1], pB1);
        SBAR();
        PUTO(2)
        DRAIN();
        armo = WS_BYTES;
        STAMP(layer * 16 + 7);
        UNR for (int k = 0; k < 8; ++k) Wk[k] = LDW(wkb, k);
        SBAR();
        f32x16 o = oo[3];
        __syncthreads();
        UNR for (int s = 0; s < 3; ++s)
          UNR for (int e = 0; e < 4; ++e) { const f32x4 t = *(LAS f32x4*)(lds + L_C + (w * 3 + s) * 4 * KB + e * KB + lo);
            o[4 * e] += t.x; o[4 * e + 1] += t.y; o[4 * e + 2] += t.z; o[4 * e + 3] += t.w; }
        const float l = half_hi(o[12]);
        float om = 0.f; UNR for (int e = 0; e < 16; e += 2) om = fmaxf(fmaxf(om, fabsf(o[e])), fabsf(o[e + 1]));
        const bool bad = !__all(l >= L_MIN && om <= L_MAX);
        if (lane == 0) *(LAS unsigned*)(lds + L_MISC + w * 4) = bad ? 1u : 0u;
        if (attempt == 1 || !bad) { if (!stored) O_STORE(layer, o, l); stored = true; }
        STAMP(layer * 16 + 8);
        __syncthreads();
        const u32x4 bw = *(LAS u32x4*)(lds + L_MISC);
        if (__builtin_expect(attempt == 1 || __builtin_amdgcn_readfirstlane(bw.x | bw.y | bw.z | bw.w) == 0u, 1)) break;
        float mx[4] = {-INFINITY, -INFINITY, -INFINITY, -INFINITY};
        UNR for (int h = 0; h < 4; ++h) q[h][1].z = hh ? 0u : q[h][1].z;
#pragma unroll 1
        for (int t = 0; t < 16; ++t) {
          const u32x4 k0 = LDC(lo, TADR(t)), k1 = LDC(lo + KB, TADR(t));
          UNR for (int h = 0; h < 4; ++h) { f32x16 s = {}; s = mfma32(k0, q[h][0], s); s = mfma32(k1, q[h][1], s);
            UNR for (int e = 0; e < 16; ++e) mx[h] = fmaxf(mx[h], s[e]); }
        }
        UNR for (int h = 0; h < 4; ++h) { const float m = half_max(mx[h]); if (hh == 0) *(LAS float*)(lds + L_MISC + 1024 + (w * 4 + ((w + 1 + h) & 3)) * 128 + r * 4) = m; }
        __syncthreads();
        UNR for (int h = 0; h < 4; ++h) { float m = -INFINITY;
          UNR for (int s = 0; s < 4; ++s) m = fmaxf(m, *(LAS float*)(lds + L_MISC + 1024 + (s * 4 + ((w + 1 + h) & 3)) * 128 + r * 4));
          q[h][1].z = hh ? (bf_ceil(m) ^ 0x8000u) : q[h][1].z; }
      }
      if (tid == 0) { __hip_atomic_store(FLAG(blk * 4 + g, 4 + layer), tag, RLX);
        if (b == 0) __hip_atomic_store(flags + EP_OFF / 8, tag, RLX); }
#define ONES() unsigned one2; asm volatile("v_mov_b32 %0, 0x3f803f80" : "=v"(one2)); const u32x4 ones = one2
#define CSV_CHAIN(k0, k1) { UNR for (int k = k0; k < k1; ++k) { if (!(k & 1) && k < 20) Wk[8 + (k >> 1)] = LDW(wkb, 8 + (k >> 1)); cv_ = mfma32(ones, Wv[k], cv_); SBAR(); } }
      ONES(); f32x16 cv_ = {};
      Z_PHASE(layer, false,
              CSV_CHAIN(0, 12), CSV_CHAIN(12, 28) csv = cv_[0]; PIN(csv),
              if ((k & 1) && k < 20) Wk[18 + (k >> 1)] = LDW(wkb, 18 + (k >> 1)); else if ((k & 1) && k < 24) WqB[(k >> 1) - 10] = LDW(wbB, (k >> 1) - 10));
    }
    {
      const int layer = 2 * pair + 1;
      IDX();
      const int wbB = WB_OFF + head * 3 * 28 * KB;
      int woz = WOB_OFF + tlc * 32 * KB; OPQ(woz);
      f32x16 csqB = {}, cskB = {};
      ONES();
#define CSK_CHAIN(k0, k1) { UNR for (int k = k0; k < k1; ++k) { if (!(k & 1) && k < 20) WqB[2 + (k >> 1)] = LDW(wbB, 2 + (k >> 1)); cskB = mfma32(Wk[k], ones, cskB); SBAR(); } }
      CSK_CHAIN(0, SPLIT_K)
      X_GATHER(layer, CSK_CHAIN(SPLIT_K, 28));
      float mu, rstd, qn;
      u32x4 K[9][2], V[9][2], q0, q1;
      const bool pv = lane < 8 && blk - 8 + lane >= 0;
      const int fbo = FB_OFF + ((blk - 8) * 16 + head) * 128 + pair * 32, fl = pv ? lane * 16 * 128 : 8 * 16 * 128;
      {
        const int dst = KVB_OFF + blk * KVB_BLK + head * 4 * KB;
        f32x16 av = {}, ak = {}, aq = {}, kf; u32x4 xf[28]; unsigned vp[8]; f32x4 rs_[4], nm_[4]; f32x2 s2_; float n0 = 0.f, n1 = 0.f;
        X_STATS_LD();
        UNR for (int k = 0; k < 6; ++k) xf[k] = XFRAG(k);
        SBAR();
        sfor<0, 84>([&](auto kc) __attribute__((always_inline)) { constexpr int k = decltype(kc)::value;
          if constexpr (k + 6 < 28) xf[k + 6] = XFRAG(k + 6);
          if constexpr (k % 5 == 1 || k % 5 == 3) { constexpr int i = (k / 5) * 2 + (k % 5 == 3);
            if constexpr (i < 16) WqB[12 + i] = LDW(wbB, 12 + i); else if constexpr (i < 23) WZ[i - 16] = LDW(woz, i - 16); else if constexpr (k > PUBK_K && i < 32) WZ[i - 22] = LDW(woz, i - 22); }
          if constexpr (k < 28) av = mfma32(xf[k], Wv[k], av); else if constexpr (k < 56) ak = mfma32(Wk[k - 28], xf[k - 28], ak); else { aq = mfma32(WqB[k - 56], xf[k - 56], aq); csqB = mfma32(WqB[k - 56], ones, csqB); }
          STATS_STEP(k)
          if constexpr (k >= 7 && k < 11) { constexpr int i = k - 7; rs_[i] = *(LAS f32x4*)(lds + L_MS + w * 256 + (8 * i + 4 * hh) * 4); nm_[i] = *(LAS f32x4*)(lds + L_MS + w * 256 + 128 + (8 * i + 4 * hh) * 4); }
          if constexpr (k >= 23 && k < 27) { constexpr int i = k - 23; nm_[i] *= csv; PIN(nm_[i]); }
          if constexpr (k == 30) asm volatile("" : "+a"(av));
          if constexpr (k >= 30 && k < 38) { constexpr int e = 2 * (k - 30), i = e >> 2, c = e & 3;
            const unsigned p0 = pk(fmaf(av[e], rs_[i][c], nm_[i][c]), fmaf(av[e + 1], rs_[i][c + 1], nm_[i][c + 1]));
            vp[e >> 1] = r < 28 ? p0 : (r == 28 ? 0x3f803f80u : 0u); PIN(vp[e >> 1]); }
          if constexpr (k == 38) { V[8][0] = (u32x4){vp[0], vp[1], vp[2], vp[3]}; V[8][1] = (u32x4){vp[4], vp[5], vp[6], vp[7]}; STC(V[8][0], lo + 2 * KB, dst); STC(V[8][1], lo + 3 * KB, dst); }
          if constexpr (k == 40) { cskB *= -mu * rstd; PIN(cskB); }
          if constexpr (k == 57) { asm volatile("" : "+a"(ak));
            kf = ak * rstd + cskB; UNR for (int e = 12; e < 16; ++e) kf[e] = hh ? 0.f : kf[e];
            pack16(kf, K[8][0], K[8][1]); K[8][1].z = hh ? 0x3f80u : K[8][1].z;
            STC(K[8][0], lo, dst); STC(K[8][1], lo + KB, dst);
            STAMP(layer * 16 + 3); }
          if constexpr (k >= 58 && k < 61) { constexpr int e0 = (k - 58) * 3; UNR for (int e = e0; e < e0 + 3 && e < 8; ++e) { n0 = fmaf(kf[e], kf[e], n0); n1 = fmaf(kf[8 + e], kf[8 + e], n1); } PIN(n0); PIN(n1); }
          if constexpr (k == 61) { n0 = half_sum(n0 + n1); PIN(n0); }
          if constexpr (k == 62) { const unsigned kown = umax32(__float_as_uint(n0)); if (lane == 0) __builtin_amdgcn_raw_buffer_store_b32(kown, rs, 8, fbo + 8 * 16 * 128, 16); }
          SBAR();
          if constexpr (k == PUBK_K) { DELAY() DRAIN(); if (lane == 0) __hip_atomic_store(flags + (fbo + 8 * 16 * 128) / 8, tag, RLX); SBAR(); } });
        aq = (aq - csqB * mu) * (rstd * QSCALE); UNR for (int e = 12; e < 16; ++e) aq[e] = hh ? 0.f : aq[e];
        pack16(aq, q0, q1);
        n0 = 0.f; n1 = 0.f; UNR for (int e = 0; e < 8; ++e) { n0 = fmaf(aq[e], aq[e], n0); n1 = fmaf(aq[8 + e], aq[8 + e], n1); }
        qn = __builtin_amdgcn_sqrtf(half_sum(n0 + n1)) * 1.02f;
      }
      STAMP(layer * 16 + 4);
      const int kvb = KVB_OFF + (blk - 8) * KVB_BLK + head * 4 * KB;
#define SRCB(j) (blk - 8 + (j) < 0 ? ZERO_OFF : kvb + (j) * KVB_BLK)
#define LKB(j) { const int s_ = SRCB(j); K[j][0] = LDC(lo, s_); K[j][1] = LDC(lo + KB, s_); SBAR(); }
#define LVB(j) { const int s_ = SRCB(j); V[j][0] = LDC(lo + 2 * KB, s_); V[j][1] = LDC(lo + 3 * KB, s_); SBAR(); }
      { u32x4 fk; unsigned spins = 0;
        do { asm volatile("" ::: "memory"); fk = LDC(fl, fbo); } while (!__all(fk.x == (unsigned)tag && fk.y == (unsigned)(tag >> 32)) && ++spins < SPIN_CAP);
        AFTER_POLL();
        STAMP(layer * 16 + 5);
        LKB(7) LKB(6) LVB(7) LKB(5) LVB(6) LKB(4)
        const unsigned kmx = __builtin_amdgcn_readfirstlane(umax16(fk.z));
        q1.z = hh ? (bf_ceil(qn * __builtin_amdgcn_sqrtf(__uint_as_float(kmx))) ^ 0x8000u) : q1.z; }
      f32x16 o; float l;
#pragma unroll 1
      for (int attempt = 0; attempt < 2; ++attempt) {
        STAMP(layer * 16 + 6);
        f32x16 sA, sB; u32x4 pA0 = q0, pA1 = pA0, pB0 = pA0, pB1 = pA0; f32x4 ea, eb;
        PIN(q0); PIN(q1); PIN(K[8][0]); PIN(K[8][1]); asm volatile("s_nop 1"); SBAR();
        MS0(sA, K[8][0], q0); MS1(sA, K[8][1], q1);
        NOP16V(sA); SBAR();
#define MASKT(S, keep) { UNR for (int e = 0; e < 16; ++e) { const int row = (e & 3) + 8 * (e >> 2) + 4 * hh; S[e] = (keep) ? S[e] : -INFINITY; } PIN(S); SBAR(); }
#define WZ2(k) { WZ[k] = LDW(woz, k); WZ[k + 1] = LDW(woz, k + 1); }
        MASKT(sA, row <= r)
        STEP(1, sA, sB, K[7][0], K[7][1], q0, q1, o, V[8][0], V[8][1], pB0, pB1, pA0, pA1, LVB(5) LKB(3), LVB(4), LKB(2));
        STEP(2, sB, sA, K[6][0], K[6][1], q0, q1, o, V[8][0], V[8][1], pA0, pA1, pB0, pB1, , LVB(3), LKB(1));
        STEP(0, sA, sB, K[5][0], K[5][1], q0, q1, o, V[7][0], V[7][1], pB0, pB1, pA0, pA1, , LVB(2), LKB(0));
        STEP(0, sB, sA, K[4][0], K[4][1], q0, q1, o, V[6][0], V[6][1], pA0, pA1, pB0, pB1, , LVB(1), LVB(0));
        STEP(0, sA, sB, K[3][0], K[3][1], q0, q1, o, V[5][0], V[5][1], pB0, pB1, pA0, pA1, , , );
        STEP(0, sB, sA, K[2][0], K[2][1], q0, q1, o, V[4][0], V[4][1], pA0, pA1, pB0, pB1, , WZ2(10), );
        STEP(0, sA, sB, K[1][0], K[1][1], q0, q1, o, V[3][0], V[3][1], pB0, pB1, pA0, pA1, , WZ2(12), WZ2(14));
        STEP(0, sB, sA, K[0][0], K[0][1], q0, q1, o, V[2][0], V[2][1], pA0, pA1, pB0, pB1, , WZ2(16), WZ2(18));
        MASKT(sA, row > r)
        STEP(4, sA, sB, K[0][0], K[0][1], q0, q1, o, V[1][0], V[1][1], pB0, pB1, pA0, pA1, , , );
        C2(pA1, 2, eb)
        MO(o, V[0][0], pA0); MO(o, V[0][1], pA1);
        SBAR();
        l = half_hi(o[12]);
        if (__builtin_expect(attempt == 1 || __all(l >= L_MIN), 1)) break;
        q1.z = hh ? 0u : q1.z;
        float mx = -INFINITY;
        UNR for (int j = 8; j >= 0; --j) {
          f32x16 s = {}; s = mfma32(K[j][0], q0, s); s = mfma32(K[j][1], q1, s); float m_ = -INFINITY;
          UNR for (int e = 0; e < 16; ++e) { const int row = (e & 3) + 8 * (e >> 2) + 4 * hh; float v = s[e];
            if (j == 8) v = (row <= r) ? v : -INFINITY; if (j == 0) v = (row > r) ? v : -INFINITY; m_ = fmaxf(m_, v); }
          mx = (blk - 8 + j >= 0) ? fmaxf(mx, m_) : mx; SBAR(); }
        mx = half_max(mx);
        q1.z = hh ? (bf_ceil(mx) ^ 0x8000u) : q1.z;
      }
      STAMP(layer * 16 + 7);
      O_STORE(layer, o, l);
      STAMP(layer * 16 + 8);
      HINT(4 + layer);
      int wkv = (g < 2 && w < 2) ? WKV_OFF + (g * 2 + dh) * 14 * KB : WS_BYTES; OPQ(wkv);
      const int G = lane >> 4, cc = lane & 15, to = L_RQ + (4 * hh + (cc >> 2)) * 224 + w * 56 + (G & 1) * 32 + (cc & 3) * 8, to2 = to + 8 * 224;
      u32x4 wf[28]; csq = (f32x16){};
#define CSQ_CHAIN(k0, k1) { if (k0 == 0) { UNR for (int k = 0; k < 6; ++k) WQF(k) SBAR(); } \
        UNR for (int k = k0; k < k1; ++k) { if (k + 6 < 28) WQF(k + 6) if (!(k & 1) && k < 24) WZ[20 + (k >> 1)] = LDW(woz, 20 + (k >> 1)); csq = mfma32(wf[k], ones, csq); SBAR(); } }
      Z_PHASE(layer, true,
              CSQ_CHAIN(0, SPLIT_K - 2), CSQ_CHAIN(SPLIT_K - 2, 28),
              if ((k & 1) && k < 28) W16[k >> 1] = LDW(wkv, k >> 1));
      cskv = (f32x4){0.f, 0.f, 0.f, 0.f};
#define CSKV_CHAIN() { if (g == 0) { UNR for (int k = 0; k < 14; ++k) cskv = mfma16(W16[k], ones, cskv); } else { UNR for (int k = 0; k < 14; ++k) cskv = mfma16(ones, W16[k], cskv); } }
      CSKV_CHAIN(); DRAIN();
      X_GATHER(layer + 1, );
    }
  }
}

extern "C" void kernel_launch(void* const* d_in, const int* in_sizes, int n_in, void* d_out, int out_size, void* d_ws, size_t ws_size, hipStream_t stream) {
  (void)in_sizes; (void)n_in; (void)out_size; (void)ws_size;
  (void)hipFuncSetAttribute((const void*)main_kernel, hipFuncAttributeMaxDynamicSharedMemorySize, L_BYTES);
  main_kernel<<<256, 256, L_BYTES, stream>>>((const u16*)d_in[0], (const u16*)d_in[1], (const u16*)d_in[2], (const u16*)d_in[3], (const u16*)d_in[4],
                                             (const u16*)d_in[6], (const u16*)d_in[7], (unsigned char*)d_ws, (u16*)d_out);
}
```

```cpp
#include <hip/hip_runtime.h>
#include <hip/hip_bf16.h>

typedef float f32x16 __attribute__((ext_vector_type(16)));
typedef float f32x4 __attribute__((ext_vector_type(4)));
typedef float f32x2 __attribute__((ext_vector_type(2)));
typedef unsigned u32x4 __attribute__((ext_vector_type(4)));
typedef unsigned u32x2 __attribute__((ext_vector_type(2)));
typedef short bf16x8 __attribute__((ext_vector_type(8)));
typedef short s16x4 __attribute__((ext_vector_type(4)));
typedef __bf16 bf2 __attribute__((ext_vector_type(2)));
typedef unsigned short u16;
typedef unsigned long long u64;
#define GAS __attribute__((address_space(1)))
#define LAS __attribute__((address_space(3)))
typedef GAS u64 gu64;
#define RLX __ATOMIC_RELAXED, __HIP_MEMORY_SCOPE_AGENT
#define SBAR() __builtin_amdgcn_sched_barrier(0)
#define PIN(x) asm volatile("" : "+v"(x))
#define OPQ(x) asm volatile("" : "+s"(x))
#define DRAIN() asm volatile("s_waitcnt vmcnt(0)" ::: "memory")
#define UNR _Pragma("unroll")

constexpr int DM = 448, NB = 64, NTL = 14, KB = 1024;
constexpr int EP_OFF = 0;
constexpr int FA_OFF = 1 * KB;
constexpr int FL_OFF = FA_OFF + 2 * 8 * 4 * KB;
constexpr int FB_OFF = FL_OFF + 256 * 128;
constexpr int ZERO_OFF = FB_OFF + 128 * KB;
constexpr int WKV_OFF = ZERO_OFF + 4 * KB;
constexpr int KVA_OFF = WKV_OFF + 56 * KB;
constexpr int KVA_L = NB * 4 * KB;
constexpr int WOA_OFF = KVA_OFF + 2 * KVA_L;
constexpr int WOB_OFF = WOA_OFF + 14 * 32 * KB;
constexpr int WB_OFF = WOB_OFF + 14 * 32 * KB;
constexpr int XT_OFF = WB_OFF + 16 * 3 * 28 * KB;
constexpr int XT_WG = 9 * KB, XT_BLK = 4 * XT_WG, XT_L = NB * XT_BLK;
constexpr int OT_OFF = XT_OFF + 2 * XT_L;
constexpr int OT_BLK = 32 * KB, OT_L = NB * OT_BLK;
constexpr int KVB_OFF = OT_OFF + 2 * OT_L;
constexpr int KVB_BLK = 64 * KB;
constexpr int WS_BYTES = KVB_OFF + NB * KVB_BLK;
#define FLAG(wg, word) (flags + (FL_OFF + (wg) * 128) / 8 + (word))
constexpr int L_X = 0;
constexpr int L_O = 0, L_C = 0;
constexpr int L_MISC = 48 * KB;
constexpr int L_Q = 52 * KB;
constexpr int L_RK = 36 * KB;
constexpr int L_RQ = L_RK + 448 * 56;
constexpr int L_MS = 159 * KB;
constexpr int L_BYTES = 160 * KB;
static_assert(L_RQ + 448 * 224 + 64 <= L_MS && L_RQ % 16 == 0, "lds");

constexpr float QSCALE = 0.18898223650461361f * 1.4426950408889634f;
constexpr unsigned SPIN_CAP = 1u << 16;
constexpr int POLL_SLEEP = 4;
constexpr int PUBK_K = 71;
constexpr int PUBA_K = 13;
constexpr int SPLIT_K = 16;
constexpr float L_MAX = 1e36f;
constexpr float L_MIN = 1e-18f;

__device__ __forceinline__ unsigned pk(float a, float b) { f32x2 v = {a, b}; bf2 r = __builtin_convertvector(v, bf2); return __builtin_bit_cast(unsigned, r); }
__device__ __forceinline__ float bflo(unsigned u) { return __uint_as_float(u << 16); }
__device__ __forceinline__ float bfhi(unsigned u) { return __uint_as_float(u & 0xffff0000u); }
__device__ __forceinline__ f32x16 mfma32(u32x4 a, u32x4 b, f32x16 c) {
  return __builtin_amdgcn_mfma_f32_32x32x16_bf16(__builtin_bit_cast(bf16x8, a), __builtin_bit_cast(bf16x8, b), c, 0, 0, 0); }
__device__ __forceinline__ f32x4 mfma16(u32x4 a, u32x4 b, f32x4 c) {
  return __builtin_amdgcn_mfma_f32_16x16x32_bf16(__builtin_bit_cast(bf16x8, a), __builtin_bit_cast(bf16x8, b), c, 0, 0, 0); }
__device__ __forceinline__ float half_sum(float v) { auto r = __builtin_amdgcn_permlane32_swap(__float_as_uint(v), __float_as_uint(v), false, false); return __uint_as_float(r[0]) + __uint_as_float(r[1]); }
__device__ __forceinline__ float half_max(float v) { auto r = __builtin_amdgcn_permlane32_swap(__float_as_uint(v), __float_as_uint(v), false, false); return fmaxf(__uint_as_float(r[0]), __uint_as_float(r[1])); }
__device__ __forceinline__ float half_hi(float v) { auto r = __builtin_amdgcn_permlane32_swap(__float_as_uint(v), __float_as_uint(v), false, false); return __uint_as_float(r[1]); }
__device__ __forceinline__ float row_sum(float v) { auto r = __builtin_amdgcn_permlane16_swap(__float_as_uint(v), __float_as_uint(v), false, false); return __uint_as_float(r[0]) + __uint_as_float(r[1]); }
__device__ __forceinline__ float dot2(unsigned a, unsigned b, float c) { return __builtin_amdgcn_fdot2_f32_bf16(__builtin_bit_cast(bf2, a), __builtin_bit_cast(bf2, b), c, false); }
template <int C> __device__ __forceinline__ float dppf(float v) { return __uint_as_float(__builtin_amdgcn_update_dpp(0u, __float_as_uint(v), C, 0xf, 0xf, true)); }
__device__ __forceinline__ float sum8(float v) { return half_sum(row_sum(v + dppf<0xb1>(v))); }
template <int N> __device__ __forceinline__ unsigned ror(unsigned v) { return __builtin_amdgcn_update_dpp(0u, v, 0x120 + N, 0xf, 0xf, true); }
__device__ __forceinline__ unsigned umax(unsigned a, unsigned b) { return a > b ? a : b; }
__device__ __forceinline__ unsigned umax16(unsigned v) { v = umax(v, ror<1>(v)); v = umax(v, ror<2>(v)); v = umax(v, ror<4>(v)); return umax(v, ror<8>(v)); }
__device__ __forceinline__ unsigned umax32(unsigned v) { v = umax16(v); auto r = __builtin_amdgcn_permlane16_swap(v, v, false, false); return umax(r[0], r[1]); }
__device__ __forceinline__ unsigned bf_ceil(float v) { unsigned u = __float_as_uint(v); unsigned t = u >> 16; return (v > 0.f && (u & 0xffffu)) ? t + 1u : t; }
__device__ __forceinline__ void pack16(const f32x16& v, u32x4& f0, u32x4& f1) {
  f0 = (u32x4){pk(v[0], v[1]), pk(v[2], v[3]), pk(v[4], v[5]), pk(v[6], v[7])};
  f1 = (u32x4){pk(v[8], v[9]), pk(v[10], v[11]), pk(v[12], v[13]), pk(v[14], v[15])}; }
__device__ __forceinline__ void unpack16(const u32x4 f0, const u32x4 f1, f32x16& v) {
  UNR for (int i = 0; i < 4; ++i) { v[2 * i] = bflo(f0[i]); v[2 * i + 1] = bfhi(f0[i]); v[8 + 2 * i] = bflo(f1[i]); v[9 + 2 * i] = bfhi(f1[i]); } }
template <int V> struct IC { static constexpr int value = V; };
template <int I, int N, class F> __device__ __forceinline__ void sfor(F&& f) { if constexpr (I < N) { f(IC<I>{}); sfor<I + 1, N>(f); } }
__device__ __forceinline__ float osum(const f32x16& v) {
  return (((v[0] + v[1]) + (v[2] + v[3])) + ((v[4] + v[5]) + (v[6] + v[7]))) + (((v[8] + v[9]) + (v[10] + v[11])) + ((v[12] + v[13]) + (v[14] + v[15]))); }
__device__ __forceinline__ u32x4 mkdesc(const void* p, unsigned bytes) { return (u32x4){(unsigned)(size_t)p, (unsigned)((size_t)p >> 32) & 0xffffu, bytes, 0x00020000u}; }

#define LDC(voff, soff) __builtin_amdgcn_raw_buffer_load_b128(rs, (voff), (soff), 16)
#define STC(v, voff, soff) __builtin_amdgcn_raw_buffer_store_b128((v), rs, (voff), (soff), 16)
#define LDS128(off) (*(LAS u32x4*)(lds + (off)))
#define LDW(base, k) LDC(lo + ((k) & 3) * KB, (base) + ((k) >> 2) * 4 * KB)
#define MS0(S, A, B) asm volatile("v_mfma_f32_32x32x16_bf16 %0, %1, %2, 0" : "=&v"(S) : "v"(A), "v"(B))
#define MS1(S, A, B) asm volatile("v_mfma_f32_32x32x16_bf16 %0, %1, %2, %0" : "+v"(S) : "v"(A), "v"(B))
#define MO(O, A, B) { O = mfma32(A, B, O); asm volatile("" : "+a"(O)); }
#define EX(x) __builtin_amdgcn_exp2f(x)
#define E4(E, S, i) { E[0] = EX(S[i]); E[1] = EX(S[i + 1]); E[2] = EX(S[i + 2]); E[3] = EX(S[i + 3]); PIN(E); SBAR(); }
#define C2(P, j, E) { P[j] = pk(E[0], E[1]); P[j + 1] = pk(E[2], E[3]); PIN(P); SBAR(); }
#define STEP(MODE, SC, SN, KN0, KN1, QN0, QN1, OP, VP0, VP1, PP0, PP1, PC0, PC1, W0, LDA, LDB) \
  W0; if (!((MODE) & 4)) MS0(SN, KN0, QN0); SBAR(); E4(ea, SC, 0) if (!((MODE) & 1)) C2(PP1, 2, eb) \
  if (!((MODE) & 4)) MS1(SN, KN1, QN1); LDA; SBAR(); E4(eb, SC, 4) C2(PC0, 0, ea) \
  if ((MODE) & 2) OP = (f32x16){}; \
  if (!((MODE) & 1)) MO(OP, VP0, PP0); SBAR(); E4(ea, SC, 8) C2(PC0, 2, eb) \
  if (!((MODE) & 1)) MO(OP, VP1, PP1); LDB; SBAR(); E4(eb, SC, 12) C2(PC1, 0, ea)
#define NOP16V(x) asm volatile("s_nop 7\n\ts_nop 7" : "+v"(x))
#define ADMA(ldsaddr, voff, dsc, soff) asm volatile("s_mov_b32 m0, %0\n\ts_nop 0\n\tbuffer_load_dwordx4 %1, %2, %3 offen lds" :: "s"(ldsaddr), "v"(voff), "s"(dsc), "s"(soff) : "memory")
#define TRRD(off) __builtin_bit_cast(u32x2, __builtin_amdgcn_ds_read_tr16_b64_v4i16((LAS s16x4*)(lds + (off))))

__device__ __forceinline__ void poll(gu64* p, u64 tag) {
  unsigned spins = 0;
  while (__hip_atomic_load(p, RLX) != tag) { if (++spins > SPIN_CAP) break; }
}
#define OUTST(v, p) *(p) = v
#define AFTER_POLL() __builtin_amdgcn_fence(__ATOMIC_ACQUIRE, "wavefront")

__global__ __launch_bounds__(256, 1) void main_kernel(const u16* __restrict__ x, const u16* __restrict__ wq, const u16* __restrict__ wk, const u16* __restrict__ wv,
                                                      const u16* __restrict__ woa, const u16* __restrict__ wb, const u16* __restrict__ wob,
                                                      unsigned char* __restrict__ ws, u16* __restrict__ out) {
  extern __shared__ __attribute__((aligned(16))) unsigned char smem[];
  const __amdgpu_buffer_rsrc_t rs = __builtin_amdgcn_make_buffer_rsrc((void*)ws, 0, WS_BYTES, 0x00020000);
  const u32x4 desc = mkdesc(ws, WS_BYTES);
  LAS unsigned char* lds = (LAS unsigned char*)smem;
  gu64* flags = (gu64*)ws;
  int tid_ = threadIdx.x, b_ = blockIdx.x, w_ = __builtin_amdgcn_readfirstlane(threadIdx.x >> 6);
#define IDX() asm volatile("" : "+v"(tid_)); asm volatile("" : "+s"(b_), "+s"(w_)); \
  const int tid = tid_, b = b_, w = w_, lane = tid & 63, r = lane & 31, hh = lane >> 5, lo = lane * 16; \
  const int blk = (b & 7) * 8 + (b >> 5), g = (b >> 3) & 3, head = 4 * g + w, tl = head; const bool act = tl < NTL; const int tlc = act ? tl : 0; \
  const int dh = w & 1, th = w >> 1; (void)r; (void)hh; (void)dh; (void)th; (void)tlc; (void)blk; (void)lo
  u64 pc_; unsigned len_;
  asm volatile("s_getpc_b64 %0\n.Lcode_here:\n\ts_mov_b32 %1, .Lcode_last-.Lcode_here" : "=s"(pc_), "=s"(len_));
#define CODEPF(ldsoff) { const u32x4 dc = mkdesc((const void*)pc_, len_); const int vo = ((b >> 3) * 4 + w) * 512 + (lane & 3) * 128; const unsigned la = (ldsoff) + w * 256; \
    asm volatile("s_mov_b32 m0, %0\n\ts_nop 0\n\tbuffer_load_dword %1, %2, 0 offen lds" :: "s"(la), "v"(vo), "s"(dc) : "memory"); }

  f32x16 xres, csq;
  f32x4 cskv;
  u32x4 W16[14];
  int rtb = 0, rta = 0, rts = 0, rtn = 0;
  u64 tag;
  {
    IDX();
    u64 ep; asm volatile("global_load_dwordx2 %0, %1, %2 sc1" : "=v"(ep) : "v"(0), "s"(ws));
    u32x4 xr[7];
    { const u32x4 dx = mkdesc(x + 32 * blk * DM, 32 * 896); const int vo = (8 * w + ((lane >> 1) & 7)) * 896 + (lane >> 4) * 32 + (lane & 1) * 16;
      UNR for (int f = 0; f < 7; ++f) asm volatile("buffer_load_dwordx4 %0, %1, %2, 0 offen offset:%3" : "=v"(xr[f]) : "v"(vo), "s"(dx), "i"(f * 128)); }
    CODEPF(L_X + 31 * KB);
    const int n = blk * 2 + (g & 1), nw = n - 84;
    const int c3 = (n * 37) >> 10, ks = n - 28 * c3;
    const int t7 = tid & 127;
    int vq[7];
    const u32x4 dq = mkdesc(wq, 448 * 448 * 2);
#define WQDMA(u0) { int u = (u0) + (blk & 7); u = u >= 7 ? u - 7 : u; UNR for (int k = 0; k < 7; ++k) { \
      const unsigned la = L_RQ + (2 * (7 * u + k) + dh) * KB; const int so = u * 64 * 896; ADMA(la, vq[k], dq, so); } }
    if (w >= 2) {
      UNR for (int k = 0; k < 7; ++k) { const int c = t7 + 128 * k, row = (c * 4682) >> 16; vq[k] = row * 896 + (c - row * 14) * 16 + g * 224; }
      WQDMA(0) WQDMA(1)
    } else if (g < 2 || n >= 84) { const u32x4 dk = g < 2 ? mkdesc(g ? wv : wk, 448 * 56) : mkdesc((nw & 16 ? wob : woa) + (nw & 15) * 28 * 448, n < 116 ? 448 * 56 : 0);
      UNR for (int k = 0; k < 14; ++k) { const int i = 2 * k + w; const unsigned la = i < 24 ? L_RK + i * KB : L_X + 32 * KB; const int so = i < 24 ? i * KB : 448 * 56;
        if (k == 12) { const unsigned l2 = L_RK + 24 * KB; const int s2 = 24 * KB; if (lane < 32) ADMA(l2, lo, dk, s2); } else ADMA(la, lo, dk, so); }
    } else { const u32x4 dk = mkdesc(wb + 16 * ks * 1344 + 448 * c3, 16 * 2688);
      UNR for (int k = 0; k < 7; ++k) { const int c = t7 + 128 * k, row = (c * 1171) >> 16; const unsigned la = L_RK + (2 * k + w) * KB;
        ADMA(la, row * 2688 + (c - row * 56) * 16, dk, 0); }
      UNR for (int k = 0; k < 7; ++k) { const unsigned la = L_X + 32 * KB; const int so = 16 * 2688; ADMA(la, lo, dk, so); } }
    asm volatile("s_waitcnt vmcnt(15)" : "+v"(xr[0]), "+v"(xr[1]), "+v"(xr[2]), "+v"(xr[3]), "+v"(xr[4]), "+v"(xr[5]), "+v"(xr[6]), "+v"(ep) :: "memory");
    { float s0 = 0.f, s1 = 0.f, q0 = 0.f, q1 = 0.f;
      UNR for (int f = 0; f < 7; ++f) UNR for (int c = 0; c < 4; c += 2) { s0 = dot2(xr[f][c], 0x3f803f80u, s0); q0 = dot2(xr[f][c], xr[f][c], q0); s1 = dot2(xr[f][c + 1], 0x3f803f80u, s1); q1 = dot2(xr[f][c + 1], xr[f][c + 1], q1); }
      SBAR(); if (w >= 2) WQDMA(2) SBAR();
      const float mu = sum8(s0 + s1) * (1.f / 448.f);
      const float rstd = __builtin_amdgcn_rsqf(fmaxf(fmaf(-mu, mu, sum8(q0 + q1) * (1.f / 448.f)), 0.f) + 1e-5f), nm = -mu * rstd;
      UNR for (int f = 0; f < 7; ++f) {
        u32x4 o; UNR for (int e = 0; e < 4; ++e) { const f32x2 t = (f32x2){bflo(xr[f][e]), bfhi(xr[f][e])} * rstd + nm; o[e] = pk(t.x, t.y); }
        LDS128(L_X + (f >> 1) * XT_WG + (f & 1) * 4 * KB + (lane >> 4) * KB + (8 * w + ((lane >> 1) & 7) + 32 * (lane & 1)) * 16) = o; }
      const float one = 32.f * (1.f - 1e-5f);
      *(LAS f32x4*)(lds + L_X + w * XT_WG + 8192 + lo) = (f32x4){0.f, one, 0.f, one};
    }
    tag = (((u64)(unsigned)__builtin_amdgcn_readfirstlane((unsigned)(ep >> 32)) << 32) | (unsigned)__builtin_amdgcn_readfirstlane((unsigned)ep)) + 1ull;
    if (w < 2) DRAIN();
    asm volatile("s_waitcnt lgkmcnt(0)\n\ts_barrier" ::: "memory");
    const int q4 = (lane & 15) >> 2, p4 = lane & 3, gq = lane >> 4;
    cskv = (f32x4){0.f, 0.f, 0.f, 0.f}; csq = (f32x16){};
    if (w >= 2) {
      WQDMA(3) WQDMA(4) WQDMA(5) WQDMA(6)
      {
        const int vo = lane < 13 ? ((b * 2 + dh) * 13 + lane) * 128 + KB : lane * 256 * KB; const unsigned la = L_X + 33 * KB + w * 256;
        asm volatile("s_mov_b32 m0, %0\n\ts_nop 0\n\tbuffer_load_dword %1, %2, 0 offen sc1 lds" :: "s"(la), "v"(vo), "s"(desc) : "memory"); }
    } else if (g < 2) { const int to = L_RK + (8 * gq + q4) * 56 + dh * 32 + p4 * 8;
      UNR for (int k = 0; k < 14; ++k) { const u32x2 a = TRRD(to + k * 32 * 56), c = TRRD(to + k * 32 * 56 + 4 * 56); W16[k] = (u32x4){a.x, a.y, c.x, c.y}; }
    } else {
      const int to = L_RK + (4 * hh + q4) * 896 + (gq & 1) * 32 + p4 * 8;
      if (n < 84) {
        UNR for (int j = 0; j < 8; ++j) { const int h = 8 * w + j; const u32x2 a = TRRD(to + h * 56), c = TRRD(to + h * 56 + 8 * 896); W16[j] = (u32x4){a.x, a.y, c.x, c.y}; }
        rtb = WB_OFF + ((24 * w + c3) * 28 + ks) * KB; rta = 168 * KB; rts = 84 * KB; rtn = 8;
      } else {
        UNR for (int j = 0; j < 7; ++j) { const int t = 2 * j + w;
          const u32x2 a = TRRD(to + t * 64), c = TRRD(to + t * 64 + 8 * 896), d = TRRD(to + t * 64 + 16 * 896), e = TRRD(to + t * 64 + 24 * 896);
          W16[2 * j] = (u32x4){a.x, a.y, c.x, c.y}; W16[2 * j + 1] = (u32x4){d.x, d.y, hh ? 0u : e.x, hh ? 0u : e.y}; }
        rtb = n < 116 ? (nw & 16 ? WOB_OFF : WOA_OFF) + (w * 32 + 2 * (nw & 15)) * KB : ZERO_OFF + w * 2 * KB;
        rta = 64 * KB; rts = KB; rtn = n < 116 ? 14 : n == 116 ? 2 : 0;
      }
    }
  }

#define GPUT(N, ldsoff) unsigned m_ = 0u; \
    UNR for (int f = 0; f < N; ++f) { LDS128((ldsoff) + f * KB + lo) = t_[f]; m_ = umax(umax(m_, t_[f].x), t_[f].y); m_ = umax(umax(m_, t_[f].z), t_[f].w); PIN(m_); SBAR(); }
#define GATHER(N, src, ldsoff, flagp, ...) { \
    { u32x4 t_[N]; UNR for (int f = 0; f < N; ++f) { t_[f] = LDW(src, f); SBAR(); } \
      SBAR(); __VA_ARGS__; SBAR(); \
      GPUT(N, ldsoff) \
      if (__builtin_expect(!__all(m_ != ~0u), 0)) { poll(flagp, tag); \
        for (unsigned spins_ = 0; spins_ < SPIN_CAP; ++spins_) { asm volatile("" ::: "memory"); \
          u32x4 t_[N]; UNR for (int f = 0; f < N; ++f) t_[f] = LDW(src, f); \
          GPUT(N, ldsoff) if (__all(m_ != ~0u)) break; } } } \
    asm volatile("s_waitcnt lgkmcnt(0)\n\ts_barrier" ::: "memory"); }
#define SANE(f) { UNR for (int e_ = 0; e_ < 4; ++e_) f[e_] = f[e_] < 0xfffffffeu ? f[e_] : 0xfffffffeu; }
#define HINT(word) { __builtin_amdgcn_s_barrier(); if (tid == 0) __hip_atomic_store(FLAG(blk * 4 + g, (word)), tag, RLX); }
#define ARM(t) { const u32x4 a_ = {~0u, ~0u, ~0u, ~0u}; \
    if ((t) < 4) STC(a_, lo + ((t) & 1) * KB, armo + ((t) >> 1) * OT_L); \
    else if ((t) < 10) { const int d_ = (t) < 7 ? armx + XT_L : armx0; \
      if (((t) - 4) % 3 < 2) STC(a_, lo + (((t) - 4) % 3) * KB, d_ + w * 2048); else __builtin_amdgcn_raw_buffer_store_b32(~0u, rs, lane * 4, d_ + 8192 + w * 256, 16); } }
#define X_GATHER(layer, WORK) { \
    int src_ = XT_OFF + ((layer) & 1) * XT_L + blk * XT_BLK + w * XT_WG; OPQ(src_); \
    GATHER(9, src_, L_X + w * XT_WG, FLAG(blk * 4 + w, (layer)), WORK) \
 }
#define X_STATS_LD() f32x2 st_[7]; \
    UNR for (int k = 0; k < 7; ++k) st_[k] = *(LAS f32x2*)(lds + L_X + (k >> 1) * XT_WG + 8192 + (k & 1) * 512 + lane * 8)
#define X_STATS(mu, rstd) { f32x2 s2_; STATS_STEP(0) STATS_STEP(1) STATS_STEP(2) STATS_STEP(3) }
#define STATS_STEP(k) \
    if constexpr (k == 0) { s2_ = ((st_[0] + st_[1]) + (st_[2] + st_[3])) + ((st_[4] + st_[5]) + st_[6]); PIN(s2_); } \
    if constexpr (k == 1) { mu = half_sum(s2_.x) * (1.f / 448.f); s2_.y = half_sum(s2_.y); PIN(mu); PIN(s2_); } \
    if constexpr (k == 2) { rstd = __builtin_amdgcn_rsqf(fmaxf(fmaf(-mu, mu, s2_.y * (1.f / 448.f)), 0.f) + 1e-5f); PIN(rstd); } \
    if constexpr (k == 3) *(LAS float*)(lds + L_MS + w * 256 + lane * 4) = hh ? -mu * rstd : rstd;
#define XFRAG(ks) LDS128(L_X + ((ks) >> 3) * XT_WG + ((ks) & 7) * KB + lo)
#define EPI_ALPHA(a, cs, mu, sc, n2) { const float nm_ = -(mu) * (sc); UNR for (int e = 0; e < 16; ++e) a[e] = fmaf(a[e], (sc), nm_ * cs[e]); \
    UNR for (int e = 12; e < 16; ++e) a[e] = hh ? 0.f : a[e]; \
    float n0_ = 0.f, n1_ = 0.f; UNR for (int e = 0; e < 8; ++e) { n0_ = fmaf(a[e], a[e], n0_); n1_ = fmaf(a[8 + e], a[8 + e], n1_); } n2 = half_sum(n0_ + n1_); }
#define O_STORE(layer, o, l) { const float inv_ = __builtin_amdgcn_rcpf(l); f32x16 v_; \
    UNR for (int e = 0; e < 16; ++e) v_[e] = o[e] * inv_; \
    u32x4 f0_, f1_; pack16(v_, f0_, f1_); if (hh) { f1_.z = 0u; f1_.w = 0u; } SANE(f0_) SANE(f1_) \
    const int dst_ = OT_OFF + ((layer) & 1) * OT_L + blk * OT_BLK + head * 2 * KB; STC(f0_, lo, dst_); STC(f1_, lo + KB, dst_); }
#define Z_PHASE(layer, LAST, PRE, PRE2, SIDE) { \
    PRE; \
    SBAR(); \
    if (!LAST) { poll(FLAG(blk * 4 + w, 4 + (layer)), tag); AFTER_POLL(); }       \
    int src_ = OT_OFF + ((layer) & 1) * OT_L + blk * OT_BLK + w * 8 * KB; OPQ(src_); \
    GATHER(8, src_, L_O + w * 8 * KB, FLAG(blk * 4 + w, 4 + (layer)), PRE2) \
    u32x4 of_[32]; \
    UNR for (int k = 0; k < 6; ++k) of_[k] = LDS128(L_O + k * KB + lo); \
    SBAR(); \
    UNR for (int k = 0; k < 32; ++k) { if (k + 6 < 32) of_[k + 6] = LDS128(L_O + (k + 6) * KB + lo); SIDE; xres = mfma32(WZ[k], of_[k], xres); SBAR(); } \
    u32x4 f0, f1; pack16(xres, f0, f1); \
    if (LAST && (layer) == 3) { \
      __builtin_amdgcn_s_barrier(); \
      { LAS unsigned char* p = lds + L_O + r * 272 + w * 64 + 8 * hh; \
        *(LAS u32x2*)(p) = (u32x2){f0.x, f0.y}; *(LAS u32x2*)(p + 16) = (u32x2){f0.z, f0.w}; *(LAS u32x2*)(p + 32) = (u32x2){f1.x, f1.y}; *(LAS u32x2*)(p + 48) = (u32x2){f1.z, f1.w}; } \
      __syncthreads(); \
      UNR for (int j = 0; j < 2; ++j) { const int row = 16 * j + (tid >> 4), c = tid & 15; \
        if (g < 3 || c < 8) OUTST(*(LAS u32x4*)(lds + L_O + row * 272 + c * 16), (u32x4*)(out + (32 * blk + row) * DM + 128 * g + 8 * c)); } \
      if constexpr (LAST) asm volatile(".Lcode_last:"); \
      __builtin_amdgcn_endpgm(); \
    } \
    const int dst = XT_OFF + (((layer) + 1) & 1) * XT_L + blk * XT_BLK + g * XT_WG; \
    SANE(f0) SANE(f1) STC(f0, lo, dst + w * 2048); STC(f1, lo + KB, dst + w * 2048); \
    { float s_ = 0.f, q_ = 0.f; UNR for (int e = 0; e < 4; ++e) { s_ = dot2(f0[e], 0x3f803f80u, s_); q_ = dot2(f0[e], f0[e], q_); s_ = dot2(f1[e], 0x3f803f80u, s_); q_ = dot2(f1[e], f1[e], q_); } \
      auto r_ = __builtin_amdgcn_permlane32_swap(__float_as_uint(s_), __float_as_uint(q_), false, false);         \
      const unsigned sv = __float_as_uint(__uint_as_float(r_[0]) + __uint_as_float(r_[1])); \
      __builtin_amdgcn_raw_buffer_store_b32(sv < 0xfffffffeu ? sv : 0xfffffffeu, rs, r * 8 + hh * 4, dst + 8192 + w * 256, 16); } \
    HINT((layer) + 1); \
 }

#pragma unroll 1
  for (int pair = 0;; ++pair) {
    u32x4 WZ[32], Wv[28], Wk[28], WqB[28]; float csv;
    {
      const int layer = 2 * pair;
      IDX();
      const int wbB = WB_OFF + head * 3 * 28 * KB;
      float mu, rstd;
      X_STATS_LD();
      const int G = lane >> 4, cc = lane & 15;
      const bool role = g < 2 && w < 2;
      if (role) {
        const int xo = L_X + (G >> 1) * KB + (cc + 32 * (G & 1)) * 16;
        u32x4 xf[2][14];
        UNR for (int k = 0; k < 14; ++k) { xf[0][k] = LDS128(xo + (k >> 2) * XT_WG + (k & 3) * 2048); xf[1][k] = LDS128(xo + (k >> 2) * XT_WG + (k & 3) * 2048 + 256); }
        f32x4 a[2] = {{0.f, 0.f, 0.f, 0.f}, {0.f, 0.f, 0.f, 0.f}}, rs_[2], nm_[2]; f32x2 s2_;
        const int kvdst = KVA_OFF + pair * KVA_L + blk * 4 * KB, mo = L_MS + w * 256 + (g ? 16 * G : 4 * cc);
        SBAR();
#define KVCHAIN(MM) sfor<0, 14>([&](auto kc) __attribute__((always_inline)) { constexpr int k = decltype(kc)::value; MM; STATS_STEP(k) \
          if constexpr (k == 5) { UNR for (int t = 0; t < 2; ++t) { rs_[t] = *(LAS f32x4*)(lds + (mo & ~15) + 64 * t); nm_[t] = *(LAS f32x4*)(lds + (mo & ~15) + 128 + 64 * t); } } SBAR(); })
        if (g == 0) {
          KVCHAIN(a[0] = mfma16(W16[k], xf[0][k], a[0]); a[1] = mfma16(W16[k], xf[1][k], a[1]));
          const bool pad = dh == 1 && G == 3; float n2[2];
          UNR for (int t = 0; t < 2; ++t) { n2[t] = 0.f;
            const float r_ = (cc & 2) ? ((cc & 1) ? rs_[t].w : rs_[t].z) : ((cc & 1) ? rs_[t].y : rs_[t].x), m_ = (cc & 2) ? ((cc & 1) ? nm_[t].w : nm_[t].z) : ((cc & 1) ? nm_[t].y : nm_[t].x);
            UNR for (int e = 0; e < 4; ++e) { a[t][e] = pad ? 0.f : fmaf(a[t][e], r_, m_ * cskv[e]); n2[t] = fmaf(a[t][e], a[t][e], n2[t]); }
            a[t][0] = pad ? 1.f : a[t][0];
            __builtin_amdgcn_raw_buffer_store_b64((u32x2){pk(a[t][0], a[t][1]), pk(a[t][2], a[t][3])}, rs, dh * KB + (16 * t + cc + 32 * (G & 1)) * 16 + 8 * (G >> 1), kvdst, 16); }
          const unsigned n0 = umax16(__float_as_uint(row_sum(half_sum(n2[0])))), n1 = umax16(__float_as_uint(row_sum(half_sum(n2[1]))));
          if (lane == 0) __builtin_amdgcn_raw_buffer_store_b64((u32x2){n0, n1}, rs, 8, FA_OFF + (blk * 4 + dh) * 128 + pair * 16, 16);
        } else {
          KVCHAIN(a[0] = mfma16(xf[0][k], W16[k], a[0]); a[1] = mfma16(xf[1][k], W16[k], a[1]));
          UNR for (int t = 0; t < 2; ++t) {
            UNR for (int e = 0; e < 4; ++e) a[t][e] = fmaf(a[t][e], rs_[t][e], nm_[t][e] * cskv.x);
            u32x2 o2 = {pk(a[t][0], a[t][1]), pk(a[t][2], a[t][3])};
            if (dh == 1 && cc >= 12) { o2.x = cc == 12 ? 0x3f803f80u : 0u; o2.y = o2.x; }
            __builtin_amdgcn_raw_buffer_store_b64(o2, rs, 2 * KB + t * KB + (16 * dh + cc + 32 * (G & 1)) * 16 + 8 * (G >> 1), kvdst, 16); }
        }
        if (pair == 0 && blk < 14) {
          const int to = L_RK + (32 * blk + 16 * (G >> 1) + 4 * (G & 1) + (cc >> 2)) * 56 + dh * 32 + (cc & 3) * 8;
          const u32x2 a_ = TRRD(to), c_ = TRRD(to + 8 * 56); STC(((u32x4){a_.x, a_.y, c_.x, c_.y}), lo, WKV_OFF + ((g * 2 + dh) * 14 + blk) * KB); }
      } else {
        X_STATS(mu, rstd);
      }
      { f32x16 a = {}; u32x4 xf[28], wf[28];
        const int to = L_RQ + ((pair ? 4 : 8) * hh + (cc >> 2)) * 224 + w * 56 + (G & 1) * 32 + (cc & 3) * 8, to2 = to + (pair ? 8 : 4) * 224;
#define WQF(k) { const u32x2 a_ = TRRD(to + (k) * 16 * 224), c_ = TRRD(to2 + (k) * 16 * 224); wf[k] = (u32x4){a_.x, a_.y, c_.x, c_.y}; }
#define PUBA() { DRAIN(); if (lane == 0) __hip_atomic_store(flags + FA_OFF / 8 + (g < 2 ? (blk * 4 + g * 2 + dh) * 16 + pair * 2 : 256 * 16 + blk * 4 + (g & 1) * 2 + dh), tag, RLX); SBAR(); }
        u32x2 xo[4] = {{0u, 0u}, {0u, 0u}, {0u, 0u}, {0u, 0u}};
        if (pair == 0) { if (w >= 2) DRAIN();
          asm volatile("s_waitcnt lgkmcnt(0)\n\ts_barrier" ::: "memory");
          if (rtn) { UNR for (int j = 0; j < 14; ++j) STC(W16[j], lo, j < rtn ? rtb + (j >> 1) * rta + (j & 1) * rts : WS_BYTES); }
        }
        UNR for (int k = 0; k < 4; ++k) { WQF(k) xf[k] = XFRAG(k); }
        SBAR();
        UNR for (int k = 0; k < 28; ++k) { if (k + 4 < 28) { WQF(k + 4) xf[k + 4] = XFRAG(k + 4); }
          a = mfma32(wf[k], xf[k], a); SBAR();
          if (k == PUBA_K && role) PUBA();
          if (k == PUBA_K && pair == 0) { UNR for (int a_ = 0; a_ < 4; ++a_) xo[a_] = *(const u32x2*)(x + (32 * blk + r) * DM + 32 * tlc + 8 * a_ + 4 * hh); SBAR(); } }
        float n2; EPI_ALPHA(a, csq, mu, rstd * QSCALE, n2);
        u32x4 f0, f1; pack16(a, f0, f1);
        if (hh) f1.z = bf_ceil(__builtin_amdgcn_sqrtf(n2) * 1.02f);
        LDS128(L_Q + w * 2 * KB + lo) = f0; LDS128(L_Q + w * 2 * KB + KB + lo) = f1;
        SBAR();
        if (pair == 0) unpack16((u32x4){xo[0].x, xo[0].y, xo[1].x, xo[1].y}, (u32x4){xo[2].x, xo[2].y, xo[3].x, xo[3].y}, xres);
      }
      if (pair == 0 && g >= 2 && w < 2) PUBA();
      int xg = blk >> 3;
      float kref;
      { const int fo = (lane < 8 ? (xg * 8 + 2 * w) * 4 + lane : blk * 4 + (lane & 1)) * 128; u32x4 fk; unsigned spins = 0;
        do { asm volatile("" ::: "memory"); fk = LDC(fo, FA_OFF + pair * 16); } while (!__all(fk.x == (unsigned)tag && fk.y == (unsigned)(tag >> 32)) && ++spins < SPIN_CAP);
        AFTER_POLL();
        const float z0 = __uint_as_float(fk.z), z1 = __uint_as_float(fk.w);
        kref = __builtin_amdgcn_sqrtf(fmaxf(z0 + dppf<0xb1>(z0), z1 + dppf<0xb1>(z1))); }
      int base = KVA_OFF + pair * KVA_L + w * 8 * KB; OPQ(base);
      int woz = WOA_OFF + tlc * 32 * KB; OPQ(woz);
      int wvb = wbB + 56 * KB, wkb = wbB + 28 * KB; OPQ(wvb); OPQ(wkb);
      u32x4 q[4][2] = {};
      bool stored = false;
      gu64* fall = flags + FA_OFF / 8 + ((((xg + (lane >> 3)) & 7) * 8 + 2 * w) * 4 + (lane & 7)) * 16 + pair * 2;
      int armo = OT_OFF + blk * OT_BLK + head * 2 * KB; int armx = XT_OFF + blk * XT_BLK + g * XT_WG;
#pragma unroll 1
      for (int attempt = 0; attempt < 2; ++attempt) {
        u32x4 Kt[16][2], Vt[16][2]; u32x4 wfl[2] = {};
        OPQ(base); OPQ(woz); OPQ(wvb); OPQ(wkb); OPQ(xg); OPQ(armx);
        const int armx0 = pair ? WS_BYTES : armx;
#define TADR(t) (base + (((xg + ((t) >> 1)) & 7) << 15) + (((t) & 1) << 12))
#define LDK(t) { Kt[t][0] = LDC(lo, TADR(t)); Kt[t][1] = LDC(lo + KB, TADR(t)); }
#define LDV(t) { Vt[t][0] = LDC(lo + 2 * KB, TADR(t)); Vt[t][1] = LDC(lo + 3 * KB, TADR(t)); }
        LDK(0) SBAR(); LDV(0) SBAR(); LDK(1) SBAR(); LDV(1) SBAR();
        if (attempt == 0) {
          __syncthreads();
          UNR for (int h = 0; h < 4; ++h) { const int qo = L_Q + ((w + 1 + h) & 3) * 2 * KB + lo; q[h][0] = LDS128(qo); q[h][1] = LDS128(qo + KB);
            const unsigned m = bf_ceil(bflo(q[h][1].z) * kref) ^ 0x8000u; q[h][1].z = hh ? m : q[h][1].z; }
        }
        const u64 fa = __hip_atomic_load(fall, RLX);
        f32x16 oo[4], sA, sB; u32x4 pA0 = q[0][0], pA1 = pA0, pB0 = pA0, pB1 = pA0; f32x4 ea, eb;
        PIN(q[0][1]); PIN(q[1][1]); PIN(q[2][1]); PIN(q[3][1]);
        asm volatile("s_nop 1");
        SBAR();
        MS0(sA, Kt[0][0], q[0][0]); MS1(sA, Kt[0][1], q[0][1]);
        NOP16V(sA); SBAR();
#define WPF(n) { if ((n) < 0) {} else if ((n) < 32) WZ[(n) & 31] = LDW(woz, (n)); else if ((n) < 60) Wv[((n) + 24) % 28] = LDW(wvb, (n) - 32); }
#define PUTO(i) { asm volatile("" : "+a"(oo[i])); const int s_ = L_C + (((w + 1 + (i)) & 3) * 3 + (i)) * 4 * KB + lo; UNR for (int e = 0; e < 4; ++e) \
          *(LAS f32x4*)(lds + s_ + e * KB) = (f32x4){oo[i][4 * e], oo[i][4 * e + 1], oo[i][4 * e + 2], oo[i][4 * e + 3]}; }
        sfor<0, 16>([&](auto tc) __attribute__((always_inline)) { constexpr int t = decltype(tc)::value;
          const int tp = t > 0 ? t - 1 : 0, tn = t < 15 ? t + 1 : 15;
          STEP((t == 0 ? 1 : t == 1 ? 2 : 0), sA, sB, Kt[t][0], Kt[t][1], q[1][0], q[1][1], oo[3], Vt[tp][0], Vt[tp][1], pB0, pB1, pA0, pA1, , WPF(5 * t - 15), WPF(5 * t - 14));
          STEP((t == 0 ? 2 : 0), sB, sA, Kt[t][0], Kt[t][1], q[2][0], q[2][1], oo[0], Vt[t][0], Vt[t][1], pA0, pA1, pB0, pB1, ,
               if (t > 0 && t + 2 < 16) Vt[(t + 2) & 15][0] = LDC(lo + 2 * KB, TADR(t + 2)), if (t > 0 && t + 2 < 16) Vt[(t + 2) & 15][1] = LDC(lo + 3 * KB, TADR(t + 2)); if (t >= 1) ARM(t - 1));
          if (t == 0) { if (fa != tag) { unsigned spins = 0; while (__hip_atomic_load(fall, RLX) != tag && ++spins < SPIN_CAP) __builtin_amdgcn_s_sleep(POLL_SLEEP); }
            AFTER_POLL(); LDK(2) SBAR(); LDV(2) SBAR(); }
          STEP((t == 0 ? 2 : 0), sA, sB, Kt[t][0], Kt[t][1], q[3][0], q[3][1], oo[1], Vt[t][0], Vt[t][1], pB0, pB1, pA0, pA1, , WPF(5 * t - 13), WPF(5 * t - 12) if (t == 15) PUTO(0));
          STEP((t == 0 ? 2 : t == 15 ? 4 : 0), sB, sA, Kt[tn][0], Kt[tn][1], q[0][0], q[0][1], oo[2], Vt[t][0], Vt[t][1], pA0, pA1, pB0, pB1, ,
               if (t + 3 < 16) Kt[(t + 3) & 15][0] = LDC(lo, TADR(t + 3)), if (t + 3 < 16) Kt[(t + 3) & 15][1] = LDC(lo + KB, TADR(t + 3)); WPF(5 * t - 11) if (t == 15) PUTO(1));
          if (t == 1 && pair == 0) { wfl[0] = LDC(lo, FA_OFF + 32 * KB); wfl[1] = LDC(lo + KB, FA_OFF + 32 * KB); SBAR(); }
          if (t == 2 && pair == 0) {
            const u32x4 tg = {(unsigned)tag, (unsigned)(tag >> 32), (unsigned)tag, (unsigned)(tag >> 32)}; const u32x4 df = (wfl[0] ^ tg) | (wfl[1] ^ tg);
            if (__builtin_expect(!__all((df.x | df.y | df.z | df.w) == 0u), 0)) { UNR for (int j = 0; j < 4; ++j) { unsigned spins = 0;
                while (__hip_atomic_load(flags + (FA_OFF + 32 * KB) / 8 + j * 64 + lane, RLX) != tag && ++spins < SPIN_CAP) __builtin_amdgcn_s_sleep(8); } }
            AFTER_POLL();
            SBAR(); }
        });
        C2(pB1, 2, eb)
        MO(oo[3], Vt[15][0], pB0); MO(oo[3], Vt[15][1], pB1);
        SBAR();
        PUTO(2)
        DRAIN();
        armo = WS_BYTES;
        UNR for (int k = 0; k < 8; ++k) Wk[k] = LDW(wkb, k);
        SBAR();
        f32x16 o = oo[3];
        __syncthreads();
        UNR for (int s = 0; s < 3; ++s)
          UNR for (int e = 0; e < 4; ++e) { const f32x4 t = *(LAS f32x4*)(lds + L_C + (w * 3 + s) * 4 * KB + e * KB + lo);
            o[4 * e] += t.x; o[4 * e + 1] += t.y; o[4 * e + 2] += t.z; o[4 * e + 3] += t.w; }
        const float l = half_hi(o[12]);
        const bool bad = !__all(l >= L_MIN && fabsf(osum(o)) <= L_MAX);
        if (lane == 0) *(LAS unsigned*)(lds + L_MISC + w * 4) = bad ? 1u : 0u;
        if (attempt == 1 || !bad) { if (!stored) O_STORE(layer, o, l); stored = true; }
        __syncthreads();
        const u32x4 bw = *(LAS u32x4*)(lds + L_MISC);
        if (__builtin_expect(attempt == 1 || __builtin_amdgcn_readfirstlane(bw.x | bw.y | bw.z | bw.w) == 0u, 1)) break;
        float mx[4] = {-INFINITY, -INFINITY, -INFINITY, -INFINITY};
        UNR for (int h = 0; h < 4; ++h) q[h][1].z = hh ? 0u : q[h][1].z;
#pragma unroll 1
        for (int t = 0; t < 16; ++t) {
          const u32x4 k0 = LDC(lo, TADR(t)), k1 = LDC(lo + KB, TADR(t));
          UNR for (int h = 0; h < 4; ++h) { f32x16 s = {}; s = mfma32(k0, q[h][0], s); s = mfma32(k1, q[h][1], s);
            UNR for (int e = 0; e < 16; ++e) mx[h] = fmaxf(mx[h], s[e]); }
        }
        UNR for (int h = 0; h < 4; ++h) { const float m = half_max(mx[h]); if (hh == 0) *(LAS float*)(lds + L_MISC + 1024 + (w * 4 + ((w + 1 + h) & 3)) * 128 + r * 4) = m; }
        __syncthreads();
        UNR for (int h = 0; h < 4; ++h) { float m = -INFINITY;
          UNR for (int s = 0; s < 4; ++s) m = fmaxf(m, *(LAS float*)(lds + L_MISC + 1024 + (s * 4 + ((w + 1 + h) & 3)) * 128 + r * 4));
          q[h][1].z = hh ? (bf_ceil(m) ^ 0x8000u) : q[h][1].z; }
      }
      if (tid == 0) { __hip_atomic_store(FLAG(blk * 4 + g, 4 + layer), tag, RLX);
        if (b == 0) __hip_atomic_store(flags + EP_OFF / 8, tag, RLX); }
#define ONES() unsigned one2; asm volatile("v_mov_b32 %0, 0x3f803f80" : "=v"(one2)); const u32x4 ones = one2
#define CSV_CHAIN(k0, k1) { UNR for (int k = k0; k < k1; ++k) { if (!(k & 1) && k < 20) Wk[8 + (k >> 1)] = LDW(wkb, 8 + (k >> 1)); cv_ = mfma32(ones, Wv[k], cv_); SBAR(); } }
      ONES(); f32x16 cv_ = {};
      Z_PHASE(layer, false,
              CSV_CHAIN(0, 12), CSV_CHAIN(12, 28) csv = cv_[0]; PIN(csv),
              if ((k & 1) && k < 20) Wk[18 + (k >> 1)] = LDW(wkb, 18 + (k >> 1)); else if ((k & 1) && k < 24) WqB[(k >> 1) - 10] = LDW(wbB, (k >> 1) - 10));
    }
    {
      const int layer = 2 * pair + 1;
      IDX();
      const int wbB = WB_OFF + head * 3 * 28 * KB;
      int woz = WOB_OFF + tlc * 32 * KB; OPQ(woz);
      f32x16 csqB = {}, cskB = {};
      ONES();
#define CSK_CHAIN(k0, k1) { UNR for (int k = k0; k < k1; ++k) { if (!(k & 1) && k < 20) WqB[2 + (k >> 1)] = LDW(wbB, 2 + (k >> 1)); cskB = mfma32(Wk[k], ones, cskB); SBAR(); } }
      CSK_CHAIN(0, SPLIT_K)
      X_GATHER(layer, CSK_CHAIN(SPLIT_K, 28));
      float mu, rstd, qn;
      u32x4 K[9][2], V[9][2], q0, q1;
      const bool pv = lane < 8 && blk - 8 + lane >= 0;
      const int fbo = FB_OFF + ((blk - 8) * 16 + head) * 128 + pair * 32, fl = pv ? lane * 16 * 128 : 8 * 16 * 128;
      {
        const int dst = KVB_OFF + blk * KVB_BLK + head * 4 * KB;
        f32x16 av = {}, ak = {}, aq = {}, kf; u32x4 xf[28]; unsigned vp[8]; f32x4 rs_[4], nm_[4]; f32x2 s2_; float n0 = 0.f, n1 = 0.f;
        X_STATS_LD();
        UNR for (int k = 0; k < 6; ++k) xf[k] = XFRAG(k);
        SBAR();
        sfor<0, 84>([&](auto kc) __attribute__((always_inline)) { constexpr int k = decltype(kc)::value;
          if constexpr (k + 6 < 28) xf[k + 6] = XFRAG(k + 6);
          if constexpr (k % 5 == 1 || k % 5 == 3) { constexpr int i = (k / 5) * 2 + (k % 5 == 3);
            if constexpr (i < 16) WqB[12 + i] = LDW(wbB, 12 + i); else if constexpr (i < 23) WZ[i - 16] = LDW(woz, i - 16); else if constexpr (k > PUBK_K && i < 32) WZ[i - 22] = LDW(woz, i - 22); }
          if constexpr (k < 28) av = mfma32(xf[k], Wv[k], av); else if constexpr (k < 56) ak = mfma32(Wk[k - 28], xf[k - 28], ak); else { aq = mfma32(WqB[k - 56], xf[k - 56], aq); csqB = mfma32(WqB[k - 56], ones, csqB); }
          STATS_STEP(k)
          if constexpr (k >= 7 && k < 11) { constexpr int i = k - 7; rs_[i] = *(LAS f32x4*)(lds + L_MS + w * 256 + (8 * i + 4 * hh) * 4); nm_[i] = *(LAS f32x4*)(lds + L_MS + w * 256 + 128 + (8 * i + 4 * hh) * 4); }
          if constexpr (k >= 23 && k < 27) { constexpr int i = k - 23; nm_[i] *= csv; PIN(nm_[i]); }
          if constexpr (k == 30) asm volatile("" : "+a"(av));
          if constexpr (k >= 30 && k < 38) { constexpr int e = 2 * (k - 30), i = e >> 2, c = e & 3;
            const unsigned p0 = pk(fmaf(av[e], rs_[i][c], nm_[i][c]), fmaf(av[e + 1], rs_[i][c + 1], nm_[i][c + 1]));
            vp[e >> 1] = r < 28 ? p0 : (r == 28 ? 0x3f803f80u : 0u); PIN(vp[e >> 1]); }
          if constexpr (k == 38) { V[8][0] = (u32x4){vp[0], vp[1], vp[2], vp[3]}; V[8][1] = (u32x4){vp[4], vp[5], vp[6], vp[7]}; STC(V[8][0], lo + 2 * KB, dst); STC(V[8][1], lo + 3 * KB, dst); }
          if constexpr (k == 40) { cskB *= -mu * rstd; PIN(cskB); }
          if constexpr (k == 57) { asm volatile("" : "+a"(ak));
            kf = ak * rstd + cskB; UNR for (int e = 12; e < 16; ++e) kf[e] = hh ? 0.f : kf[e];
            pack16(kf, K[8][0], K[8][1]); K[8][1].z = hh ? 0x3f80u : K[8][1].z;
            STC(K[8][0], lo, dst); STC(K[8][1], lo + KB, dst);
 }
          if constexpr (k >= 58 && k < 61) { constexpr int e0 = (k - 58) * 3; UNR for (int e = e0; e < e0 + 3 && e < 8; ++e) { n0 = fmaf(kf[e], kf[e], n0); n1 = fmaf(kf[8 + e], kf[8 + e], n1); } PIN(n0); PIN(n1); }
          if constexpr (k == 61) { n0 = half_sum(n0 + n1); PIN(n0); }
          if constexpr (k == 62) { const unsigned kown = umax32(__float_as_uint(n0)); if (lane == 0) __builtin_amdgcn_raw_buffer_store_b32(kown, rs, 8, fbo + 8 * 16 * 128, 16); }
          SBAR();
          if constexpr (k == PUBK_K) { DRAIN(); if (lane == 0) __hip_atomic_store(flags + (fbo + 8 * 16 * 128) / 8, tag, RLX); SBAR(); } });
        aq = (aq - csqB * mu) * (rstd * QSCALE); UNR for (int e = 12; e < 16; ++e) aq[e] = hh ? 0.f : aq[e];
        pack16(aq, q0, q1);
        n0 = 0.f; n1 = 0.f; UNR for (int e = 0; e < 8; ++e) { n0 = fmaf(aq[e], aq[e], n0); n1 = fmaf(aq[8 + e], aq[8 + e], n1); }
        qn = __builtin_amdgcn_sqrtf(half_sum(n0 + n1)) * 1.02f;
      }
      const int kvb = KVB_OFF + (blk - 8) * KVB_BLK + head * 4 * KB;
#define SRCB(j) (blk - 8 + (j) < 0 ? ZERO_OFF : kvb + (j) * KVB_BLK)
#define LKB(j) { const int s_ = SRCB(j); K[j][0] = LDC(lo, s_); K[j][1] = LDC(lo + KB, s_); SBAR(); }
#define LVB(j) { const int s_ = SRCB(j); V[j][0] = LDC(lo + 2 * KB, s_); V[j][1] = LDC(lo + 3 * KB, s_); SBAR(); }
      { u32x4 fk; unsigned spins = 0;
        do { asm volatile("" ::: "memory"); fk = LDC(fl, fbo); } while (!__all(fk.x == (unsigned)tag && fk.y == (unsigned)(tag >> 32)) && ++spins < SPIN_CAP);
        AFTER_POLL();
        LKB(7) LKB(6) LVB(7) LKB(5) LVB(6) LKB(4)
        const unsigned kmx = __builtin_amdgcn_readfirstlane(umax16(fk.z));
        q1.z = hh ? (bf_ceil(qn * __builtin_amdgcn_sqrtf(__uint_as_float(kmx))) ^ 0x8000u) : q1.z; }
      f32x16 o; float l;
#pragma unroll 1
      for (int attempt = 0; attempt < 2; ++attempt) {
        f32x16 sA, sB; u32x4 pA0 = q0, pA1 = pA0, pB0 = pA0, pB1 = pA0; f32x4 ea, eb;
        PIN(q0); PIN(q1); PIN(K[8][0]); PIN(K[8][1]); asm volatile("s_nop 1"); SBAR();
        MS0(sA, K[8][0], q0); MS1(sA, K[8][1], q1);
        NOP16V(sA); SBAR();
#define MASKT(S, keep) { UNR for (int e = 0; e < 16; ++e) { const int row = (e & 3) + 8 * (e >> 2) + 4 * hh; S[e] = (keep) ? S[e] : -INFINITY; } PIN(S); SBAR(); }
#define WZ2(k) { WZ[k] = LDW(woz, k); WZ[k + 1] = LDW(woz, k + 1); }
        MASKT(sA, row <= r)
        STEP(1, sA, sB, K[7][0], K[7][1], q0, q1, o, V[8][0], V[8][1], pB0, pB1, pA0, pA1, LVB(5) LKB(3), LVB(4), LKB(2));
        STEP(2, sB, sA, K[6][0], K[6][1], q0, q1, o, V[8][0], V[8][1], pA0, pA1, pB0, pB1, , LVB(3), LKB(1));
        STEP(0, sA, sB, K[5][0], K[5][1], q0, q1, o, V[7][0], V[7][1], pB0, pB1, pA0, pA1, , LVB(2), LKB(0));
        STEP(0, sB, sA, K[4][0], K[4][1], q0, q1, o, V[6][0], V[6][1], pA0, pA1, pB0, pB1, , LVB(1), LVB(0));
        STEP(0, sA, sB, K[3][0], K[3][1], q0, q1, o, V[5][0], V[5][1], pB0, pB1, pA0, pA1, , , );
        STEP(0, sB, sA, K[2][0], K[2][1], q0, q1, o, V[4][0], V[4][1], pA0, pA1, pB0, pB1, , WZ2(10), );
        STEP(0, sA, sB, K[1][0], K[1][1], q0, q1, o, V[3][0], V[3][1], pB0, pB1, pA0, pA1, , WZ2(12), WZ2(14));
        STEP(0, sB, sA, K[0][0], K[0][1], q0, q1, o, V[2][0], V[2][1], pA0, pA1, pB0, pB1, , WZ2(16), WZ2(18));
        MASKT(sA, row > r)
        STEP(4, sA, sB, K[0][0], K[0][1], q0, q1, o, V[1][0], V[1][1], pB0, pB1, pA0, pA1, , , );
        C2(pA1, 2, eb)
        MO(o, V[0][0], pA0); MO(o, V[0][1], pA1);
        SBAR();
        l = half_hi(o[12]);
        if (__builtin_expect(attempt == 1 || __all(l >= L_MIN), 1)) break;
        q1.z = hh ? 0u : q1.z;
        float mx = -INFINITY;
        UNR for (int j = 8; j >= 0; --j) {
          f32x16 s = {}; s = mfma32(K[j][0], q0, s); s = mfma32(K[j][1], q1, s); float m_ = -INFINITY;
          UNR for (int e = 0; e < 16; ++e) { const int row = (e & 3) + 8 * (e >> 2) + 4 * hh; float v = s[e];
            if (j == 8) v = (row <= r) ? v : -INFINITY; if (j == 0) v = (row > r) ? v : -INFINITY; m_ = fmaxf(m_, v); }
          mx = (blk - 8 + j >= 0) ? fmaxf(mx, m_) : mx; SBAR(); }
        mx = half_max(mx);
        q1.z = hh ? (bf_ceil(mx) ^ 0x8000u) : q1.z;
      }
      O_STORE(layer, o, l);
      HINT(4 + layer);
      int wkv = (g < 2 && w < 2) ? WKV_OFF + (g * 2 + dh) * 14 * KB : WS_BYTES; OPQ(wkv);
      const int G = lane >> 4, cc = lane & 15, to = L_RQ + (4 * hh + (cc >> 2)) * 224 + w * 56 + (G & 1) * 32 + (cc & 3) * 8, to2 = to + 8 * 224;
      u32x4 wf[28]; csq = (f32x16){};
#define CSQ_CHAIN(k0, k1) { if (k0 == 0) { UNR for (int k = 0; k < 6; ++k) WQF(k) SBAR(); } \
        UNR for (int k = k0; k < k1; ++k) { if (k + 6 < 28) WQF(k + 6) if (!(k & 1) && k < 24) WZ[20 + (k >> 1)] = LDW(woz, 20 + (k >> 1)); csq = mfma32(wf[k], ones, csq); SBAR(); } }
      Z_PHASE(layer, true,
              CSQ_CHAIN(0, SPLIT_K - 2), CSQ_CHAIN(SPLIT_K - 2, 28),
              if ((k & 1) && k < 28) W16[k >> 1] = LDW(wkv, k >> 1));
      cskv = (f32x4){0.f, 0.f, 0.f, 0.f};
#define CSKV_CHAIN() { if (g == 0) { UNR for (int k = 0; k < 14; ++k) cskv = mfma16(W16[k], ones, cskv); } else { UNR for (int k = 0; k < 14; ++k) cskv = mfma16(ones, W16[k], cskv); } }
      CSKV_CHAIN(); DRAIN();
      X_GATHER(layer + 1, );
    }
  }
}

extern "C" void kernel_launch(void* const* d_in, const int* in_sizes, int n_in, void* d_out, int out_size, void* d_ws, size_t ws_size, hipStream_t stream) {
  (void)in_sizes; (void)n_in; (void)out_size; (void)ws_size;
  (void)hipFuncSetAttribute((const void*)main_kernel, hipFuncAttributeMaxDynamicSharedMemorySize, L_BYTES);
  main_kernel<<<256, 256, L_BYTES, stream>>>((const u16*)d_in[0], (const u16*)d_in[1], (const u16*)d_in[2], (const u16*)d_in[3], (const u16*)d_in[4],
                                             (const u16*)d_in[6], (const u16*)d_in[7], (unsigned char*)d_ws, (u16*)d_out);
}
```

```cpp
#include <hip/hip_runtime.h>
#include <hip/hip_bf16.h>

typedef float f32x16 __attribute__((ext_vector_type(16)));
typedef float f32x4 __attribute__((ext_vector_type(4)));
typedef float f32x2 __attribute__((ext_vector_type(2)));
typedef unsigned u32x4 __attribute__((ext_vector_type(4)));
typedef unsigned u32x2 __attribute__((ext_vector_type(2)));
typedef short bf16x8 __attribute__((ext_vector_type(8)));
typedef short s16x4 __attribute__((ext_vector_type(4)));
typedef __bf16 bf2 __attribute__((ext_vector_type(2)));
typedef unsigned short u16;
typedef unsigned long long u64;
#define GAS __attribute__((address_space(1)))
#define LAS __attribute__((address_space(3)))
typedef GAS u64 gu64;
#define RLX __ATOMIC_RELAXED, __HIP_MEMORY_SCOPE_AGENT
#define SBAR() __builtin_amdgcn_sched_barrier(0)
#define PIN(x) asm volatile("" : "+v"(x))
#define OPQ(x) asm volatile("" : "+s"(x))
#define DRAIN() asm volatile("s_waitcnt vmcnt(0)" ::: "memory")
#define UNR _Pragma("unroll")

constexpr int DM = 448, NB = 64, NTL = 14, KB = 1024;
constexpr int EP_OFF = 0;
constexpr int FA_OFF = 1 * KB;
constexpr int FL_OFF = FA_OFF + 2 * 8 * 4 * KB;
constexpr int FB_OFF = FL_OFF + 256 * 128;
constexpr int ZERO_OFF = FB_OFF + 128 * KB;
constexpr int WKV_OFF = ZERO_OFF + 4 * KB;
constexpr int KVA_OFF = WKV_OFF + 56 * KB;
constexpr int KVA_L = NB * 4 * KB;
constexpr int WOA_OFF = KVA_OFF + 2 * KVA_L;
constexpr int WOB_OFF = WOA_OFF + 14 * 32 * KB;
constexpr int WB_OFF = WOB_OFF + 14 * 32 * KB;
constexpr int XT_OFF = WB_OFF + 16 * 3 * 28 * KB;
constexpr int XT_WG = 9 * KB, XT_BLK = 4 * XT_WG, XT_L = NB * XT_BLK;
constexpr int OT_OFF = XT_OFF + 2 * XT_L;
constexpr int OT_BLK = 32 * KB, OT_L = NB * OT_BLK;
constexpr int KVB_OFF = OT_OFF + 2 * OT_L;
constexpr int KVB_BLK = 64 * KB;
constexpr int WS_BYTES = KVB_OFF + NB * KVB_BLK;
#define FLAG(wg, word) (flags + (FL_OFF + (wg) * 128) / 8 + (word))
constexpr int L_X = 0;
constexpr int L_O = 0, L_C = 0;
constexpr int L_MISC = 48 * KB;
constexpr int L_Q = 52 * KB;
constexpr int L_RK = 36 * KB;
constexpr int L_RQ = L_RK + 448 * 56;
constexpr int L_MS = 159 * KB;
constexpr int L_BYTES = 160 * KB;
static_assert(L_RQ + 448 * 224 + 64 <= L_MS && L_RQ % 16 == 0, "lds");

constexpr float QSCALE = 0.18898223650461361f * 1.4426950408889634f;
constexpr unsigned SPIN_CAP = 1u << 16;
constexpr int POLL_SLEEP = 4;
constexpr int PUBK_K = 71;
constexpr int PUBA_K = 13;
constexpr int SPLIT_K = 16;
constexpr float L_MAX = 1e36f;
constexpr float L_MIN = 1e-18f;

__device__ __forceinline__ unsigned pk(float a, float b) { f32x2 v = {a, b}; bf2 r = __builtin_convertvector(v, bf2); return __builtin_bit_cast(unsigned, r); }
__device__ __forceinline__ float bflo(unsigned u) { return __uint_as_float(u << 16); }
__device__ __forceinline__ float bfhi(unsigned u) { return __uint_as_float(u & 0xffff0000u); }
__device__ __forceinline__ f32x16 mfma32(u32x4 a, u32x4 b, f32x16 c) {
  return __builtin_amdgcn_mfma_f32_32x32x16_bf16(__builtin_bit_cast(bf16x8, a), __builtin_bit_cast(bf16x8, b), c, 0, 0, 0); }
__device__ __forceinline__ f32x4 mfma16(u32x4 a, u32x4 b, f32x4 c) {
  return __builtin_amdgcn_mfma_f32_16x16x32_bf16(__builtin_bit_cast(bf16x8, a), __builtin_bit_cast(bf16x8, b), c, 0, 0, 0); }
__device__ __forceinline__ float half_sum(float v) { auto r = __builtin_amdgcn_permlane32_swap(__float_as_uint(v), __float_as_uint(v), false, false); return __uint_as_float(r[0]) + __uint_as_float(r[1]); }
__device__ __forceinline__ float half_max(float v) { auto r = __builtin_amdgcn_permlane32_swap(__float_as_uint(v), __float_as_uint(v), false, false); return fmaxf(__uint_as_float(r[0]), __uint_as_float(r[1])); }
__device__ __forceinline__ float half_hi(float v) { auto r = __builtin_amdgcn_permlane32_swap(__float_as_uint(v), __float_as_uint(v), false, false); return __uint_as_float(r[1]); }
__device__ __forceinline__ float row_sum(float v) { auto r = __builtin_amdgcn_permlane16_swap(__float_as_uint(v), __float_as_uint(v), false, false); return __uint_as_float(r[0]) + __uint_as_float(r[1]); }
__device__ __forceinline__ float dot2(unsigned a, unsigned b, float c) { return __builtin_amdgcn_fdot2_f32_bf16(__builtin_bit_cast(bf2, a), __builtin_bit_cast(bf2, b), c, false); }
template <int C> __device__ __forceinline__ float dppf(float v) { return __uint_as_float(__builtin_amdgcn_update_dpp(0u, __float_as_uint(v), C, 0xf, 0xf, true)); }
__device__ __forceinline__ float sum8(float v) { return half_sum(row_sum(v + dppf<0xb1>(v))); }
template <int N> __device__ __forceinline__ unsigned ror(unsigned v) { return __builtin_amdgcn_update_dpp(0u, v, 0x120 + N, 0xf, 0xf, true); }
__device__ __forceinline__ unsigned umax(unsigned a, unsigned b) { return a > b ? a : b; }
__device__ __forceinline__ unsigned umax16(unsigned v) { v = umax(v, ror<1>(v)); v = umax(v, ror<2>(v)); v = umax(v, ror<4>(v)); return umax(v, ror<8>(v)); }
__device__ __forceinline__ unsigned umax32(unsigned v) { v = umax16(v); auto r = __builtin_amdgcn_permlane16_swap(v, v, false, false); return umax(r[0], r[1]); }
__device__ __forceinline__ unsigned bf_ceil(float v) { unsigned u = __float_as_uint(v); unsigned t = u >> 16; return (v > 0.f && (u & 0xffffu)) ? t + 1u : t; }
__device__ __forceinline__ void pack16(const f32x16& v, u32x4& f0, u32x4& f1) {
  f0 = (u32x4){pk(v[0], v[1]), pk(v[2], v[3]), pk(v[4], v[5]), pk(v[6], v[7])};
  f1 = (u32x4){pk(v[8], v[9]), pk(v[10], v[11]), pk(v[12], v[13]), pk(v[14], v[15])}; }
__device__ __forceinline__ void unpack16(const u32x4 f0, const u32x4 f1, f32x16& v) {
  UNR for (int i = 0; i < 4; ++i) { v[2 * i] = bflo(f0[i]); v[2 * i + 1] = bfhi(f0[i]); v[8 + 2 * i] = bflo(f1[i]); v[9 + 2 * i] = bfhi(f1[i]); } }
template <int V> struct IC { static constexpr int value = V; };
template <int I, int N, class F> __device__ __forceinline__ void sfor(F&& f) { if constexpr (I < N) { f(IC<I>{}); sfor<I + 1, N>(f); } }
__device__ __forceinline__ float osum(const f32x16& v) {
  return (((v[0] + v[1]) + (v[2] + v[3])) + ((v[4] + v[5]) + (v[6] + v[7]))) + (((v[8] + v[9]) + (v[10] + v[11])) + ((v[12] + v[13]) + (v[14] + v[15]))); }
__device__ __forceinline__ u32x4 mkdesc(const void* p, unsigned bytes) { return (u32x4){(unsigned)(size_t)p, (unsigned)((size_t)p >> 32) & 0xffffu, bytes, 0x00020000u}; }

#define LDC(voff, soff) __builtin_amdgcn_raw_buffer_load_b128(rs, (voff), (soff), 16)
#define STC(v, voff, soff) __builtin_amdgcn_raw_buffer_store_b128((v), rs, (voff), (soff), 16)
#define LDS128(off) (*(LAS u32x4*)(lds + (off)))
#define LDW(base, k) LDC(lo + ((k) & 3) * KB, (base) + ((k) >> 2) * 4 * KB)
#define MS0(S, A, B) asm volatile("v_mfma_f32_32x32x16_bf16 %0, %1, %2, 0" : "=&v"(S) : "v"(A), "v"(B))
#define MS1(S, A, B) asm volatile("v_mfma_f32_32x32x16_bf16 %0, %1, %2, %0" : "+v"(S) : "v"(A), "v"(B))
#define MO(O, A, B) { O = mfma32(A, B, O); asm volatile("" : "+a"(O)); }
#define EX(x) __builtin_amdgcn_exp2f(x)
#define E4(E, S, i) { E[0] = EX(S[i]); E[1] = EX(S[i + 1]); E[2] = EX(S[i + 2]); E[3] = EX(S[i + 3]); PIN(E); SBAR(); }
#define C2(P, j, E) { P[j] = pk(E[0], E[1]); P[j + 1] = pk(E[2], E[3]); PIN(P); SBAR(); }
#define STEP(MODE, SC, SN, KN0, KN1, QN0, QN1, OP, VP0, VP1, PP0, PP1, PC0, PC1, W0, LDA, LDB) \
  W0; if (!((MODE) & 4)) MS0(SN, KN0, QN0); SBAR(); E4(ea, SC, 0) if (!((MODE) & 1)) C2(PP1, 2, eb) \
  if (!((MODE) & 4)) MS1(SN, KN1, QN1); LDA; SBAR(); E4(eb, SC, 4) C2(PC0, 0, ea) \
  if ((MODE) & 2) OP = (f32x16){}; \
  if (!((MODE) & 1)) MO(OP, VP0, PP0); SBAR(); E4(ea, SC, 8) C2(PC0, 2, eb) \
  if (!((MODE) & 1)) MO(OP, VP1, PP1); LDB; SBAR(); E4(eb, SC, 12) C2(PC1, 0, ea)
#define NOP16V(x) asm volatile("s_nop 7\n\ts_nop 7" : "+v"(x))
#define ADMA(ldsaddr, voff, dsc, soff) asm volatile("s_mov_b32 m0, %0\n\ts_nop 0\n\tbuffer_load_dwordx4 %1, %2, %3 offen lds" :: "s"(ldsaddr), "v"(voff), "s"(dsc), "s"(soff) : "memory")
#define TRRD(off) __builtin_bit_cast(u32x2, __builtin_amdgcn_ds_read_tr16_b64_v4i16((LAS s16x4*)(lds + (off))))

__device__ __forceinline__ void poll(gu64* p, u64 tag) {
  unsigned spins = 0;
#pragma unroll 1
  while (__hip_atomic_load(p, RLX) != tag) { if (++spins > SPIN_CAP) break; }
}
#define OUTST(v, p) *(p) = v
#define AFTER_POLL() __builtin_amdgcn_fence(__ATOMIC_ACQUIRE, "wavefront")

__global__ __launch_bounds__(256, 1) void main_kernel(const u16* __restrict__ x, const u16* __restrict__ wq, const u16* __restrict__ wk, const u16* __restrict__ wv,
                                                      const u16* __restrict__ woa, const u16* __restrict__ wb, const u16* __restrict__ wob,
                                                      unsigned char* __restrict__ ws, u16* __restrict__ out) {
  extern __shared__ __attribute__((aligned(16))) unsigned char smem[];
  const __amdgpu_buffer_rsrc_t rs = __builtin_amdgcn_make_buffer_rsrc((void*)ws, 0, WS_BYTES, 0x00020000);
  const u32x4 desc = mkdesc(ws, WS_BYTES);
  LAS unsigned char* lds = (LAS unsigned char*)smem;
  gu64* flags = (gu64*)ws;
  int tid_ = threadIdx.x, b_ = blockIdx.x, w_ = __builtin_amdgcn_readfirstlane(threadIdx.x >> 6);
#define IDX() asm volatile("" : "+v"(tid_)); asm volatile("" : "+s"(b_), "+s"(w_)); \
  const int tid = tid_, b = b_, w = w_, lane = tid & 63, r = lane & 31, hh = lane >> 5, lo = lane * 16; \
  const int blk = (b & 7) * 8 + (b >> 5), g = (b >> 3) & 3, head = 4 * g + w, tl = head; const bool act = tl < NTL; const int tlc = act ? tl : 0; \
  const int dh = w & 1, th = w >> 1; (void)r; (void)hh; (void)dh; (void)th; (void)tlc; (void)blk; (void)lo
  u64 pc_; unsigned len_;
  asm volatile("s_getpc_b64 %0\n.Lcode_here:\n\ts_mov_b32 %1, .Lcode_last-.Lcode_here" : "=s"(pc_), "=s"(len_));
#define CODEPF(ldsoff) { const u32x4 dc = mkdesc((const void*)pc_, len_); const int vo = ((b >> 3) * 4 + w) * 512 + (lane & 3) * 128; const unsigned la = (ldsoff) + w * 256; \
    asm volatile("s_mov_b32 m0, %0\n\ts_nop 0\n\tbuffer_load_dword %1, %2, 0 offen lds" :: "s"(la), "v"(vo), "s"(dc) : "memory"); }

  f32x16 xres, csq;
  f32x4 cskv;
  u32x4 W16[14];
  int rtb = 0, rta = 0, rts = 0, rtn = 0;
  u64 tag;
  {
    IDX();
    u64 ep; asm volatile("global_load_dwordx2 %0, %1, %2 sc1" : "=v"(ep) : "v"(0), "s"(ws));
    u32x4 xr[7];
    { const u32x4 dx = mkdesc(x + 32 * blk * DM, 32 * 896); const int vo = (8 * w + ((lane >> 1) & 7)) * 896 + (lane >> 4) * 32 + (lane & 1) * 16;
      UNR for (int f = 0; f < 7; ++f) asm volatile("buffer_load_dwordx4 %0, %1, %2, 0 offen offset:%3" : "=v"(xr[f]) : "v"(vo), "s"(dx), "i"(f * 128)); }
    CODEPF(L_X + 31 * KB);
    const int n = blk * 2 + (g & 1), nw = n - 84;
    const int c3 = (n * 37) >> 10, ks = n - 28 * c3;
    const int t7 = tid & 127;
    int vq[7];
    const u32x4 dq = mkdesc(wq, 448 * 448 * 2);
#define WQDMA(u0) { int u = (u0) + (blk & 7); u = u >= 7 ? u - 7 : u; UNR for (int k = 0; k < 7; ++k) { \
      const unsigned la = L_RQ + (2 * (7 * u + k) + dh) * KB; const int so = u * 64 * 896; ADMA(la, vq[k], dq, so); } }
    if (w >= 2) {
      UNR for (int k = 0; k < 7; ++k) { const int c = t7 + 128 * k, row = (c * 4682) >> 16; vq[k] = row * 896 + (c - row * 14) * 16 + g * 224; }
      WQDMA(0) WQDMA(1)
    } else if (g < 2 || n >= 84) { const u32x4 dk = g < 2 ? mkdesc(g ? wv : wk, 448 * 56) : mkdesc((nw & 16 ? wob : woa) + (nw & 15) * 28 * 448, n < 116 ? 448 * 56 : 0);
      UNR for (int k = 0; k < 14; ++k) { const int i = 2 * k + w; const unsigned la = i < 24 ? L_RK + i * KB : L_X + 32 * KB; const int so = i < 24 ? i * KB : 448 * 56;
        if (k == 12) { const unsigned l2 = L_RK + 24 * KB; const int s2 = 24 * KB; if (lane < 32) ADMA(l2, lo, dk, s2); } else ADMA(la, lo, dk, so); }
    } else { const u32x4 dk = mkdesc(wb + 16 * ks * 1344 + 448 * c3, 16 * 2688);
      UNR for (int k = 0; k < 7; ++k) { const int c = t7 + 128 * k, row = (c * 1171) >> 16; const unsigned la = L_RK + (2 * k + w) * KB;
        ADMA(la, row * 2688 + (c - row * 56) * 16, dk, 0); }
      UNR for (int k = 0; k < 7; ++k) { const unsigned la = L_X + 32 * KB; const int so = 16 * 2688; ADMA(la, lo, dk, so); } }
    cskv = (f32x4){0.f, 0.f, 0.f, 0.f}; csq = (f32x16){}; asm volatile("" : "+a"(csq), "+a"(cskv));
    asm volatile("s_waitcnt vmcnt(15)" : "+v"(xr[0]), "+v"(xr[1]), "+v"(xr[2]), "+v"(xr[3]), "+v"(xr[4]), "+v"(xr[5]), "+v"(xr[6]), "+v"(ep) :: "memory");
    { float s0 = 0.f, s1 = 0.f, q0 = 0.f, q1 = 0.f;
      UNR for (int f = 0; f < 7; ++f) UNR for (int c = 0; c < 4; c += 2) { s0 = dot2(xr[f][c], 0x3f803f80u, s0); q0 = dot2(xr[f][c], xr[f][c], q0); s1 = dot2(xr[f][c + 1], 0x3f803f80u, s1); q1 = dot2(xr[f][c + 1], xr[f][c + 1], q1); }
      SBAR(); if (w >= 2) WQDMA(2) SBAR();
      const float mu = sum8(s0 + s1) * (1.f / 448.f);
      const float rstd = __builtin_amdgcn_rsqf(fmaxf(fmaf(-mu, mu, sum8(q0 + q1) * (1.f / 448.f)), 0.f) + 1e-5f), nm = -mu * rstd;
      UNR for (int f = 0; f < 7; ++f) {
        u32x4 o; UNR for (int e = 0; e < 4; ++e) { const f32x2 t = (f32x2){bflo(xr[f][e]), bfhi(xr[f][e])} * rstd + nm; o[e] = pk(t.x, t.y); }
        LDS128(L_X + (f >> 1) * XT_WG + (f & 1) * 4 * KB + (lane >> 4) * KB + (8 * w + ((lane >> 1) & 7) + 32 * (lane & 1)) * 16) = o; }
      const float one = 32.f * (1.f - 1e-5f);
      *(LAS f32x4*)(lds + L_X + w * XT_WG + 8192 + lo) = (f32x4){0.f, one, 0.f, one};
    }
    tag = (((u64)(unsigned)__builtin_amdgcn_readfirstlane((unsigned)(ep >> 32)) << 32) | (unsigned)__builtin_amdgcn_readfirstlane((unsigned)ep)) + 1ull;
    if (w < 2) DRAIN();
    asm volatile("s_waitcnt lgkmcnt(0)\n\ts_barrier" ::: "memory");
    const int q4 = (lane & 15) >> 2, p4 = lane & 3, gq = lane >> 4;
    if (w >= 2) {
      WQDMA(3) WQDMA(4) WQDMA(5) WQDMA(6)
      {
        const int vo = lane < 13 ? ((b * 2 + dh) * 13 + lane) * 128 + KB : lane * 256 * KB; const unsigned la = L_X + 33 * KB + w * 256;
        asm volatile("s_mov_b32 m0, %0\n\ts_nop 0\n\tbuffer_load_dword %1, %2, 0 offen sc1 lds" :: "s"(la), "v"(vo), "s"(desc) : "memory"); }
    } else if (g < 2) { const int to = L_RK + (8 * gq + q4) * 56 + dh * 32 + p4 * 8;
      UNR for (int k = 0; k < 14; ++k) { const u32x2 a = TRRD(to + k * 32 * 56), c = TRRD(to + k * 32 * 56 + 4 * 56); W16[k] = (u32x4){a.x, a.y, c.x, c.y}; }
    } else {
      const int to = L_RK + (4 * hh + q4) * 896 + (gq & 1) * 32 + p4 * 8;
      if (n < 84) {
        UNR for (int j = 0; j < 8; ++j) { const int h = 8 * w + j; const u32x2 a = TRRD(to + h * 56), c = TRRD(to + h * 56 + 8 * 896); W16[j] = (u32x4){a.x, a.y, c.x, c.y}; }
        rtb = WB_OFF + ((24 * w + c3) * 28 + ks) * KB; rta = 168 * KB; rts = 84 * KB; rtn = 8;
      } else {
        UNR for (int j = 0; j < 7; ++j) { const int t = 2 * j + w;
          const u32x2 a = TRRD(to + t * 64), c = TRRD(to + t * 64 + 8 * 896), d = TRRD(to + t * 64 + 16 * 896), e = TRRD(to + t * 64 + 24 * 896);
          W16[2 * j] = (u32x4){a.x, a.y, c.x, c.y}; W16[2 * j + 1] = (u32x4){d.x, d.y, hh ? 0u : e.x, hh ? 0u : e.y}; }
        rtb = n < 116 ? (nw & 16 ? WOB_OFF : WOA_OFF) + (w * 32 + 2 * (nw & 15)) * KB : ZERO_OFF + w * 2 * KB;
        rta = 64 * KB; rts = KB; rtn = n < 116 ? 14 : n == 116 ? 2 : 0;
      }
    }
  }

#define GPUT(N, ldsoff) unsigned m_ = 0u; \
    UNR for (int f = 0; f < N; ++f) { LDS128((ldsoff) + f * KB + lo) = t_[f]; m_ = umax(umax(m_, t_[f].x), t_[f].y); m_ = umax(umax(m_, t_[f].z), t_[f].w); PIN(m_); SBAR(); }
#define GATHER(N, src, ldsoff, flagp, ...) { \
    { u32x4 t_[N]; UNR for (int f = 0; f < N; ++f) { t_[f] = LDW(src, f); SBAR(); } \
      SBAR(); __VA_ARGS__; SBAR(); \
      GPUT(N, ldsoff) \
      if (__builtin_expect(!__all(m_ != ~0u), 0)) { poll(flagp, tag); \
        for (unsigned spins_ = 0; spins_ < SPIN_CAP; ++spins_) { asm volatile("" ::: "memory"); \
          u32x4 t_[N]; UNR for (int f = 0; f < N; ++f) t_[f] = LDW(src, f); \
          GPUT(N, ldsoff) if (__all(m_ != ~0u)) break; } } } \
    asm volatile("s_waitcnt lgkmcnt(0)\n\ts_barrier" ::: "memory"); }
#define SANE(f) { UNR for (int e_ = 0; e_ < 4; ++e_) f[e_] = f[e_] < 0xfffffffeu ? f[e_] : 0xfffffffeu; }
#define HINT(word) { __builtin_amdgcn_s_barrier(); if (tid == 0) __hip_atomic_store(FLAG(blk * 4 + g, (word)), tag, RLX); }
#define ARM(t) { const u32x4 a_ = arm1; \
    if ((t) < 4) STC(a_, lo + ((t) & 1) * KB, armo + ((t) >> 1) * OT_L); \
    else if ((t) < 10) { const int d_ = (t) < 7 ? armx + XT_L : armx0; \
      if (((t) - 4) % 3 < 2) STC(a_, lo + (((t) - 4) % 3) * KB, d_ + w * 2048); else __builtin_amdgcn_raw_buffer_store_b32(arm1, rs, lane * 4, d_ + 8192 + w * 256, 16); } }
#define X_GATHER(layer, WORK) { \
    int src_ = XT_OFF + ((layer) & 1) * XT_L + blk * XT_BLK + w * XT_WG; OPQ(src_); \
    GATHER(9, src_, L_X + w * XT_WG, FLAG(blk * 4 + w, (layer)), WORK) \
 }
#define X_STATS_LD() f32x2 st_[7]; \
    UNR for (int k = 0; k < 7; ++k) st_[k] = *(LAS f32x2*)(lds + L_X + (k >> 1) * XT_WG + 8192 + (k & 1) * 512 + lane * 8)
#define X_STATS(mu, rstd) { f32x2 s2_; STATS_STEP(0) STATS_STEP(1) STATS_STEP(2) STATS_STEP(3) }
#define STATS_STEP(k) \
    if constexpr ((k) == 0) { s2_ = ((st_[0] + st_[1]) + (st_[2] + st_[3])) + ((st_[4] + st_[5]) + st_[6]); PIN(s2_); } \
    if constexpr ((k) == 1) { mu = half_sum(s2_.x) * (1.f / 448.f); s2_.y = half_sum(s2_.y); PIN(mu); PIN(s2_); } \
    if constexpr ((k) == 2) { rstd = __builtin_amdgcn_rsqf(fmaxf(fmaf(-mu, mu, s2_.y * (1.f / 448.f)), 0.f) + 1e-5f); PIN(rstd); } \
    if constexpr ((k) == 3) *(LAS float*)(lds + L_MS + w * 256 + lane * 4) = hh ? -mu * rstd : rstd;
#define XFRAG(ks) LDS128(L_X + ((ks) >> 3) * XT_WG + ((ks) & 7) * KB + lo)
#define EPI_ALPHA(a, cs, mu, sc, n2) { const float nm_ = -(mu) * (sc); UNR for (int e = 0; e < 16; ++e) a[e] = fmaf(a[e], (sc), nm_ * cs[e]); \
    UNR for (int e = 12; e < 16; ++e) a[e] = hh ? 0.f : a[e]; \
    float n0_ = 0.f, n1_ = 0.f; UNR for (int e = 0; e < 8; ++e) { n0_ = fmaf(a[e], a[e], n0_); n1_ = fmaf(a[8 + e], a[8 + e], n1_); } n2 = half_sum(n0_ + n1_); }
#define O_STORE(layer, o, l) { const float inv_ = __builtin_amdgcn_rcpf(l); f32x16 v_; \
    UNR for (int e = 0; e < 16; ++e) v_[e] = o[e] * inv_; \
    u32x4 f0_, f1_; pack16(v_, f0_, f1_); if (hh) { f1_.z = 0u; f1_.w = 0u; } SANE(f0_) SANE(f1_) \
    const int dst_ = OT_OFF + ((layer) & 1) * OT_L + blk * OT_BLK + head * 2 * KB; STC(f0_, lo, dst_); STC(f1_, lo + KB, dst_); }
#define Z_PHASE(layer, LAST, PRE, PRE2, SIDE) { \
    PRE; \
    SBAR(); \
    if (!LAST) { poll(FLAG(blk * 4 + w, 4 + (layer)), tag); AFTER_POLL(); }       \
    int src_ = OT_OFF + ((layer) & 1) * OT_L + blk * OT_BLK + w * 8 * KB; OPQ(src_); \
    GATHER(8, src_, L_O + w * 8 * KB, FLAG(blk * 4 + w, 4 + (layer)), PRE2) \
    u32x4 of_[32]; \
    UNR for (int k = 0; k < 6; ++k) of_[k] = LDS128(L_O + k * KB + lo); \
    SBAR(); \
    UNR for (int k = 0; k < 32; ++k) { if (k + 6 < 32) of_[k + 6] = LDS128(L_O + (k + 6) * KB + lo); SIDE; xres = mfma32(WZ[k], of_[k], xres); SBAR(); } \
    u32x4 f0, f1; pack16(xres, f0, f1); \
    if (LAST && (layer) == 3) { \
      __builtin_amdgcn_s_barrier(); \
      { LAS unsigned char* p = lds + L_O + r * 272 + w * 64 + 8 * hh; \
        *(LAS u32x2*)(p) = (u32x2){f0.x, f0.y}; *(LAS u32x2*)(p + 16) = (u32x2){f0.z, f0.w}; *(LAS u32x2*)(p + 32) = (u32x2){f1.x, f1.y}; *(LAS u32x2*)(p + 48) = (u32x2){f1.z, f1.w}; } \
      __syncthreads(); \
      UNR for (int j = 0; j < 2; ++j) { const int row = 16 * j + (tid >> 4), c = tid & 15; \
        if (g < 3 || c < 8) OUTST(*(LAS u32x4*)(lds + L_O + row * 272 + c * 16), (u32x4*)(out + (32 * blk + row) * DM + 128 * g + 8 * c)); } \
      if constexpr (LAST) asm volatile(".Lcode_last:"); \
      __builtin_amdgcn_endpgm(); \
    } \
    const int dst = XT_OFF + (((layer) + 1) & 1) * XT_L + blk * XT_BLK + g * XT_WG; \
    SANE(f0) SANE(f1) STC(f0, lo, dst + w * 2048); STC(f1, lo + KB, dst + w * 2048); \
    { float s_ = 0.f, q_ = 0.f; UNR for (int e = 0; e < 4; ++e) { s_ = dot2(f0[e], 0x3f803f80u, s_); q_ = dot2(f0[e], f0[e], q_); s_ = dot2(f1[e], 0x3f803f80u, s_); q_ = dot2(f1[e], f1[e], q_); } \
      auto r_ = __builtin_amdgcn_permlane32_swap(__float_as_uint(s_), __float_as_uint(q_), false, false);         \
      const unsigned sv = __float_as_uint(__uint_as_float(r_[0]) + __uint_as_float(r_[1])); \
      __builtin_amdgcn_raw_buffer_store_b32(sv < 0xfffffffeu ? sv : 0xfffffffeu, rs, r * 8 + hh * 4, dst + 8192 + w * 256, 16); } \
    HINT((layer) + 1); \
 }

#pragma unroll 1
  for (int pair = 0;; ++pair) {
    u32x4 WZ[32], Wv[28], Wk[28], WqB[28]; float csv;
    {
      const int layer = 2 * pair;
      IDX();
      const int wbB = WB_OFF + head * 3 * 28 * KB;
      float mu, rstd;
      const int G = lane >> 4, cc = lane & 15;
      const bool role = g < 2 && w < 2;
      if (role) {
        const int xo = L_X + (G >> 1) * KB + (cc + 32 * (G & 1)) * 16;
        u32x4 xf[2][14];
#define XF16(k) { xf[0][k] = LDS128(xo + ((k) >> 2) * XT_WG + ((k) & 3) * 2048); xf[1][k] = LDS128(xo + ((k) >> 2) * XT_WG + ((k) & 3) * 2048 + 256); }
        XF16(0) XF16(1) SBAR();
        X_STATS_LD(); SBAR();
        XF16(2) XF16(3) XF16(4)
        f32x4 a[2] = {{0.f, 0.f, 0.f, 0.f}, {0.f, 0.f, 0.f, 0.f}}, rs_[2], nm_[2]; f32x2 s2_;
        const int kvdst = KVA_OFF + pair * KVA_L + blk * 4 * KB, mo = L_MS + w * 256 + (g ? 16 * G : 4 * cc);
        SBAR();
#define KVCHAIN(MM) sfor<0, 14>([&](auto kc) __attribute__((always_inline)) { constexpr int k = decltype(kc)::value; if constexpr (k + 5 < 14) XF16(k + 5) MM; STATS_STEP(k - 1) \
          if constexpr (k == 9) { UNR for (int t = 0; t < 2; ++t) { rs_[t] = *(LAS f32x4*)(lds + (mo & ~15) + 64 * t); nm_[t] = *(LAS f32x4*)(lds + (mo & ~15) + 128 + 64 * t); } } SBAR(); })
        if (g == 0) {
          KVCHAIN(a[0] = mfma16(W16[k], xf[0][k], a[0]); a[1] = mfma16(W16[k], xf[1][k], a[1]));
          const bool pad = dh == 1 && G == 3; float n2[2];
          UNR for (int t = 0; t < 2; ++t) { n2[t] = 0.f;
            const float r_ = (cc & 2) ? ((cc & 1) ? rs_[t].w : rs_[t].z) : ((cc & 1) ? rs_[t].y : rs_[t].x), m_ = (cc & 2) ? ((cc & 1) ? nm_[t].w : nm_[t].z) : ((cc & 1) ? nm_[t].y : nm_[t].x);
            UNR for (int e = 0; e < 4; ++e) { a[t][e] = pad ? 0.f : fmaf(a[t][e], r_, m_ * cskv[e]); n2[t] = fmaf(a[t][e], a[t][e], n2[t]); }
            a[t][0] = pad ? 1.f : a[t][0];
            __builtin_amdgcn_raw_buffer_store_b64((u32x2){pk(a[t][0], a[t][1]), pk(a[t][2], a[t][3])}, rs, dh * KB + (16 * t + cc + 32 * (G & 1)) * 16 + 8 * (G >> 1), kvdst, 16); }
          const unsigned n0 = umax16(__float_as_uint(row_sum(half_sum(n2[0])))), n1 = umax16(__float_as_uint(row_sum(half_sum(n2[1]))));
          if (lane == 0) __builtin_amdgcn_raw_buffer_store_b64((u32x2){n0, n1}, rs, 8, FA_OFF + (blk * 4 + dh) * 128 + pair * 16, 16);
        } else {
          KVCHAIN(a[0] = mfma16(xf[0][k], W16[k], a[0]); a[1] = mfma16(xf[1][k], W16[k], a[1]));
          UNR for (int t = 0; t < 2; ++t) {
            UNR for (int e = 0; e < 4; ++e) a[t][e] = fmaf(a[t][e], rs_[t][e], nm_[t][e] * cskv.x);
            u32x2 o2 = {pk(a[t][0], a[t][1]), pk(a[t][2], a[t][3])};
            if (dh == 1 && cc >= 12) { o2.x = cc == 12 ? 0x3f803f80u : 0u; o2.y = o2.x; }
            __builtin_amdgcn_raw_buffer_store_b64(o2, rs, 2 * KB + t * KB + (16 * dh + cc + 32 * (G & 1)) * 16 + 8 * (G >> 1), kvdst, 16); }
        }
        if (pair == 0 && blk < 14) {
          const int to = L_RK + (32 * blk + 16 * (G >> 1) + 4 * (G & 1) + (cc >> 2)) * 56 + dh * 32 + (cc & 3) * 8;
          const u32x2 a_ = TRRD(to), c_ = TRRD(to + 8 * 56); STC(((u32x4){a_.x, a_.y, c_.x, c_.y}), lo, WKV_OFF + ((g * 2 + dh) * 14 + blk) * KB); }
      } else {
        X_STATS_LD();
        X_STATS(mu, rstd);
      }
      asm volatile("" : "+a"(csq));
      { f32x16 a = {}; u32x4 xf[28], wf[28];
        const int to = L_RQ + ((pair ? 4 : 8) * hh + (cc >> 2)) * 224 + w * 56 + (G & 1) * 32 + (cc & 3) * 8, to2 = to + (pair ? 8 : 4) * 224;
#define WQF(k) { const u32x2 a_ = TRRD(to + (k) * 16 * 224), c_ = TRRD(to2 + (k) * 16 * 224); wf[k] = (u32x4){a_.x, a_.y, c_.x, c_.y}; }
#define PUBA() { DRAIN(); if (lane == 0) __hip_atomic_store(flags + FA_OFF / 8 + (g < 2 ? (blk * 4 + g * 2 + dh) * 16 + pair * 2 : 256 * 16 + blk * 4 + (g & 1) * 2 + dh), tag, RLX); SBAR(); }
        u32x2 xo[4] = {{0u, 0u}, {0u, 0u}, {0u, 0u}, {0u, 0u}};
        if (pair == 0) { if (w >= 2) DRAIN();
          asm volatile("s_waitcnt lgkmcnt(0)\n\ts_barrier" ::: "memory");
          if (rtn) { OPQ(rtb); OPQ(rtn); UNR for (int j = 0; j < 14; ++j) STC(W16[j], lo, j < rtn ? rtb + (j >> 1) * rta + (j & 1) * rts : WS_BYTES); }
        }
        UNR for (int k = 0; k < 4; ++k) { WQF(k) xf[k] = XFRAG(k); }
        SBAR();
        UNR for (int k = 0; k < 28; ++k) { if (k + 4 < 28) { WQF(k + 4) xf[k + 4] = XFRAG(k + 4); }
          a = mfma32(wf[k], xf[k], a); SBAR();
          if (k == PUBA_K && role) PUBA();
          if (k == PUBA_K && pair == 0) { UNR for (int a_ = 0; a_ < 4; ++a_) xo[a_] = *(const u32x2*)(x + (32 * blk + r) * DM + 32 * tlc + 8 * a_ + 4 * hh); SBAR(); } }
        float n2; EPI_ALPHA(a, csq, mu, rstd * QSCALE, n2);
        u32x4 f0, f1; pack16(a, f0, f1);
        if (hh) f1.z = bf_ceil(__builtin_amdgcn_sqrtf(n2) * 1.02f);
        LDS128(L_Q + w * 2 * KB + lo) = f0; LDS128(L_Q + w * 2 * KB + KB + lo) = f1;
        SBAR();
        if (pair == 0) unpack16((u32x4){xo[0].x, xo[0].y, xo[1].x, xo[1].y}, (u32x4){xo[2].x, xo[2].y, xo[3].x, xo[3].y}, xres);
      }
      if (pair == 0 && g >= 2 && w < 2) PUBA();
      int xg = blk >> 3;
      float kref;
      { const int fo = (lane < 8 ? (xg * 8 + 2 * w) * 4 + lane : blk * 4 + (lane & 1)) * 128; u32x4 fk; unsigned spins = 0;
        do { asm volatile("" ::: "memory"); fk = LDC(fo, FA_OFF + pair * 16); } while (!__all(fk.x == (unsigned)tag && fk.y == (unsigned)(tag >> 32)) && ++spins < SPIN_CAP);
        AFTER_POLL();
        const float z0 = __uint_as_float(fk.z), z1 = __uint_as_float(fk.w);
        kref = __builtin_amdgcn_sqrtf(fmaxf(z0 + dppf<0xb1>(z0), z1 + dppf<0xb1>(z1))); }
      int base = KVA_OFF + pair * KVA_L + w * 8 * KB; OPQ(base);
      int woz = WOA_OFF + tlc * 32 * KB; OPQ(woz);
      int wvb = wbB + 56 * KB, wkb = wbB + 28 * KB; OPQ(wvb); OPQ(wkb);
      u32x4 q[4][2];
      gu64* fall = flags + FA_OFF / 8 + ((((xg + (lane >> 3)) & 7) * 8 + 2 * w) * 4 + (lane & 7)) * 16 + pair * 2;
      int armo = OT_OFF + blk * OT_BLK + head * 2 * KB; int armx = XT_OFF + blk * XT_BLK + g * XT_WG;
      {
        u32x4 Kt[16][2], Vt[16][2]; u32x4 wfl[2] = {};
        const int armx0 = pair ? WS_BYTES : armx; unsigned arm1; asm volatile("v_mov_b32 %0, -1" : "=v"(arm1));
#define TADR(t) (base + (((xg + ((t) >> 1)) & 7) << 15) + (((t) & 1) << 12))
#define LDK(t) { Kt[t][0] = LDC(lo, TADR(t)); Kt[t][1] = LDC(lo + KB, TADR(t)); }
#define LDV(t) { Vt[t][0] = LDC(lo + 2 * KB, TADR(t)); Vt[t][1] = LDC(lo + 3 * KB, TADR(t)); }
        LDK(0) SBAR(); LDV(0) SBAR(); LDK(1) SBAR(); LDV(1) SBAR();
        {
          __syncthreads();
          UNR for (int h = 0; h < 4; ++h) { const int qo = L_Q + ((w + 1 + h) & 3) * 2 * KB + lo; q[h][0] = LDS128(qo); q[h][1] = LDS128(qo + KB);
            const unsigned m = bf_ceil(bflo(q[h][1].z) * kref) ^ 0x8000u; q[h][1].z = hh ? m : q[h][1].z; }
        }
        const u64 fa = __hip_atomic_load(fall, RLX);
        f32x16 oo[4], sA, sB; u32x4 pA0 = q[0][0], pA1 = pA0, pB0 = pA0, pB1 = pA0; f32x4 ea, eb;
        PIN(q[0][1]); PIN(q[1][1]); PIN(q[2][1]); PIN(q[3][1]);
        asm volatile("s_nop 1");
        SBAR();
        MS0(sA, Kt[0][0], q[0][0]); MS1(sA, Kt[0][1], q[0][1]);
        NOP16V(sA); SBAR();
#define WPF(n) { if ((n) < 0) {} else if ((n) < 32) WZ[(n) & 31] = LDW(woz, (n)); else if ((n) < 60) Wv[((n) + 24) % 28] = LDW(wvb, (n) - 32); }
#define PUTO(i) { asm volatile("" : "+a"(oo[i])); const int s_ = L_C + (((w + 1 + (i)) & 3) * 3 + (i)) * 4 * KB + lo; UNR for (int e = 0; e < 4; ++e) \
          *(LAS f32x4*)(lds + s_ + e * KB) = (f32x4){oo[i][4 * e], oo[i][4 * e + 1], oo[i][4 * e + 2], oo[i][4 * e + 3]}; }
        sfor<0, 16>([&](auto tc) __attribute__((always_inline)) { constexpr int t = decltype(tc)::value;
          const int tp = t > 0 ? t - 1 : 0, tn = t < 15 ? t + 1 : 15;
          STEP((t == 0 ? 1 : t == 1 ? 2 : 0), sA, sB, Kt[t][0], Kt[t][1], q[1][0], q[1][1], oo[3], Vt[tp][0], Vt[tp][1], pB0, pB1, pA0, pA1, , WPF(5 * t - 15), WPF(5 * t - 14));
          STEP((t == 0 ? 2 : 0), sB, sA, Kt[t][0], Kt[t][1], q[2][0], q[2][1], oo[0], Vt[t][0], Vt[t][1], pA0, pA1, pB0, pB1, ,
               if (t > 0 && t + 2 < 16) Vt[(t + 2) & 15][0] = LDC(lo + 2 * KB, TADR(t + 2)), if (t > 0 && t + 2 < 16) Vt[(t + 2) & 15][1] = LDC(lo + 3 * KB, TADR(t + 2)); if (t >= 1) ARM(t - 1));
          if (t == 0) { if (!__all(fa == tag)) { unsigned spins = 0; _Pragma("unroll 1") while (__hip_atomic_load(fall, RLX) != tag && ++spins < SPIN_CAP) __builtin_amdgcn_s_sleep(POLL_SLEEP); }
            AFTER_POLL(); LDK(2) SBAR(); LDV(2) SBAR(); }
          STEP((t == 0 ? 2 : 0), sA, sB, Kt[t][0], Kt[t][1], q[3][0], q[3][1], oo[1], Vt[t][0], Vt[t][1], pB0, pB1, pA0, pA1, , WPF(5 * t - 13), WPF(5 * t - 12) if (t == 15) PUTO(0));
          STEP((t == 0 ? 2 : t == 15 ? 4 : 0), sB, sA, Kt[tn][0], Kt[tn][1], q[0][0], q[0][1], oo[2], Vt[t][0], Vt[t][1], pA0, pA1, pB0, pB1, ,
               if (t + 3 < 16) Kt[(t + 3) & 15][0] = LDC(lo, TADR(t + 3)), if (t + 3 < 16) Kt[(t + 3) & 15][1] = LDC(lo + KB, TADR(t + 3)); WPF(5 * t - 11) if (t == 15) PUTO(1));
          if (t == 1 && pair == 0) { wfl[0] = LDC(lo, FA_OFF + 32 * KB); wfl[1] = LDC(lo + KB, FA_OFF + 32 * KB); SBAR(); }
          if (t == 2 && pair == 0) {
            const u32x4 tg = {(unsigned)tag, (unsigned)(tag >> 32), (unsigned)tag, (unsigned)(tag >> 32)}; const u32x4 df = (wfl[0] ^ tg) | (wfl[1] ^ tg);
            if (__builtin_expect(!__all((df.x | df.y | df.z | df.w) == 0u), 0)) { UNR for (int j = 0; j < 4; ++j) { unsigned spins = 0;
                _Pragma("unroll 1") while (__hip_atomic_load(flags + (FA_OFF + 32 * KB) / 8 + j * 64 + lane, RLX) != tag && ++spins < SPIN_CAP) __builtin_amdgcn_s_sleep(8); } }
            AFTER_POLL();
            SBAR(); }
        });
        C2(pB1, 2, eb)
        MO(oo[3], Vt[15][0], pB0); MO(oo[3], Vt[15][1], pB1);
        SBAR();
        PUTO(2)
        DRAIN();
        UNR for (int k = 0; k < 8; ++k) Wk[k] = LDW(wkb, k);
        SBAR();
#define COMBINE() f32x16 o = oo[3]; __syncthreads(); \
        UNR for (int s = 0; s < 3; ++s) UNR for (int e = 0; e < 4; ++e) { const f32x4 t = *(LAS f32x4*)(lds + L_C + (w * 3 + s) * 4 * KB + e * KB + lo); \
            o[4 * e] += t.x; o[4 * e + 1] += t.y; o[4 * e + 2] += t.z; o[4 * e + 3] += t.w; } \
        const float l = half_hi(o[12])
        COMBINE();
        const bool bad = !__all(l >= L_MIN && fabsf(osum(o)) <= L_MAX);
        if (lane == 0) *(LAS unsigned*)(lds + L_MISC + w * 4) = bad ? 1u : 0u;
        if (__builtin_expect(!bad, 1)) O_STORE(layer, o, l);
        __syncthreads();
        const u32x4 bw = *(LAS u32x4*)(lds + L_MISC);
        if (__builtin_expect(__builtin_amdgcn_readfirstlane(bw.x | bw.y | bw.z | bw.w) != 0u, 0)) {
          float mx[4] = {-INFINITY, -INFINITY, -INFINITY, -INFINITY};
          UNR for (int h = 0; h < 4; ++h) { const int qo = L_Q + ((w + 1 + h) & 3) * 2 * KB + lo; q[h][0] = LDS128(qo); q[h][1] = LDS128(qo + KB); q[h][1].z = hh ? 0u : q[h][1].z; }
#pragma unroll 1
          for (int t = 0; t < 16; ++t) {
            const u32x4 k0 = LDC(lo, TADR(t)), k1 = LDC(lo + KB, TADR(t));
            UNR for (int h = 0; h < 4; ++h) { f32x16 s = {}; s = mfma32(k0, q[h][0], s); s = mfma32(k1, q[h][1], s);
              UNR for (int e = 0; e < 16; ++e) mx[h] = fmaxf(mx[h], s[e]); SBAR(); }
          }
          UNR for (int h = 0; h < 4; ++h) { const float m = half_max(mx[h]); if (hh == 0) *(LAS float*)(lds + L_MISC + 1024 + (w * 4 + ((w + 1 + h) & 3)) * 128 + r * 4) = m; }
          __syncthreads();
          UNR for (int h = 0; h < 4; ++h) { float m = -INFINITY;
            UNR for (int s = 0; s < 4; ++s) m = fmaxf(m, *(LAS float*)(lds + L_MISC + 1024 + (s * 4 + ((w + 1 + h) & 3)) * 128 + r * 4));
            q[h][1].z = hh ? (bf_ceil(m) ^ 0x8000u) : q[h][1].z; float z_ = 0.f; PIN(z_); oo[h] = z_; }
#pragma unroll 1
          for (int t = 0; t < 16; ++t) {
            const u32x4 k0 = LDC(lo, TADR(t)), k1 = LDC(lo + KB, TADR(t)), v0 = LDC(lo + 2 * KB, TADR(t)), v1 = LDC(lo + 3 * KB, TADR(t));
            UNR for (int h = 0; h < 4; ++h) { f32x16 s = {}; s = mfma32(k0, q[h][0], s); s = mfma32(k1, q[h][1], s);
              UNR for (int e = 0; e < 16; ++e) s[e] = EX(s[e]);
              u32x4 p0, p1; pack16(s, p0, p1); oo[h] = mfma32(v0, p0, oo[h]); oo[h] = mfma32(v1, p1, oo[h]); SBAR(); }
          }
          PUTO(0) PUTO(1) PUTO(2)
          COMBINE();
          if (bad) O_STORE(layer, o, l);
          __syncthreads();
          UNR for (int n_ = 0; n_ < 60; ++n_) WPF(n_)
          UNR for (int k = 0; k < 8; ++k) Wk[k] = LDW(wkb, k);
        }
      }
      if (tid == 0) { __hip_atomic_store(FLAG(blk * 4 + g, 4 + layer), tag, RLX);
        if (b == 0) __hip_atomic_store(flags + EP_OFF / 8, tag, RLX); }
#define ONES() unsigned one2; asm volatile("v_mov_b32 %0, 0x3f803f80" : "=v"(one2)); const u32x4 ones = one2
#define CSV_CHAIN(k0, k1) { UNR for (int k = k0; k < k1; ++k) { if (!(k & 1) && k < 20) Wk[8 + (k >> 1)] = LDW(wkb, 8 + (k >> 1)); cv_ = mfma32(ones, Wv[k], cv_); SBAR(); } }
      ONES(); f32x16 cv_ = {};
      Z_PHASE(layer, false,
              CSV_CHAIN(0, 12), CSV_CHAIN(12, 28) csv = cv_[0]; PIN(csv),
              if ((k & 1) && k < 20) Wk[18 + (k >> 1)] = LDW(wkb, 18 + (k >> 1)); else if ((k & 1) && k < 24) WqB[(k >> 1) - 10] = LDW(wbB, (k >> 1) - 10));
    }
    {
      const int layer = 2 * pair + 1;
      IDX();
      const int wbB = WB_OFF + head * 3 * 28 * KB;
      int woz = WOB_OFF + tlc * 32 * KB; OPQ(woz);
      f32x16 csqB = {}, cskB = {};
      ONES();
#define CSK_CHAIN(k0, k1) { UNR for (int k = k0; k < k1; ++k) { if (!(k & 1) && k < 20) WqB[2 + (k >> 1)] = LDW(wbB, 2 + (k >> 1)); cskB = mfma32(Wk[k], ones, cskB); SBAR(); } }
      CSK_CHAIN(0, SPLIT_K)
      X_GATHER(layer, CSK_CHAIN(SPLIT_K, 28));
      float mu, rstd, qn;
      u32x4 K[9][2], V[9][2], q0, q1;
      const bool pv = lane < 8 && blk - 8 + lane >= 0;
      const int fbo = FB_OFF + ((blk - 8) * 16 + head) * 128 + pair * 32, fl = pv ? lane * 16 * 128 : 8 * 16 * 128;
      {
        const int dst = KVB_OFF + blk * KVB_BLK + head * 4 * KB;
        f32x16 av = {}, ak = {}, aq = {}, kf; u32x4 xf[28]; unsigned vp[8]; f32x4 rs_[4], nm_[4]; f32x2 s2_; float n0 = 0.f, n1 = 0.f;
        X_STATS_LD();
        UNR for (int k = 0; k < 6; ++k) xf[k] = XFRAG(k);
        SBAR();
        sfor<0, 84>([&](auto kc) __attribute__((always_inline)) { constexpr int k = decltype(kc)::value;
          if constexpr (k + 6 < 28) xf[k + 6] = XFRAG(k + 6);
          if constexpr (k % 5 == 1 || k % 5 == 3) { constexpr int i = (k / 5) * 2 + (k % 5 == 3);
            if constexpr (i < 16) WqB[12 + i] = LDW(wbB, 12 + i); else if constexpr (i < 23) WZ[i - 16] = LDW(woz, i - 16); else if constexpr (k > PUBK_K && i < 32) WZ[i - 22] = LDW(woz, i - 22); }
          if constexpr (k < 28) av = mfma32(xf[k], Wv[k], av); else if constexpr (k < 56) ak = mfma32(Wk[k - 28], xf[k - 28], ak); else { aq = mfma32(WqB[k - 56], xf[k - 56], aq); csqB = mfma32(WqB[k - 56], ones, csqB); }
          STATS_STEP(k)
          if constexpr (k >= 7 && k < 11) { constexpr int i = k - 7; rs_[i] = *(LAS f32x4*)(lds + L_MS + w * 256 + (8 * i + 4 * hh) * 4); nm_[i] = *(LAS f32x4*)(lds + L_MS + w * 256 + 128 + (8 * i + 4 * hh) * 4); }
          if constexpr (k >= 23 && k < 27) { constexpr int i = k - 23; nm_[i] *= csv; PIN(nm_[i]); }
          if constexpr (k == 30) asm volatile("" : "+a"(av));
          if constexpr (k >= 30 && k < 38) { constexpr int e = 2 * (k - 30), i = e >> 2, c = e & 3;
            const unsigned p0 = pk(fmaf(av[e], rs_[i][c], nm_[i][c]), fmaf(av[e + 1], rs_[i][c + 1], nm_[i][c + 1]));
            vp[e >> 1] = r < 28 ? p0 : (r == 28 ? 0x3f803f80u : 0u); PIN(vp[e >> 1]); }
          if constexpr (k == 38) { V[8][0] = (u32x4){vp[0], vp[1], vp[2], vp[3]}; V[8][1] = (u32x4){vp[4], vp[5], vp[6], vp[7]}; STC(V[8][0], lo + 2 * KB, dst); STC(V[8][1], lo + 3 * KB, dst); }
          if constexpr (k == 40) { cskB *= -mu * rstd; PIN(cskB); }
          if constexpr (k == 57) { asm volatile("" : "+a"(ak));
            kf = ak * rstd + cskB; UNR for (int e = 12; e < 16; ++e) kf[e] = hh ? 0.f : kf[e];
            pack16(kf, K[8][0], K[8][1]); K[8][1].z = hh ? 0x3f80u : K[8][1].z;
            STC(K[8][0], lo, dst); STC(K[8][1], lo + KB, dst);
 }
          if constexpr (k >= 58 && k < 61) { constexpr int e0 = (k - 58) * 3; UNR for (int e = e0; e < e0 + 3 && e < 8; ++e) { n0 = fmaf(kf[e], kf[e], n0); n1 = fmaf(kf[8 + e], kf[8 + e], n1); } PIN(n0); PIN(n1); }
          if constexpr (k == 61) { n0 = half_sum(n0 + n1); PIN(n0); }
          if constexpr (k == 62) { const unsigned kown = umax32(__float_as_uint(n0)); if (lane == 0) __builtin_amdgcn_raw_buffer_store_b32(kown, rs, 8, fbo + 8 * 16 * 128, 16); }
          SBAR();
          if constexpr (k == PUBK_K) { DRAIN(); if (lane == 0) __hip_atomic_store(flags + (fbo + 8 * 16 * 128) / 8, tag, RLX); SBAR(); } });
        aq = (aq - csqB * mu) * (rstd * QSCALE); UNR for (int e = 12; e < 16; ++e) aq[e] = hh ? 0.f : aq[e];
        pack16(aq, q0, q1);
        n0 = 0.f; n1 = 0.f; UNR for (int e = 0; e < 8; ++e) { n0 = fmaf(aq[e], aq[e], n0); n1 = fmaf(aq[8 + e], aq[8 + e], n1); }
        qn = __builtin_amdgcn_sqrtf(half_sum(n0 + n1)) * 1.02f;
      }
      const int kvb = KVB_OFF + (blk - 8) * KVB_BLK + head * 4 * KB;
#define SRCB(j) (blk - 8 + (j) < 0 ? ZERO_OFF : kvb + (j) * KVB_BLK)
#define LKB(j) { const int s_ = SRCB(j); K[j][0] = LDC(lo, s_); K[j][1] = LDC(lo + KB, s_); SBAR(); }
#define LVB(j) { const int s_ = SRCB(j); V[j][0] = LDC(lo + 2 * KB, s_); V[j][1] = LDC(lo + 3 * KB, s_); SBAR(); }
      { u32x4 fk; unsigned spins = 0;
        do { asm volatile("" ::: "memory"); fk = LDC(fl, fbo); } while (!__all(fk.x == (unsigned)tag && fk.y == (unsigned)(tag >> 32)) && ++spins < SPIN_CAP);
        AFTER_POLL();
        LKB(7) LKB(6) LVB(7) LKB(5) LVB(6) LKB(4)
        const unsigned kmx = __builtin_amdgcn_readfirstlane(umax16(fk.z));
        q1.z = hh ? (bf_ceil(qn * __builtin_amdgcn_sqrtf(__uint_as_float(kmx))) ^ 0x8000u) : q1.z; }
      f32x16 o; float l;
      {
        f32x16 sA, sB; u32x4 pA0 = q0, pA1 = pA0, pB0 = pA0, pB1 = pA0; f32x4 ea, eb;
        PIN(q0); PIN(q1); PIN(K[8][0]); PIN(K[8][1]); asm volatile("s_nop 1"); SBAR();
        MS0(sA, K[8][0], q0); MS1(sA, K[8][1], q1);
        NOP16V(sA); SBAR();
#define MASKT(S, keep) { UNR for (int e = 0; e < 16; ++e) { const int row = (e & 3) + 8 * (e >> 2) + 4 * hh; S[e] = (keep) ? S[e] : -INFINITY; } PIN(S); SBAR(); }
#define WZ2(k) { WZ[k] = LDW(woz, k); WZ[k + 1] = LDW(woz, k + 1); }
        MASKT(sA, row <= r)
        STEP(1, sA, sB, K[7][0], K[7][1], q0, q1, o, V[8][0], V[8][1], pB0, pB1, pA0, pA1, LVB(5) LKB(3), LVB(4), LKB(2));
        STEP(2, sB, sA, K[6][0], K[6][1], q0, q1, o, V[8][0], V[8][1], pA0, pA1, pB0, pB1, , LVB(3), LKB(1));
        STEP(0, sA, sB, K[5][0], K[5][1], q0, q1, o, V[7][0], V[7][1], pB0, pB1, pA0, pA1, , LVB(2), LKB(0));
        STEP(0, sB, sA, K[4][0], K[4][1], q0, q1, o, V[6][0], V[6][1], pA0, pA1, pB0, pB1, , LVB(1), LVB(0));
        STEP(0, sA, sB, K[3][0], K[3][1], q0, q1, o, V[5][0], V[5][1], pB0, pB1, pA0, pA1, , , );
        STEP(0, sB, sA, K[2][0], K[2][1], q0, q1, o, V[4][0], V[4][1], pA0, pA1, pB0, pB1, , WZ2(10), );
        STEP(0, sA, sB, K[1][0], K[1][1], q0, q1, o, V[3][0], V[3][1], pB0, pB1, pA0, pA1, , WZ2(12), WZ2(14));
        STEP(0, sB, sA, K[0][0], K[0][1], q0, q1, o, V[2][0], V[2][1], pA0, pA1, pB0, pB1, , WZ2(16), WZ2(18));
        MASKT(sA, row > r)
        STEP(4, sA, sB, K[0][0], K[0][1], q0, q1, o, V[1][0], V[1][1], pB0, pB1, pA0, pA1, , , );
        C2(pA1, 2, eb)
        MO(o, V[0][0], pA0); MO(o, V[0][1], pA1);
        SBAR();
        l = half_hi(o[12]);
        if (__builtin_expect(!__all(l >= L_MIN), 0)) {
          q1.z = hh ? 0u : q1.z;
          const int j0 = blk < 8 ? 8 - blk : 0;
#pragma unroll 1
          for (int pass = 0; pass < 2; ++pass) {
            float mx = -INFINITY, z_ = 0.f; PIN(z_); o = z_;
#pragma unroll 1
            for (int j = j0; j < 9; ++j) { const int s_ = kvb + j * KVB_BLK;
              const u32x4 k0 = LDC(lo, s_), k1 = LDC(lo + KB, s_), v0 = LDC(lo + 2 * KB, s_), v1 = LDC(lo + 3 * KB, s_);
              f32x16 s = {}; s = mfma32(k0, q0, s); s = mfma32(k1, q1, s);
              UNR for (int e = 0; e < 16; ++e) { const bool le = (e & 3) + 8 * (e >> 2) + 4 * hh <= r; const float v = ((j == 8 && !le) || (j == 0 && le)) ? -INFINITY : s[e];
                mx = fmaxf(mx, v); s[e] = EX(v); }
              u32x4 p0, p1; pack16(s, p0, p1); o = mfma32(v0, p0, o); o = mfma32(v1, p1, o); }
            if (pass == 0) { mx = half_max(mx); q1.z = hh ? (bf_ceil(mx) ^ 0x8000u) : q1.z; }
          }
          l = half_hi(o[12]);
        }
      }
      O_STORE(layer, o, l);
      HINT(4 + layer);
      int wkv = (g < 2 && w < 2) ? WKV_OFF + (g * 2 + dh) * 14 * KB : WS_BYTES; OPQ(wkv);
      const int G = lane >> 4, cc = lane & 15, to = L_RQ + (4 * hh + (cc >> 2)) * 224 + w * 56 + (G & 1) * 32 + (cc & 3) * 8, to2 = to + 8 * 224;
      u32x4 wf[28]; csq = (f32x16){};
#define CSQ_CHAIN(k0, k1) { if (k0 == 0) { UNR for (int k = 0; k < 6; ++k) WQF(k) SBAR(); } \
        UNR for (int k = k0; k < k1; ++k) { if (k + 6 < 28) WQF(k + 6) if (!(k & 1) && k < 24) WZ[20 + (k >> 1)] = LDW(woz, 20 + (k >> 1)); csq = mfma32(wf[k], ones, csq); SBAR(); } }
      Z_PHASE(layer, true,
              CSQ_CHAIN(0, SPLIT_K - 2), CSQ_CHAIN(SPLIT_K - 2, 28),
              if ((k & 1) && k < 28) W16[k >> 1] = LDW(wkv, k >> 1));
      cskv = (f32x4){0.f, 0.f, 0.f, 0.f};
#define CSKV_CHAIN() { if (g == 0) { UNR for (int k = 0; k < 14; ++k) cskv = mfma16(W16[k], ones, cskv); } else { UNR for (int k = 0; k < 14; ++k) cskv = mfma16(ones, W16[k], cskv); } }
      CSKV_CHAIN(); DRAIN();
      X_GATHER(layer + 1, );
    }
  }
}

extern "C" void kernel_launch(void* const* d_in, const int* in_sizes, int n_in, void* d_out, int out_size, void* d_ws, size_t ws_size, hipStream_t stream) {
  (void)in_sizes; (void)n_in; (void)out_size; (void)ws_size;
  (void)hipFuncSetAttribute((const void*)main_kernel, hipFuncAttributeMaxDynamicSharedMemorySize, L_BYTES);
  main_kernel<<<256, 256, L_BYTES, stream>>>((const u16*)d_in[0], (const u16*)d_in[1], (const u16*)d_in[2], (const u16*)d_in[3], (const u16*)d_in[4],
                                             (const u16*)d_in[6], (const u16*)d_in[7], (unsigned char*)d_ws, (u16*)d_out);
}
```
